```python
import torch

def _softmax(x, axis=-1):
    return torch.softmax(x, dim=axis)
def _gelu(x):
    return torch.nn.functional.gelu(x, approximate="tanh")
def _silu(x):
    return torch.nn.functional.silu(x)
def _sigmoid(x):
    return torch.sigmoid(x)
def _layernorm(x, axis=-1, eps=1e-5):
    m = x.mean(dim=axis, keepdim=True)
    v = x.var(dim=axis, keepdim=True, unbiased=False)
    return (x - m) / torch.sqrt(v + eps)
def _scatter_add(tgt, idx, src, axis=0):
    return tgt.clone().index_add_(axis, idx, src)
def _segment_sum(src, seg, n):
    out = torch.zeros((n,) + src.shape[1:], dtype=src.dtype)
    return out.index_add_(0, seg, src)
def _pad(x, pw):
    flat = tuple(v for pair in reversed(pw) for v in pair)
    return torch.nn.functional.pad(x, flat)

kernel_name = 'parallel_sum__ssm_conv_s4__dense_activation_relu__prompt'

def setup_inputs():

    _w = (lambda a: (lambda t: t.to(torch.bfloat16) if t.is_floating_point() else t)(torch.as_tensor(a)))
    def _A_setup():
        B, L, D, N = 1, 2048, 448, 256
        A_re = -(lambda _x: torch.exp(torch.as_tensor(_x)))((lambda *s: torch.randn(*s, dtype=torch.bfloat16) * 6.4)(D, N))
        A_im = torch.pi * (lambda *s: torch.randn(*s, dtype=torch.bfloat16) * 0.1)(D, N)
        B_re = (lambda *s: torch.randn(*s, dtype=torch.bfloat16) * 0.2)(D, N)
        B_im = (lambda *s: torch.randn(*s, dtype=torch.bfloat16) * 0.2)(D, N)
        C_re = (lambda *s: torch.randn(*s, dtype=torch.bfloat16) * 0.2)(D, N)
        C_im = (lambda *s: torch.randn(*s, dtype=torch.bfloat16) * 0.2)(D, N)
        log_dt = (lambda *s: torch.randn(*s, dtype=torch.bfloat16) * 0.1)(D) * 0.5 - 4.0
        Dskip = (lambda *s: torch.randn(*s, dtype=torch.bfloat16) * 0.1)(D)
        return {
            "u": _w((lambda *s: torch.randn(*s, dtype=torch.bfloat16) * 0.05)(B, L, D)),
            "A_re": _w(A_re),
            "A_im": _w(A_im),
            "B_re": _w(B_re),
            "B_im": _w(B_im),
            "C_re": _w(C_re),
            "C_im": _w(C_im),
            "log_dt": _w(log_dt),
            "Dskip": _w(Dskip),
        }
    _A_in = _A_setup()

    def _B_setup():
        B, S, D, Dff = 1, 2048, 448, 1792
        return {
            "x": _w((lambda *s: torch.randn(*s, dtype=torch.bfloat16) * 0.05)(B, S, D)),
            "w1": _w((lambda *s: torch.randn(*s, dtype=torch.bfloat16) * 0.05)(D, Dff)),
            "w2": _w((lambda *s: torch.randn(*s, dtype=torch.bfloat16) * 0.05)(Dff, D)),
        }
    _B_in = _B_setup()

    return {
        "A_u": _A_in["u"],
        "A_A_re": _A_in["A_re"],
        "A_A_im": _A_in["A_im"],
        "A_B_re": _A_in["B_re"],
        "A_B_im": _A_in["B_im"],
        "A_C_re": _A_in["C_re"],
        "A_C_im": _A_in["C_im"],
        "A_log_dt": _A_in["log_dt"],
        "A_Dskip": _A_in["Dskip"],
        "B_x": _B_in["x"],
        "B_w1": _B_in["w1"],
        "B_w2": _B_in["w2"],
    }

def reference(A_u, A_A_re, A_A_im, A_B_re, A_B_im, A_C_re, A_C_im, A_log_dt, A_Dskip, B_x, B_w1, B_w2):
    def _A_ref(u, A_re, A_im, B_re, B_im, C_re, C_im, log_dt, Dskip):
        Bsz, L, D = u.shape
        dt = torch.exp(log_dt)[:, None]
        A = A_re + 1j * A_im
        Bc = B_re + 1j * B_im
        Cc = C_re + 1j * C_im
        logdA = dt * A
        dB = dt * Bc
        l = (lambda x, dt: x.to(dt))(torch.arange(L), A_re.dtype)
        P = torch.exp(logdA[:, :, None] * l[None, None, :])
        K = torch.einsum("dn,dnl,dn->dl", Cc, P, dB).real
        L2 = 2 * L
        Kp = _pad(K, ((0, 0), (0, L)))
        up = (lambda x, *d: x.permute(*d))(u, 0, 2, 1)
        up = _pad(up, ((0, 0), (0, 0), (0, L)))
        Kf = (lambda x, axes=(-2,-1): torch.fft.rfft2(x.float(), dim=axes))(Kp[:, None, :], axes=(-2, -1))
        Uf = (lambda x, axes=(-2,-1): torch.fft.rfft2(x.float(), dim=axes))(up[:, :, None, :], axes=(-2, -1))
        Yf = Uf * Kf[None, :, :, :]
        y = (lambda x, s, axes=(-2,-1): torch.fft.irfft2(x, s=s, dim=axes).to(torch.bfloat16))(Yf, (1, L2), axes=(-2, -1))[:, :, 0, :L]
        y = (lambda x, *d: x.permute(*d))(y, 0, 2, 1) + u * Dskip[None, None, :]
        return y

    def _B_ref(x, w1, w2):
        h = torch.einsum("bsd,df->bsf", x, w1)
        zero = torch.zeros_like(h)
        h = torch.maximum(h, zero)
        return torch.einsum("bsf,fd->bsd", h, w2)

    x = A_u
    xn = _layernorm(x, axis=-1)
    return x + _A_ref(xn, A_A_re, A_A_im, A_B_re, A_B_im, A_C_re, A_C_im, A_log_dt, A_Dskip) + _B_ref(xn, B_w1, B_w2)
```

```cpp
#include <hip/hip_runtime.h>
#include <hip/hip_bf16.h>
#include <stdint.h>

typedef unsigned short u16;
#define L_ 2048
#define D_ 448
#define N_ 256
#define F_ 1792

__device__ __forceinline__ float bf2f(u16 v) { return __uint_as_float(((unsigned)v) << 16); }
__device__ __forceinline__ u16 f2bf(float f) { __hip_bfloat16 b = __float2bfloat16(f); return *reinterpret_cast<u16*>(&b); }
__device__ __forceinline__ float rbf(float f) { return bf2f(f2bf(f)); }

__global__ void k_ln(const u16* x, u16* xn) {
    int row = blockIdx.x; int t = threadIdx.x;
    float v[7]; float s = 0;
    for (int i = 0; i < 7; i++) { v[i] = bf2f(x[row * D_ + t + 64 * i]); s += v[i]; }
    for (int m = 32; m >= 1; m >>= 1) s += __shfl_xor(s, m);
    float mean = s / D_; float q = 0;
    for (int i = 0; i < 7; i++) { float d = v[i] - mean; q += d * d; }
    for (int m = 32; m >= 1; m >>= 1) q += __shfl_xor(q, m);
    float var = rbf(q / D_); float mb = rbf(mean);
    float sd = rbf(sqrtf(rbf(var + 1e-5f)));
    for (int i = 0; i < 7; i++) xn[row * D_ + t + 64 * i] = f2bf(rbf(v[i] - mb) / sd);
}

__global__ void k_gen(const u16* Are, const u16* Aim, const u16* Bre, const u16* Bim, const u16* Cre, const u16* Cim,
                      const u16* logdt, float* K) {
    int d = blockIdx.x; int t = threadIdx.x;
    __shared__ float sa[N_], sb[N_], cr[N_], ci[N_];
    float dt = rbf(expf(bf2f(logdt[d])));
    {
        int n = t;
        float ar = bf2f(Are[d * N_ + n]) * dt, ai = bf2f(Aim[d * N_ + n]) * dt;
        float br = bf2f(Bre[d * N_ + n]) * dt, bi = bf2f(Bim[d * N_ + n]) * dt;
        float c0 = bf2f(Cre[d * N_ + n]), c1 = bf2f(Cim[d * N_ + n]);
        sa[n] = ar; sb[n] = ai; cr[n] = c0 * br - c1 * bi; ci[n] = c0 * bi + c1 * br;
    }
    __syncthreads();
    for (int l = t; l < L_; l += 256) {
        float lf = rbf((float)l);
        float acc = 0;
        for (int n = 0; n < N_; n++) {
            float e = expf(sa[n] * lf); float s, c; sincosf(sb[n] * lf, &s, &c);
            acc += e * (cr[n] * c - ci[n] * s);
        }
        K[d * L_ + l] = acc;
    }
}

__global__ void k_conv(const float* K, const u16* xn, u16* y) {
    int d = blockIdx.x; int t = threadIdx.x;
    __shared__ float sk[L_], su[L_];
    for (int l = t; l < L_; l += 256) { sk[l] = K[d * L_ + l]; su[l] = bf2f(xn[l * D_ + d]); }
    __syncthreads();
    for (int o = t; o < L_; o += 256) {
        float acc = 0;
        for (int l = 0; l <= o; l++) acc += sk[l] * su[o - l];
        y[o * D_ + d] = f2bf(acc);
    }
}

__global__ void k_g1(const u16* xn, const u16* w1, u16* h) {
    int row = blockIdx.x; int t = threadIdx.x;
    __shared__ float sx[D_];
    for (int i = t; i < D_; i += 256) sx[i] = bf2f(xn[row * D_ + i]);
    __syncthreads();
    for (int f = t; f < F_; f += 256) {
        float acc = 0;
        for (int k = 0; k < D_; k++) acc += sx[k] * bf2f(w1[k * F_ + f]);
        h[row * F_ + f] = f2bf(fmaxf(rbf(acc), 0.f));
    }
}

__global__ void k_g2(const u16* h, const u16* w2, const u16* x, const u16* xn, const u16* y, const u16* Dk, u16* out) {
    int row = blockIdx.x; int t = threadIdx.x;
    __shared__ float sh[F_];
    for (int i = t; i < F_; i += 448) sh[i] = bf2f(h[row * F_ + i]);
    __syncthreads();
    float acc = 0;
    for (int k = 0; k < F_; k++) acc += sh[k] * bf2f(w2[k * D_ + t]);
    float a = rbf(bf2f(y[row * D_ + t]) + rbf(bf2f(xn[row * D_ + t]) * bf2f(Dk[t])));
    float r = rbf(bf2f(x[row * D_ + t]) + a);
    out[row * D_ + t] = f2bf(r + rbf(acc));
}

extern "C" void kernel_launch(void* const* d_in, const int* in_sizes, int n_in,
                              void* d_out, int out_size, void* d_ws, size_t ws_size,
                              hipStream_t stream) {
    const u16* x = (const u16*)d_in[0];
    char* ws = (char*)d_ws;
    u16* xn = (u16*)ws; ws += L_ * D_ * 2;
    u16* y = (u16*)ws; ws += L_ * D_ * 2;
    u16* h = (u16*)ws; ws += L_ * F_ * 2;
    float* K = (float*)ws;
    k_ln<<<L_, 64, 0, stream>>>(x, xn);
    k_gen<<<D_, 256, 0, stream>>>((const u16*)d_in[1], (const u16*)d_in[2], (const u16*)d_in[3], (const u16*)d_in[4], (const u16*)d_in[5], (const u16*)d_in[6], (const u16*)d_in[7], K);
    k_conv<<<D_, 256, 0, stream>>>(K, xn, y);
    k_g1<<<L_, 256, 0, stream>>>(xn, (const u16*)d_in[10], h);
    k_g2<<<L_, 448, 0, stream>>>(h, (const u16*)d_in[11], x, xn, y, (const u16*)d_in[8], (u16*)d_out);
}
```

```cpp
#include <hip/hip_runtime.h>
#include <hip/hip_bf16.h>
#include <stdint.h>

typedef unsigned short u16;
#define L_ 2048
#define D_ 448
#define N_ 256
#define F_ 1792

__device__ __forceinline__ float bf2f(u16 v) { return __uint_as_float(((unsigned)v) << 16); }
__device__ __forceinline__ u16 f2bf(float f) { __hip_bfloat16 b = __float2bfloat16(f); return *reinterpret_cast<u16*>(&b); }
__device__ __forceinline__ float rbf(float f) { return bf2f(f2bf(f)); }

__global__ void k_ln(const u16* x, u16* xn) {
    int row = blockIdx.x; int t = threadIdx.x;
    float v[7]; float s = 0;
    for (int i = 0; i < 7; i++) { v[i] = bf2f(x[row * D_ + t + 64 * i]); s += v[i]; }
    for (int m = 32; m >= 1; m >>= 1) s += __shfl_xor(s, m);
    float mean = s / D_; float q = 0;
    for (int i = 0; i < 7; i++) { float d = v[i] - mean; q += d * d; }
    for (int m = 32; m >= 1; m >>= 1) q += __shfl_xor(q, m);
    float var = rbf(q / D_); float mb = rbf(mean);
    float sd = rbf(sqrtf(rbf(var + 1e-5f)));
    for (int i = 0; i < 7; i++) xn[row * D_ + t + 64 * i] = f2bf(rbf(v[i] - mb) / sd);
}

__global__ void k_gen(const u16* Are, const u16* Aim, const u16* Bre, const u16* Bim, const u16* Cre, const u16* Cim,
                      const u16* logdt, float* K) {
    int d = blockIdx.x; int t = threadIdx.x;
    __shared__ float sa[N_], sb[N_], cr[N_], ci[N_];
    float dt = rbf(expf(bf2f(logdt[d])));
    {
        int n = t;
        float ar = bf2f(Are[d * N_ + n]) * dt, ai = bf2f(Aim[d * N_ + n]) * dt;
        float br = bf2f(Bre[d * N_ + n]) * dt, bi = bf2f(Bim[d * N_ + n]) * dt;
        float c0 = bf2f(Cre[d * N_ + n]), c1 = bf2f(Cim[d * N_ + n]);
        sa[n] = ar; sb[n] = ai; cr[n] = c0 * br - c1 * bi; ci[n] = c0 * bi + c1 * br;
    }
    __syncthreads();
    for (int l = t; l < L_; l += 256) {
        float lf = rbf((float)l);
        float acc = 0;
        for (int n = 0; n < N_; n++) {
            float e = expf(sa[n] * lf); float s, c; sincosf(sb[n] * lf, &s, &c);
            acc += e * (cr[n] * c - ci[n] * s);
        }
        K[d * L_ + l] = acc;
    }
}

__global__ void k_conv(const float* K, const u16* xn, u16* y) {
    int d = blockIdx.x; int t = threadIdx.x;
    __shared__ float sk[L_], su[L_];
    for (int l = t; l < L_; l += 256) { sk[l] = K[d * L_ + l]; su[l] = bf2f(xn[l * D_ + d]); }
    __syncthreads();
    for (int o = t; o < L_; o += 256) {
        float acc = 0;
        for (int l = 0; l <= o; l++) acc += sk[l] * su[o - l];
        y[o * D_ + d] = f2bf(acc);
    }
}

__global__ void k_g1(const u16* xn, const u16* w1, u16* h) {
    int row = blockIdx.x; int t = threadIdx.x;
    __shared__ float sx[D_];
    for (int i = t; i < D_; i += 256) sx[i] = bf2f(xn[row * D_ + i]);
    __syncthreads();
    for (int f = t; f < F_; f += 256) {
        float acc = 0;
        for (int k = 0; k < D_; k++) acc += sx[k] * bf2f(w1[k * F_ + f]);
        h[row * F_ + f] = f2bf(fmaxf(rbf(acc), 0.f));
    }
}

__global__ void k_g2(const u16* h, const u16* w2, const u16* x, const u16* xn, const u16* y, const u16* Dk, u16* out) {
    int row = blockIdx.x; int t = threadIdx.x;
    __shared__ float sh[F_];
    for (int i = t; i < F_; i += 448) sh[i] = bf2f(h[row * F_ + i]);
    __syncthreads();
    float acc = 0;
    for (int k = 0; k < F_; k++) acc += sh[k] * bf2f(w2[k * D_ + t]);
    float a = rbf(bf2f(y[row * D_ + t]) + rbf(bf2f(xn[row * D_ + t]) * bf2f(Dk[t])));
    float r = rbf(bf2f(x[row * D_ + t]) + a);
    out[row * D_ + t] = f2bf(r + rbf(acc));
}


#define GAS __attribute__((address_space(1)))
#define LAS __attribute__((address_space(3)))
typedef unsigned v4u __attribute__((ext_vector_type(4)));
__device__ __forceinline__ unsigned long long rt() { return __builtin_amdgcn_s_memrealtime(); }
__global__ __launch_bounds__(512, 2) void k_probe(const u16* x, const u16* w1, const u16* w2, unsigned* sink) {
    extern __shared__ __attribute__((aligned(16))) char smem[];
    unsigned long long t0 = rt();
    int b = blockIdx.x, xcd = b & 7, idx = b >> 3;
    int tid = threadIdx.x, lane = tid & 63; int wave = __builtin_amdgcn_readfirstlane(tid >> 6);
    if (idx >= 28) return;
    unsigned acc = 0;

    if (lane < 56) { v4u v = *(const v4u*)(x + (size_t)(b * 8 + wave) * 448 + lane * 8); acc ^= v.x ^ v.y ^ v.z ^ v.w; }

    asm volatile("s_waitcnt vmcnt(0)" ::: "memory");
    __syncthreads();
    unsigned long long t2 = rt();
    if (b == 9 && wave == 0) { unsigned long long tend = t2 + 100 * (t2 - t0); while (rt() < tend) __builtin_amdgcn_s_sleep(8); }
    acc ^= *(unsigned*)(smem + tid * 4);
    if (acc == 0x12345678) sink[tid] = acc;
}

extern "C" void kernel_launch(void* const* d_in, const int* in_sizes, int n_in,
                              void* d_out, int out_size, void* d_ws, size_t ws_size,
                              hipStream_t stream) {
    const u16* x = (const u16*)d_in[0];
    char* ws = (char*)d_ws;
    u16* xn = (u16*)ws; ws += L_ * D_ * 2;
    u16* y = (u16*)ws; ws += L_ * D_ * 2;
    u16* h = (u16*)ws; ws += L_ * F_ * 2;
    float* K = (float*)ws;
    hipFuncSetAttribute((const void*)k_probe, hipFuncAttributeMaxDynamicSharedMemorySize, 160 * 1024);
    k_probe<<<256, 512, 150 * 1024, stream>>>(x, (const u16*)d_in[10], (const u16*)d_in[11], (unsigned*)((char*)d_ws + (64 << 20)));
    k_ln<<<L_, 64, 0, stream>>>(x, xn);
    k_gen<<<D_, 256, 0, stream>>>((const u16*)d_in[1], (const u16*)d_in[2], (const u16*)d_in[3], (const u16*)d_in[4], (const u16*)d_in[5], (const u16*)d_in[6], (const u16*)d_in[7], K);
    k_conv<<<D_, 256, 0, stream>>>(K, xn, y);
    k_g1<<<L_, 256, 0, stream>>>(xn, (const u16*)d_in[10], h);
    k_g2<<<L_, 448, 0, stream>>>(h, (const u16*)d_in[11], x, xn, y, (const u16*)d_in[8], (u16*)d_out);
}
```

```cpp
#include <hip/hip_runtime.h>
#include <hip/hip_bf16.h>
#include <stdint.h>

typedef unsigned short u16;
#define L_ 2048
#define D_ 448
#define N_ 256
#define F_ 1792

__device__ __forceinline__ float bf2f(u16 v) { return __uint_as_float(((unsigned)v) << 16); }
__device__ __forceinline__ u16 f2bf(float f) { __hip_bfloat16 b = __float2bfloat16(f); return *reinterpret_cast<u16*>(&b); }
__device__ __forceinline__ float rbf(float f) { return bf2f(f2bf(f)); }

__global__ void k_ln(const u16* x, u16* xn) {
    int row = blockIdx.x; int t = threadIdx.x;
    float v[7]; float s = 0;
    for (int i = 0; i < 7; i++) { v[i] = bf2f(x[row * D_ + t + 64 * i]); s += v[i]; }
    for (int m = 32; m >= 1; m >>= 1) s += __shfl_xor(s, m);
    float mean = s / D_; float q = 0;
    for (int i = 0; i < 7; i++) { float d = v[i] - mean; q += d * d; }
    for (int m = 32; m >= 1; m >>= 1) q += __shfl_xor(q, m);
    float var = rbf(q / D_); float mb = rbf(mean);
    float sd = rbf(sqrtf(rbf(var + 1e-5f)));
    for (int i = 0; i < 7; i++) xn[row * D_ + t + 64 * i] = f2bf(rbf(v[i] - mb) / sd);
}

__global__ void k_gen(const u16* Are, const u16* Aim, const u16* Bre, const u16* Bim, const u16* Cre, const u16* Cim,
                      const u16* logdt, float* K) {
    int d = blockIdx.x; int t = threadIdx.x;
    __shared__ float sa[N_], sb[N_], cr[N_], ci[N_];
    float dt = rbf(expf(bf2f(logdt[d])));
    {
        int n = t;
        float ar = bf2f(Are[d * N_ + n]) * dt, ai = bf2f(Aim[d * N_ + n]) * dt;
        float br = bf2f(Bre[d * N_ + n]) * dt, bi = bf2f(Bim[d * N_ + n]) * dt;
        float c0 = bf2f(Cre[d * N_ + n]), c1 = bf2f(Cim[d * N_ + n]);
        sa[n] = ar; sb[n] = ai; cr[n] = c0 * br - c1 * bi; ci[n] = c0 * bi + c1 * br;
    }
    __syncthreads();
    for (int l = t; l < L_; l += 256) {
        float lf = rbf((float)l);
        float acc = 0;
        for (int n = 0; n < N_; n++) {
            float e = expf(sa[n] * lf); float s, c; sincosf(sb[n] * lf, &s, &c);
            acc += e * (cr[n] * c - ci[n] * s);
        }
        K[d * L_ + l] = acc;
    }
}

__global__ void k_conv(const float* K, const u16* xn, u16* y) {
    int d = blockIdx.x; int t = threadIdx.x;
    __shared__ float sk[L_], su[L_];
    for (int l = t; l < L_; l += 256) { sk[l] = K[d * L_ + l]; su[l] = bf2f(xn[l * D_ + d]); }
    __syncthreads();
    for (int o = t; o < L_; o += 256) {
        float acc = 0;
        for (int l = 0; l <= o; l++) acc += sk[l] * su[o - l];
        y[o * D_ + d] = f2bf(acc);
    }
}

__global__ void k_g1(const u16* xn, const u16* w1, u16* h) {
    int row = blockIdx.x; int t = threadIdx.x;
    __shared__ float sx[D_];
    for (int i = t; i < D_; i += 256) sx[i] = bf2f(xn[row * D_ + i]);
    __syncthreads();
    for (int f = t; f < F_; f += 256) {
        float acc = 0;
        for (int k = 0; k < D_; k++) acc += sx[k] * bf2f(w1[k * F_ + f]);
        h[row * F_ + f] = f2bf(fmaxf(rbf(acc), 0.f));
    }
}

__global__ void k_g2(const u16* h, const u16* w2, const u16* x, const u16* xn, const u16* y, const u16* Dk, u16* out) {
    int row = blockIdx.x; int t = threadIdx.x;
    __shared__ float sh[F_];
    for (int i = t; i < F_; i += 448) sh[i] = bf2f(h[row * F_ + i]);
    __syncthreads();
    float acc = 0;
    for (int k = 0; k < F_; k++) acc += sh[k] * bf2f(w2[k * D_ + t]);
    float a = rbf(bf2f(y[row * D_ + t]) + rbf(bf2f(xn[row * D_ + t]) * bf2f(Dk[t])));
    float r = rbf(bf2f(x[row * D_ + t]) + a);
    out[row * D_ + t] = f2bf(r + rbf(acc));
}


#define GAS __attribute__((address_space(1)))
#define LAS __attribute__((address_space(3)))
typedef unsigned v4u __attribute__((ext_vector_type(4)));
__device__ __forceinline__ unsigned long long rt() { return __builtin_amdgcn_s_memrealtime(); }
__global__ __launch_bounds__(512, 2) void k_probe(const u16* x, const u16* w1, const u16* w2, unsigned* sink) {
    extern __shared__ __attribute__((aligned(16))) char smem[];
    unsigned long long t0 = rt();
    int b = blockIdx.x, xcd = b & 7, idx = b >> 3;
    int tid = threadIdx.x, lane = tid & 63; int wave = __builtin_amdgcn_readfirstlane(tid >> 6);
    if (idx >= 28) return;
    unsigned acc = 0;
    unsigned dd1 = 0, dd2 = 0, dd3 = 0;
    { const char* p_ = (const char*)x + (size_t)((xcd >> 1) * 3584 + idx * 128 + tid) * 128; if (tid < 128) asm volatile("global_load_dword %0, %1, off" : "+v"(dd1) : "v"(p_) : "memory"); }
    { const char* p_ = (const char*)w1 + (size_t)((idx * 224 + tid - 128) / 14) * 3584 + ((xcd & 1) * 14 + (idx * 224 + tid - 128) % 14) * 128; if (tid >= 128 && tid < 352) asm volatile("global_load_dword %0, %1, off" : "+v"(dd2) : "v"(p_) : "memory"); }

    asm volatile("s_waitcnt vmcnt(0)" ::: "memory");
    asm volatile("" :: "v"(dd1), "v"(dd2), "v"(dd3));
    __syncthreads();
    unsigned long long t2 = rt();
    if (b == 9 && wave == 0) { unsigned long long tend = t2 + 100 * (t2 - t0); while (rt() < tend) __builtin_amdgcn_s_sleep(8); }
    acc ^= *(unsigned*)(smem + tid * 4);
    if (acc == 0x12345678) sink[tid] = acc;
}

extern "C" void kernel_launch(void* const* d_in, const int* in_sizes, int n_in,
                              void* d_out, int out_size, void* d_ws, size_t ws_size,
                              hipStream_t stream) {
    const u16* x = (const u16*)d_in[0];
    char* ws = (char*)d_ws;
    u16* xn = (u16*)ws; ws += L_ * D_ * 2;
    u16* y = (u16*)ws; ws += L_ * D_ * 2;
    u16* h = (u16*)ws; ws += L_ * F_ * 2;
    float* K = (float*)ws;
    hipFuncSetAttribute((const void*)k_probe, hipFuncAttributeMaxDynamicSharedMemorySize, 160 * 1024);
    k_probe<<<256, 512, 150 * 1024, stream>>>(x, (const u16*)d_in[10], (const u16*)d_in[11], (unsigned*)((char*)d_ws + (64 << 20)));
    k_ln<<<L_, 64, 0, stream>>>(x, xn);
    k_gen<<<D_, 256, 0, stream>>>((const u16*)d_in[1], (const u16*)d_in[2], (const u16*)d_in[3], (const u16*)d_in[4], (const u16*)d_in[5], (const u16*)d_in[6], (const u16*)d_in[7], K);
    k_conv<<<D_, 256, 0, stream>>>(K, xn, y);
    k_g1<<<L_, 256, 0, stream>>>(xn, (const u16*)d_in[10], h);
    k_g2<<<L_, 448, 0, stream>>>(h, (const u16*)d_in[11], x, xn, y, (const u16*)d_in[8], (u16*)d_out);
}
```

```cpp
#include <hip/hip_runtime.h>
#include <hip/hip_bf16.h>
#include <stdint.h>

typedef unsigned short u16;
#define L_ 2048
#define D_ 448
#define N_ 256
#define F_ 1792

__device__ __forceinline__ float bf2f(u16 v) { return __uint_as_float(((unsigned)v) << 16); }
__device__ __forceinline__ u16 f2bf(float f) { __hip_bfloat16 b = __float2bfloat16(f); return *reinterpret_cast<u16*>(&b); }
__device__ __forceinline__ float rbf(float f) { return bf2f(f2bf(f)); }

__global__ void k_ln(const u16* x, u16* xn) {
    int row = blockIdx.x; int t = threadIdx.x;
    float v[7]; float s = 0;
    for (int i = 0; i < 7; i++) { v[i] = bf2f(x[row * D_ + t + 64 * i]); s += v[i]; }
    for (int m = 32; m >= 1; m >>= 1) s += __shfl_xor(s, m);
    float mean = s / D_; float q = 0;
    for (int i = 0; i < 7; i++) { float d = v[i] - mean; q += d * d; }
    for (int m = 32; m >= 1; m >>= 1) q += __shfl_xor(q, m);
    float var = rbf(q / D_); float mb = rbf(mean);
    float sd = rbf(sqrtf(rbf(var + 1e-5f)));
    for (int i = 0; i < 7; i++) xn[row * D_ + t + 64 * i] = f2bf(rbf(v[i] - mb) / sd);
}

__global__ void k_gen(const u16* Are, const u16* Aim, const u16* Bre, const u16* Bim, const u16* Cre, const u16* Cim,
                      const u16* logdt, float* K) {
    int d = blockIdx.x; int t = threadIdx.x;
    __shared__ float sa[N_], sb[N_], cr[N_], ci[N_];
    float dt = rbf(expf(bf2f(logdt[d])));
    {
        int n = t;
        float ar = bf2f(Are[d * N_ + n]) * dt, ai = bf2f(Aim[d * N_ + n]) * dt;
        float br = bf2f(Bre[d * N_ + n]) * dt, bi = bf2f(Bim[d * N_ + n]) * dt;
        float c0 = bf2f(Cre[d * N_ + n]), c1 = bf2f(Cim[d * N_ + n]);
        sa[n] = ar; sb[n] = ai; cr[n] = c0 * br - c1 * bi; ci[n] = c0 * bi + c1 * br;
    }
    __syncthreads();
    for (int l = t; l < L_; l += 256) {
        float lf = rbf((float)l);
        float acc = 0;
        for (int n = 0; n < N_; n++) {
            float e = expf(sa[n] * lf); float s, c; sincosf(sb[n] * lf, &s, &c);
            acc += e * (cr[n] * c - ci[n] * s);
        }
        K[d * L_ + l] = acc;
    }
}

__global__ void k_conv(const float* K, const u16* xn, u16* y) {
    int d = blockIdx.x; int t = threadIdx.x;
    __shared__ float sk[L_], su[L_];
    for (int l = t; l < L_; l += 256) { sk[l] = K[d * L_ + l]; su[l] = bf2f(xn[l * D_ + d]); }
    __syncthreads();
    for (int o = t; o < L_; o += 256) {
        float acc = 0;
        for (int l = 0; l <= o; l++) acc += sk[l] * su[o - l];
        y[o * D_ + d] = f2bf(acc);
    }
}

__global__ void k_g1(const u16* xn, const u16* w1, u16* h) {
    int row = blockIdx.x; int t = threadIdx.x;
    __shared__ float sx[D_];
    for (int i = t; i < D_; i += 256) sx[i] = bf2f(xn[row * D_ + i]);
    __syncthreads();
    for (int f = t; f < F_; f += 256) {
        float acc = 0;
        for (int k = 0; k < D_; k++) acc += sx[k] * bf2f(w1[k * F_ + f]);
        h[row * F_ + f] = f2bf(fmaxf(rbf(acc), 0.f));
    }
}

__global__ void k_g2(const u16* h, const u16* w2, const u16* x, const u16* xn, const u16* y, const u16* Dk, u16* out) {
    int row = blockIdx.x; int t = threadIdx.x;
    __shared__ float sh[F_];
    for (int i = t; i < F_; i += 448) sh[i] = bf2f(h[row * F_ + i]);
    __syncthreads();
    float acc = 0;
    for (int k = 0; k < F_; k++) acc += sh[k] * bf2f(w2[k * D_ + t]);
    float a = rbf(bf2f(y[row * D_ + t]) + rbf(bf2f(xn[row * D_ + t]) * bf2f(Dk[t])));
    float r = rbf(bf2f(x[row * D_ + t]) + a);
    out[row * D_ + t] = f2bf(r + rbf(acc));
}


#define GAS __attribute__((address_space(1)))
#define LAS __attribute__((address_space(3)))
typedef unsigned v4u __attribute__((ext_vector_type(4)));
__device__ __forceinline__ unsigned long long rt() { return __builtin_amdgcn_s_memrealtime(); }
__global__ __launch_bounds__(512, 2) void k_probe(const u16* x, const u16* w1, const u16* w2, unsigned* sink) {
    extern __shared__ __attribute__((aligned(16))) char smem[];
    unsigned long long t0 = rt();
    int b = blockIdx.x, xcd = b & 7, idx = b >> 3;
    int tid = threadIdx.x, lane = tid & 63; int wave = __builtin_amdgcn_readfirstlane(tid >> 6);
    if (idx >= 28) return;
    unsigned acc = 0;
    unsigned dd1 = 0, dd2 = 0, dd3 = 0;
    int rp = 4 * (xcd >> 1) + idx / 7, cc = (xcd & 1) * 7 + idx % 7;

    { int k0 = (idx % 7 + 2 * (idx / 7)) % 7;
      const char* xp = (const char*)(x + (size_t)(rp * 128 + wave * 16 + (lane >> 3)) * 448) + (lane & 7) * 16;
      const char* wp = (const char*)(w1 + (size_t)(wave * 8 + (lane >> 4)) * 1792 + cc * 128) + (lane & 15) * 16;
#pragma unroll
      for (int s = 0; s < 7; s++) { int kt = k0 + s; if (kt >= 7) kt -= 7;
        __builtin_amdgcn_global_load_lds((const GAS unsigned*)(xp + kt * 128), (LAS unsigned*)(smem + (s & 3) * 32768 + wave * 2048), 16, 0, 0);
        __builtin_amdgcn_global_load_lds((const GAS unsigned*)(xp + kt * 128 + 8 * 896), (LAS unsigned*)(smem + (s & 3) * 32768 + wave * 2048 + 1024), 16, 0, 0);
        __builtin_amdgcn_global_load_lds((const GAS unsigned*)(wp + (size_t)kt * 64 * 3584), (LAS unsigned*)(smem + (s & 3) * 32768 + 16384 + wave * 2048), 16, 0, 0);
        __builtin_amdgcn_global_load_lds((const GAS unsigned*)(wp + (size_t)kt * 64 * 3584 + 4 * 3584), (LAS unsigned*)(smem + (s & 3) * 32768 + 16384 + wave * 2048 + 1024), 16, 0, 0);
        __builtin_amdgcn_s_barrier();
      } }

    asm volatile("s_waitcnt vmcnt(0)" ::: "memory");
    asm volatile("" :: "v"(dd1), "v"(dd2), "v"(dd3));
    __syncthreads();
    unsigned long long t2 = rt();
    if (b == 9 && wave == 0) { unsigned long long tend = t2 + 100 * (t2 - t0); while (rt() < tend) __builtin_amdgcn_s_sleep(8); }
    acc ^= *(unsigned*)(smem + tid * 4);
    if (acc == 0x12345678) sink[tid] = acc;
}

extern "C" void kernel_launch(void* const* d_in, const int* in_sizes, int n_in,
                              void* d_out, int out_size, void* d_ws, size_t ws_size,
                              hipStream_t stream) {
    const u16* x = (const u16*)d_in[0];
    char* ws = (char*)d_ws;
    u16* xn = (u16*)ws; ws += L_ * D_ * 2;
    u16* y = (u16*)ws; ws += L_ * D_ * 2;
    u16* h = (u16*)ws; ws += L_ * F_ * 2;
    float* K = (float*)ws;
    hipFuncSetAttribute((const void*)k_probe, hipFuncAttributeMaxDynamicSharedMemorySize, 160 * 1024);
    k_probe<<<256, 512, 150 * 1024, stream>>>(x, (const u16*)d_in[10], (const u16*)d_in[11], (unsigned*)((char*)d_ws + (64 << 20)));
    k_ln<<<L_, 64, 0, stream>>>(x, xn);
    k_gen<<<D_, 256, 0, stream>>>((const u16*)d_in[1], (const u16*)d_in[2], (const u16*)d_in[3], (const u16*)d_in[4], (const u16*)d_in[5], (const u16*)d_in[6], (const u16*)d_in[7], K);
    k_conv<<<D_, 256, 0, stream>>>(K, xn, y);
    k_g1<<<L_, 256, 0, stream>>>(xn, (const u16*)d_in[10], h);
    k_g2<<<L_, 448, 0, stream>>>(h, (const u16*)d_in[11], x, xn, y, (const u16*)d_in[8], (u16*)d_out);
}
```

```cpp
#include <hip/hip_runtime.h>
#include <hip/hip_bf16.h>
#include <stdint.h>

typedef unsigned short u16;
#define L_ 2048
#define D_ 448
#define N_ 256
#define F_ 1792

__device__ __forceinline__ float bf2f(u16 v) { return __uint_as_float(((unsigned)v) << 16); }
__device__ __forceinline__ u16 f2bf(float f) { __hip_bfloat16 b = __float2bfloat16(f); return *reinterpret_cast<u16*>(&b); }
__device__ __forceinline__ float rbf(float f) { return bf2f(f2bf(f)); }

__global__ void k_ln(const u16* x, u16* xn) {
    int row = blockIdx.x; int t = threadIdx.x;
    float v[7]; float s = 0;
    for (int i = 0; i < 7; i++) { v[i] = bf2f(x[row * D_ + t + 64 * i]); s += v[i]; }
    for (int m = 32; m >= 1; m >>= 1) s += __shfl_xor(s, m);
    float mean = s / D_; float q = 0;
    for (int i = 0; i < 7; i++) { float d = v[i] - mean; q += d * d; }
    for (int m = 32; m >= 1; m >>= 1) q += __shfl_xor(q, m);
    float var = rbf(q / D_); float mb = rbf(mean);
    float sd = rbf(sqrtf(rbf(var + 1e-5f)));
    for (int i = 0; i < 7; i++) xn[row * D_ + t + 64 * i] = f2bf(rbf(v[i] - mb) / sd);
}

__global__ void k_gen(const u16* Are, const u16* Aim, const u16* Bre, const u16* Bim, const u16* Cre, const u16* Cim,
                      const u16* logdt, float* K) {
    int d = blockIdx.x; int t = threadIdx.x;
    __shared__ float sa[N_], sb[N_], cr[N_], ci[N_];
    float dt = rbf(expf(bf2f(logdt[d])));
    {
        int n = t;
        float ar = bf2f(Are[d * N_ + n]) * dt, ai = bf2f(Aim[d * N_ + n]) * dt;
        float br = bf2f(Bre[d * N_ + n]) * dt, bi = bf2f(Bim[d * N_ + n]) * dt;
        float c0 = bf2f(Cre[d * N_ + n]), c1 = bf2f(Cim[d * N_ + n]);
        sa[n] = ar; sb[n] = ai; cr[n] = c0 * br - c1 * bi; ci[n] = c0 * bi + c1 * br;
    }
    __syncthreads();
    for (int l = t; l < L_; l += 256) {
        float lf = rbf((float)l);
        float acc = 0;
        for (int n = 0; n < N_; n++) {
            float e = expf(sa[n] * lf); float s, c; sincosf(sb[n] * lf, &s, &c);
            acc += e * (cr[n] * c - ci[n] * s);
        }
        K[d * L_ + l] = acc;
    }
}

__global__ void k_conv(const float* K, const u16* xn, u16* y) {
    int d = blockIdx.x; int t = threadIdx.x;
    __shared__ float sk[L_], su[L_];
    for (int l = t; l < L_; l += 256) { sk[l] = K[d * L_ + l]; su[l] = bf2f(xn[l * D_ + d]); }
    __syncthreads();
    for (int o = t; o < L_; o += 256) {
        float acc = 0;
        for (int l = 0; l <= o; l++) acc += sk[l] * su[o - l];
        y[o * D_ + d] = f2bf(acc);
    }
}

__global__ void k_g1(const u16* xn, const u16* w1, u16* h) {
    int row = blockIdx.x; int t = threadIdx.x;
    __shared__ float sx[D_];
    for (int i = t; i < D_; i += 256) sx[i] = bf2f(xn[row * D_ + i]);
    __syncthreads();
    for (int f = t; f < F_; f += 256) {
        float acc = 0;
        for (int k = 0; k < D_; k++) acc += sx[k] * bf2f(w1[k * F_ + f]);
        h[row * F_ + f] = f2bf(fmaxf(rbf(acc), 0.f));
    }
}

__global__ void k_g2(const u16* h, const u16* w2, const u16* x, const u16* xn, const u16* y, const u16* Dk, u16* out) {
    int row = blockIdx.x; int t = threadIdx.x;
    __shared__ float sh[F_];
    for (int i = t; i < F_; i += 448) sh[i] = bf2f(h[row * F_ + i]);
    __syncthreads();
    float acc = 0;
    for (int k = 0; k < F_; k++) acc += sh[k] * bf2f(w2[k * D_ + t]);
    float a = rbf(bf2f(y[row * D_ + t]) + rbf(bf2f(xn[row * D_ + t]) * bf2f(Dk[t])));
    float r = rbf(bf2f(x[row * D_ + t]) + a);
    out[row * D_ + t] = f2bf(r + rbf(acc));
}


#define REP8(x) x x x x x x x x
#define REP64(x) REP8(REP8(x))
#define REP512(x) REP8(REP64(x))
__device__ __forceinline__ unsigned long long rt() { return __builtin_amdgcn_s_memrealtime(); }
__global__ __launch_bounds__(512, 2) void k_probe(float* sink) {
    unsigned long long t00 = rt();
    int b = blockIdx.x; int tid = threadIdx.x, lane = tid & 63; int wave = __builtin_amdgcn_readfirstlane(tid >> 6);
    float a0 = threadIdx.x * 0.001f, a1 = a0 + 1, a2 = a0 + 2, a3 = a0 + 3; float c = 0.999f;
    unsigned dd = 0;

    { unsigned long long pc; asm volatile("s_getpc_b64 %0" : "=s"(pc));
      const char* p = (const char*)(pc & ~127ull) + min((b >> 3) * 5 + lane, 140) * 128;
      if (tid < 5) asm volatile("global_load_dword %0, %1, off" : "+v"(dd) : "v"(p) : "memory"); }

    while (rt() < t00 + 150) __builtin_amdgcn_s_sleep(1);

    unsigned long long t0 = rt();
    REP512(asm volatile("v_fma_f32 %0, %0, %4, %0\n v_fma_f32 %1, %1, %4, %1\n v_fma_f32 %2, %2, %4, %2\n v_fma_f32 %3, %3, %4, %3" : "+v"(a0), "+v"(a1), "+v"(a2), "+v"(a3) : "v"(c));)
    unsigned long long t2 = rt();
    asm volatile("s_waitcnt vmcnt(0)" :: "v"(dd) : "memory");
    if (b == 9 && wave == 0) { unsigned long long tend = t2 + 50 * (t2 - t0); while (rt() < tend) __builtin_amdgcn_s_sleep(8); }
    if (a0 + a1 + a2 + a3 == 0.12345f) sink[tid] = a0;
}

extern "C" void kernel_launch(void* const* d_in, const int* in_sizes, int n_in,
                              void* d_out, int out_size, void* d_ws, size_t ws_size,
                              hipStream_t stream) {
    const u16* x = (const u16*)d_in[0];
    char* ws = (char*)d_ws;
    u16* xn = (u16*)ws; ws += L_ * D_ * 2;
    u16* y = (u16*)ws; ws += L_ * D_ * 2;
    u16* h = (u16*)ws; ws += L_ * F_ * 2;
    float* K = (float*)ws;
    k_probe<<<256, 512, 0, stream>>>((float*)((char*)d_ws + (64 << 20)));
    k_ln<<<L_, 64, 0, stream>>>(x, xn);
    k_gen<<<D_, 256, 0, stream>>>((const u16*)d_in[1], (const u16*)d_in[2], (const u16*)d_in[3], (const u16*)d_in[4], (const u16*)d_in[5], (const u16*)d_in[6], (const u16*)d_in[7], K);
    k_conv<<<D_, 256, 0, stream>>>(K, xn, y);
    k_g1<<<L_, 256, 0, stream>>>(xn, (const u16*)d_in[10], h);
    k_g2<<<L_, 448, 0, stream>>>(h, (const u16*)d_in[11], x, xn, y, (const u16*)d_in[8], (u16*)d_out);
}
```

```cpp
#include <hip/hip_runtime.h>
#include <stdint.h>

typedef unsigned short u16;
typedef unsigned int u32;
typedef unsigned long long u64;
typedef __attribute__((ext_vector_type(8))) __bf16 bf8;
typedef __attribute__((ext_vector_type(2))) __bf16 bf2;
typedef __attribute__((ext_vector_type(4))) short s4;
typedef __attribute__((ext_vector_type(4))) float f4;
typedef __attribute__((ext_vector_type(2))) float f2;
typedef __attribute__((ext_vector_type(4))) u32 u4;
typedef __attribute__((ext_vector_type(2))) u32 u2;
#define LAS __attribute__((address_space(3)))
#define DI __device__ __forceinline__
#define LDS(T, off) (*(LAS T*)(uintptr_t)(u32)(off))

constexpr u32 WS_EPOCH = 0;
constexpr u32 WS_STAT = 4096;
constexpr u32 WS_CL = WS_STAT + 16384;
constexpr u32 WS_XG = 65536;
constexpr u32 WS_YG = WS_XG + 3670016;
constexpr u32 WS_H = WS_YG + 3670016;
constexpr u32 WS_STAMP = 32u << 20;
constexpr u32 L_SCR = 98304;
constexpr u32 L_EX = 131072;
constexpr u32 L_XT = L_EX + 2048;
constexpr u32 L_KC = L_EX + 9344;
constexpr u32 L_TAB = L_EX + 12416;
constexpr u32 L_CS = L_EX + 20608;
constexpr u32 L_U = L_EX + 23552;
constexpr u32 LDS_BYTES = 163840;
constexpr int SPIN_MAX = 1 << 16;

#define STAMP(i)
#define STAMP_DECL
#define STAMP_FLUSH(n)

DI float bflo(u32 v) { return __uint_as_float(v << 16); }
DI float bfhi(u32 v) { return __uint_as_float(v & 0xffff0000u); }
DI u32 pkbf(float a, float b) { f2 v = {a, b}; return __builtin_bit_cast(u32, __builtin_convertvector(v, bf2)); }
DI float rbf(float a) { return bflo(pkbf(a, a)); }
DI f4 mfma(u4 a, u4 b, f4 c) { return __builtin_amdgcn_mfma_f32_16x16x32_bf16(__builtin_bit_cast(bf8, a), __builtin_bit_cast(bf8, b), c, 0, 0, 0); }
DI u2 trread(u32 addr) { return __builtin_bit_cast(u2, __builtin_amdgcn_ds_read_tr16_b64_v4i16((LAS s4*)(uintptr_t)addr)); }
template <int C> DI float dpp(float x) { return __builtin_bit_cast(float, __builtin_amdgcn_mov_dpp(__builtin_bit_cast(int, x), C, 0xf, 0xf, true)); }
DI float xsum16_32(float x) {
    auto s = __builtin_amdgcn_permlane16_swap(__float_as_uint(x), __float_as_uint(x), false, false);
    x = __uint_as_float(s[0]) + __uint_as_float(s[1]);
    auto t = __builtin_amdgcn_permlane32_swap(__float_as_uint(x), __float_as_uint(x), false, false);
    return __uint_as_float(t[0]) + __uint_as_float(t[1]);
}
DI float wsum(float x) { x += dpp<0xB1>(x); x += dpp<0x4E>(x); x += dpp<0x141>(x); x += dpp<0x140>(x); return xsum16_32(x); }
DI f4 self4(int c, f4 a, f4 b) { return c ? a : b; }
DI float sum2(u32 v, float c) { return __builtin_amdgcn_fdot2_f32_bf16(__builtin_bit_cast(bf2, v), __builtin_bit_cast(bf2, 0x3f803f80u), c, false); }
DI f2 cmul(f2 a, f2 b) { f2 r; r.x = a.x * b.x - a.y * b.y; r.y = a.x * b.y + a.y * b.x; return r; }
DI f2 zpow(float a2, float rev, float p) {
    float e = __builtin_amdgcn_exp2f(a2 * p), r = __builtin_amdgcn_fractf(rev * p);
    f2 z = {e * __builtin_amdgcn_cosf(r), e * __builtin_amdgcn_sinf(r)}; return z;
}

#define BARRIER() asm volatile("s_waitcnt lgkmcnt(0)\n\ts_barrier" ::: "memory")
#define WAITV(n) asm volatile("s_waitcnt vmcnt(" #n ")" ::: "memory")
DI void dma16(u32 voff, const void* base, u32 lds) {
    asm volatile("s_mov_b32 m0, %2\n\ts_nop 0\n\tglobal_load_lds_dwordx4 %0, %1" :: "v"(voff), "s"(base), "s"(lds) : "memory");
}
#define GLD4(dst, voff, base, mod) asm volatile("global_load_dwordx4 %0, %1, %2 " mod : "+v"(dst) : "v"(voff), "s"(base) : "memory")
#define GLD2(dst, voff, base, mod) asm volatile("global_load_dwordx2 %0, %1, %2 " mod : "+v"(dst) : "v"(voff), "s"(base) : "memory")
#define GLDH(dst, voff, base) asm volatile("global_load_ushort %0, %1, %2" : "=v"(dst) : "v"(voff), "s"(base) : "memory")
#define GST4(voff, val, base, mod) asm volatile("global_store_dwordx4 %0, %1, %2 " mod "\n\ts_nop 1" :: "v"(voff), "v"(val), "s"(base) : "memory")
#define GST2(voff, val, base, mod) asm volatile("global_store_dwordx2 %0, %1, %2 " mod :: "v"(voff), "v"(val), "s"(base) : "memory")
#define PIN(x) asm volatile("" : "+v"(x))
#define UNDEF(x) asm volatile("" : "=v"(x))

DI int cidx(int l) {
    int s = (l >= 256) + (l >= 512) + (l >= 1024);
    int r = (l + ((1 << s) >> 1) - (s > 0) + ((l >> s) & (s > 0))) >> s;
    return l < 0 ? 641 : 128 * s + r;
}

template <int TS>
DI void conv_pair(f4 (&acc)[4], int k0, u32 ub, u32 la, u32 q2, u32 p3, const u4& sel2, const u4& sel3) {
    constexpr int T[2][4] = {{0, 3, 4, 7}, {1, 2, 5, 6}};
    constexpr int NT = T[TS][3] + 1;
#pragma unroll 1
    for (int kk = 0; kk < 2; ++kk, ++k0) {
        u4 A[NT], B[NT];
        const u32 u = ub - 64 * k0, l = la - 64 * k0, q = q2 + 64 * k0, p = p3 + 16 * k0;
#pragma unroll
        for (int s = 0; s < NT; ++s) B[s] = LDS(u4, u + 512 * s);
        auto lin = [&](u32 a) { u4 r = {LDS(u32, a), LDS(u32, a + 4), LDS(u32, a + 8), LDS(u32, a + 12)}; return r; };
        auto seg2 = [&](u32 a) { u2 v = LDS(u2, a); u4 r;
            r.x = __builtin_amdgcn_perm(v.y, v.x, sel2.x); r.y = __builtin_amdgcn_perm(v.y, v.x, sel2.y);
            r.z = __builtin_amdgcn_perm(v.y, v.x, sel2.z); r.w = __builtin_amdgcn_perm(v.y, v.x, sel2.w); return r; };
        auto seg3 = [&](u32 a) { u32 v = LDS(u32, a); u4 r;
            r.x = __builtin_amdgcn_perm(v, v, sel3.x); r.y = __builtin_amdgcn_perm(v, v, sel3.y);
            r.z = __builtin_amdgcn_perm(v, v, sel3.z); r.w = __builtin_amdgcn_perm(v, v, sel3.w); return r; };
        A[0] = lin(l + 1120); A[1] = lin(l + 608);
        if (k0 == 0) { A[2] = lin(la + 96); A[4] = lin(la); }
        else { A[2] = seg2(q); A[4] = seg3(p); }
        A[3] = seg2(q + 512);
#pragma unroll
        for (int d = 5; d < NT; ++d) A[d] = seg3(p + 128 * (d - 4));
#pragma unroll
        for (int d = 0; d < NT; ++d)
#pragma unroll
            for (int i = 0; i < 4; ++i)
                if (T[TS][i] >= d) acc[i] = mfma(A[d], B[T[TS][i] - d], acc[i]);
    }
}

__global__ __launch_bounds__(512, 2) void s4ffn_kernel(const u16* __restrict__ x, const u16* __restrict__ Are, const u16* __restrict__ Aim,
        const u16* __restrict__ Bre, const u16* __restrict__ Bim, const u16* __restrict__ Cre, const u16* __restrict__ Cim,
        const u16* __restrict__ logdt, const u16* __restrict__ Dsk, const u16* __restrict__ w1, const u16* __restrict__ w2,
        u16* __restrict__ out, char* __restrict__ ws) {
    const int tid = threadIdx.x, lane = tid & 63, wave = __builtin_amdgcn_readfirstlane(tid >> 6);
    const int b = blockIdx.x, xcd = b & 7, idx = b >> 3;
    const int fl = lane & 15, g = lane >> 4;
    u32 pfd = 0;
    STAMP_DECL;
    {
        u64 pc; asm volatile("s_getpc_b64 %0" : "=s"(pc));
        pc &= ~127ull;
        if (tid < 5) asm volatile("global_load_dword %0, %1, %2" : "+v"(pfd) : "v"((idx * 5 + tid) * 128), "s"(pc) : "memory");
    }
    STAMP(0);
    asm volatile("" :: "s"(x), "s"(Are), "s"(Aim), "s"(Bre), "s"(Bim), "s"(Cre), "s"(Cim), "s"(logdt), "s"(Dsk), "s"(w1), "s"(w2), "s"(out), "s"(ws));
    u32 tag; asm volatile("s_load_dword %0, %1, 0x0" : "=s"(tag) : "s"(ws));
    const bool ffn = idx < 28;
    const int q7 = idx / 7, r7 = idx - 7 * q7;
    const int rp = 4 * (xcd >> 1) + q7, cc = 7 * (xcd & 1) + r7, P = 4 * xcd + q7, ct = r7, dp = 28 * xcd + idx;
    u4 xr; UNDEF(xr); GLD4(xr, min(lane, 55) * 16, (const char*)x + (b * 8 + wave) * 896, "");
    u32 pa0, pa1, pb0, pb1, pc0, pc1;
    u32 ldt2, dsk2;
    asm volatile("s_load_dword %0, %1, 0x0" : "=s"(ldt2) : "s"((const char*)logdt + min(dp, 223) * 4));
    asm volatile("s_load_dword %0, %1, 0x0" : "=s"(dsk2) : "s"((const char*)Dsk + min(dp, 223) * 4));
    u32 vx0, vx1, vw0, vw1; const char *xb, *wb; int kt;
    const int rg = wave >> 1, ch = wave & 1;
    const char* hb = ws + WS_H + ((2 * rp + (rg >> 1)) * 56 + 4 * cc + 2 * ch) * 4096 + (rg & 1) * 2048;
    if (ffn) {
        const u32 po = ((2 * dp + (wave >> 2)) * 256 + (wave & 3) * 64) * 2;
        GLDH(pa0, lane * 2, (const char*)Are + po); GLDH(pa1, lane * 2, (const char*)Aim + po);
        GLDH(pb0, lane * 2, (const char*)Bre + po); GLDH(pb1, lane * 2, (const char*)Bim + po);
        GLDH(pc0, lane * 2, (const char*)Cre + po); GLDH(pc1, lane * 2, (const char*)Cim + po);
        kt = (r7 + 2 * q7) % 7;
        const int r8 = lane >> 3, p8 = lane & 7;
        vx0 = (16 * wave + r8) * 896 + 16 * (p8 ^ (r8 >> 1));
        vx1 = (16 * wave + 8 + r8) * 896 + 16 * (p8 ^ (4 + (r8 >> 1)));
        vw0 = (8 * wave + g) * 3584 + 16 * (fl ^ ((g << 2) | ((2 * wave) & 3)));
        vw1 = (8 * wave + 4 + g) * 3584 + 16 * (fl ^ ((g << 2) | ((2 * wave + 1) & 3)));
        xb = (const char*)x + rp * 114688; wb = (const char*)w1 + cc * 256;
    }
#define RING_DMA(slot) do { const u32 l_ = (slot) * 32768 + wave * 2048; \
        dma16(vx0, xb + kt * 128, l_); dma16(vx1, xb + kt * 128, l_ + 1024); \
        dma16(vw0, wb + kt * 229376, l_ + 16384); dma16(vw1, wb + kt * 229376, l_ + 17408); \
        kt = kt == 6 ? 0 : kt + 1; } while (0)
    if (ffn) {
        RING_DMA(0); RING_DMA(1);
        const u4 ones = {~0u, ~0u, ~0u, ~0u};
        GST4(lane * 16, ones, hb, "sc1"); GST4(lane * 16, ones, hb, "offset:1024 sc1");
        GST4(lane * 16, ones, hb + 4096, "sc1"); GST4(lane * 16, ones, hb + 4096, "offset:1024 sc1");
        WAITV(12);
    } else WAITV(0);
    asm volatile("s_waitcnt lgkmcnt(0)" : "+v"(xr), "+s"(tag), "+s"(ldt2), "+s"(dsk2), "+v"(pfd) :: "memory");
    tag += 1;
    STAMP(1);
    {
        float v[8];
#pragma unroll
        for (int i = 0; i < 4; ++i) { v[2 * i] = bflo(xr[i]); v[2 * i + 1] = bfhi(xr[i]); }
        const float act = lane < 56 ? 1.f : 0.f;
        float s = ((v[0] + v[1]) + (v[2] + v[3])) + ((v[4] + v[5]) + (v[6] + v[7]));
        const float mean = wsum(s * act) * (1.f / 448.f);
        float q = 0;
#pragma unroll
        for (int i = 0; i < 8; ++i) { float d = v[i] - mean; q += d * d; }
        const float var = wsum(q * act) * (1.f / 448.f);
        const float mb = rbf(mean), sd = rbf(__builtin_sqrtf(rbf(rbf(var) + 1e-5f))), rs = 1.f / sd;
        u4 o;
#pragma unroll
        for (int i = 0; i < 4; ++i) { u32 t = pkbf(v[2 * i] - mb, v[2 * i + 1] - mb); o[i] = pkbf(bflo(t) * rs, bfhi(t) * rs); }
        if (lane < 56) LDS(u4, L_XT + wave * 912 + lane * 16) = o;
        u2 st = {(__float_as_uint(mb) >> 16) | __float_as_uint(sd), tag};
        if (lane == 0) GST2(0, st, ws + WS_STAT + (b * 8 + wave) * 8, "sc1");
        if (tid < 240) LDS(u32, L_U + (tid >= 120 ? 4608 - 480 : 0) + tid * 4) = 0;
        BARRIER();
        if (lane < 56) {
#pragma unroll
            for (int i = 0; i < 2; ++i) {
                const int o16 = wave * 112 + i * 56 + lane, p = o16 >> 2, qq = o16 & 3;
                const u32 a = L_XT + qq * 1824 + p * 4;
                u4 gr = {LDS(u32, a), tag, LDS(u32, a + 912), tag};
                GST4(o16 * 16, gr, ws + WS_XG + b * 14336, "sc1");
            }
        }
    }
    STAMP(2);
    if (!ffn) return;
    RING_DMA(2);
    asm volatile("" : "+v"(pa0), "+v"(pa1), "+v"(pb0), "+v"(pb1), "+v"(pc0), "+v"(pc1));
    const int dsel = wave >> 2;
    {
        const float dt = rbf(expf(dsel ? bfhi(ldt2) : bflo(ldt2)));
        const float a2 = dt * bflo(pa0) * 1.44269504089f, rev = dt * bflo(pa1) * 0.15915494309f;
        f2 bb = {dt * bflo(pb0), dt * bflo(pb1)}, cv = {bflo(pc0), bflo(pc1)};
        const f2 c = cmul(cv, bb);
        const u32 sw = L_SCR + wave * 4352 + lane * 4, sr = L_SCR + wave * 4352 + fl * 272 + g * 16;
        const f2 z1 = zpow(a2, rev, 1.f), z8 = zpow(a2, rev, 8.f), z64 = zpow(a2, rev, 64.f), z256 = zpow(a2, rev, 256.f);
        const f2 z2 = cmul(z1, z1), z4 = cmul(z2, z2);
#define PUTC(e, v) LDS(u32, sw + (e) * 272) = pkbf((v).x, -(v).y)
#define PUTR(e, v) LDS(u32, sw + (e) * 272) = pkbf((v).x, (v).y)
        f2 e1 = cmul(c, z256), e2 = cmul(e1, z256), e3 = cmul(e2, z256);
        { f2 t = c; PUTC(0, t);
#pragma unroll
          for (int j = 1; j < 8; ++j) { t = cmul(t, z1); PUTC(j, t); }
          t = e1; PUTC(8, t);
#pragma unroll
          for (int j = 9; j < 12; ++j) { t = cmul(t, z2); PUTC(j, t); }
          PUTC(12, e2); t = cmul(e2, z4); PUTC(13, t); PUTC(14, e3); t = cmul(e3, z4); PUTC(15, t); }
        u4 B0[4], B1[4];
#pragma unroll
        for (int s = 0; s < 4; ++s) B0[s] = LDS(u4, sr + s * 64);
        asm volatile("" ::: "memory");
#pragma unroll
        for (int j = 0; j < 5; ++j) { e3 = cmul(e3, z256); PUTC(j, e3); }
#pragma unroll
        for (int s = 0; s < 4; ++s) B1[s] = LDS(u4, sr + s * 64);
        asm volatile("" ::: "memory");
        f4 dk[4];
        f2 r0 = {1.f, 0.f}, r1 = z64;
        const f2 z128 = cmul(z64, z64), z192 = cmul(z128, z64);
#pragma unroll
        for (int h = 0; h < 2; ++h) {
            if (h) { r0 = z128; r1 = z192; }
            PUTR(0, r0); PUTR(8, r1);
#pragma unroll
            for (int j = 1; j < 8; ++j) { r0 = cmul(r0, z8); r1 = cmul(r1, z8); PUTR(j, r0); PUTR(8 + j, r1); }
            f4 d0 = {0, 0, 0, 0}, d1 = {0, 0, 0, 0};
#pragma unroll
            for (int s = 0; s < 4; ++s) { u4 a = LDS(u4, sr + s * 64); d0 = mfma(a, B0[s], d0); d1 = mfma(a, B1[s], d1); }
            asm volatile("" ::: "memory");
            dk[2 * h] = d0; dk[2 * h + 1] = d1;
        }
#pragma unroll
        for (int t = 0; t < 4; ++t) LDS(f4, L_SCR + wave * 4352 + t * 1024 + lane * 16) = dk[t];
        WAITV(7);
        { u2 cl = {tag, tag}; if (lane == 0) GST2(0, cl, ws + WS_CL + ((rp * 14 + cc) * 8 + wave) * 8, "sc1"); }
        BARRIER();
        {
            const u32 a = L_SCR + dsel * 4 * 4352 + (wave & 3) * 1024 + lane * 16;
            f4 s = (LDS(f4, a) + LDS(f4, a + 4352)) + (LDS(f4, a + 8704) + LDS(f4, a + 13056));
            const int j = (wave & 1) * 16 + fl, i0 = (wave & 2) * 8 + 4 * g;
            const int mul = j < 8 ? 8 : j < 12 ? 4 : j < 16 ? 2 : 1;
            const int base = j < 8 ? j : j < 12 ? 248 + j : j < 16 ? 384 + (j & 1) + 64 * ((j - 12) >> 1) : 512 + 32 * (j - 16);
            const u32 o = L_KC + dsel * 1536 + (base + mul * i0) * 2;
            const u32 p01 = pkbf(s.x, s.y), p23 = pkbf(s.z, s.w);
            if (j < 20 || (j == 20 && i0 == 0)) LDS(u16, o) = (u16)p01;
            if (j < 20) { LDS(u16, o + mul * 2) = (u16)(p01 >> 16); LDS(u16, o + mul * 4) = (u16)p23; LDS(u16, o + mul * 6) = (u16)(p23 >> 16); }
            if (j == 21 && i0 == 0) LDS(u16, L_KC + dsel * 1536 + 641 * 2) = 0;
        }
        BARRIER();
        RING_DMA(3);
        {
            const int t = tid & 255;
            const u32 kc = L_KC + dsel * 1536, tb = L_TAB + dsel * 4096;
            auto put = [&](u32 dst, int i0, int i1) { LDS(u32, dst) = (u32)LDS(u16, kc + i0 * 2) | ((u32)LDS(u16, kc + i1 * 2) << 16); };
            auto lin = [&](int u) { const int pi = u >= 304, ip = u - 304 * pi, top = (ip < 24 ? 1056 : 592) - pi - 2 * ip; put(tb + u * 4, cidx(top), cidx(top - 1)); };
            lin(t); lin(t + 256);
            if (t < 96) lin(t + 512);
            else if (t < 223) put(tb + 3456 + (t - 96) * 4, 417 + t, 418 + t);
            if (t < 246) { const int i0 = 388 + (t >> 1) + 2 * (t & 1); put(tb + 2432 + t * 4, i0, i0 + 1); }
        }
    }
    STAMP(3);
    f4 acc[4][2];
#pragma unroll
    for (int c = 0; c < 4; ++c) { acc[c][0] = f4{0, 0, 0, 0}; acc[c][1] = f4{0, 0, 0, 0}; }
    float csum0 = 0, csum1 = 0;
    u2 st0, st1; u4 xg0, xg1; u2 clf;
    {
        const int q4 = fl >> 2, p4 = lane & 3;
        const u32 ax0 = ((32 * rg + fl) * 128 + 16 * (g ^ (fl >> 1))) ^ (64 * (rg & 1));
        const u32 aw0 = (16384 + (8 * g + q4) * 256 + 16 * ((8 * ch + (p4 >> 1)) ^ ((q4 << 2) | ((2 * g) & 3))) + 8 * (p4 & 1)) ^ (64 * (rg >> 1)) ^ (8192 * (rg & 1));
        auto tile = [&](int s) {
            const u32 so = (s & 3) * 32768;
#pragma unroll
            for (int ks = 0; ks < 2; ++ks) {
                u4 xf[2], wf[4];
#pragma unroll
                for (int tt = 0; tt < 2; ++tt) xf[tt] = LDS(u4, so + (ax0 ^ (64 * ks)) + tt * 2048);
#pragma unroll
                for (int c = 0; c < 4; ++c) {
                    u2 lo = trread(so + (aw0 ^ (32 * c) ^ (ks * 8192))), hi = trread(so + (aw0 ^ (32 * c) ^ 1040 ^ (ks * 8192)));
                    wf[c] = u4{lo.x, lo.y, hi.x, hi.y};
                }
                if (ks == 0) {
#pragma unroll
                    for (int i = 0; i < 4; ++i) {
                        csum0 = sum2(wf[0][i], csum0); csum1 = sum2(wf[1][i], csum1);
                    }
                }
#pragma unroll
                for (int c = 0; c < 4; ++c)
#pragma unroll
                    for (int tt = 0; tt < 2; ++tt) acc[c][tt] = mfma(wf[c], xf[tt], acc[c][tt]);
            }
        };
        WAITV(8);
        BARRIER();
#pragma unroll 1
        for (int s = 0; s < 4; ++s) { tile(s); WAITV(8); BARRIER(); if (s < 3) RING_DMA(s); }
        UNDEF(st0); UNDEF(st1); UNDEF(xg0); UNDEF(xg1);
        GLD2(st0, (32 * rg + fl) * 8, ws + WS_STAT + rp * 1024, "sc1"); GLD2(st1, (32 * rg + fl) * 8, ws + WS_STAT + rp * 1024, "offset:128 sc1");
        GLD4(xg0, (tid >> 2) * 14336 + (tid & 3) * 16, ws + WS_XG + dp * 64, "sc1");
        GLD4(xg1, (tid >> 2) * 14336 + (tid & 3) * 16, ws + WS_XG + dp * 64 + 128 * 14336, "sc1");
#pragma unroll 1
        for (int s = 4; s < 7; ++s) { tile(s); if (s == 4) { WAITV(8); BARRIER(); } else if (s == 5) { WAITV(4); BARRIER(); } }
        csum0 = xsum16_32(csum0); csum1 = xsum16_32(csum1);
        if (lane < 32) LDS(float, L_CS + (rg & 1) * 512 + (64 * ch + 32 * (rg >> 1) + lane) * 4) = lane < 16 ? csum0 : csum1;
    }
    BARRIER();
    STAMP(4);
    const u32 reg16 = wave * 16384;
    const u32 vw2 = (lane >> 3) * 896 + 16 * ((lane & 7) ^ (((lane >> 4) & 3) << 1));
    const char* w2b = (const char*)w2 + wave * 224 * 896 + ct * 128;
    UNDEF(clf);
    GLD2(clf, min(lane, 55) % 14 * 64 + min(lane, 55) / 14 * 8, ws + WS_CL + (P >> 1) * 896 + (P & 1) * 32, "sc1");
#pragma unroll 1
    for (int i = 0; i < 16; ++i) dma16(vw2, w2b + i * 7168, reg16 + i * 1024);
    {
        asm volatile("s_waitcnt vmcnt(19)" : "+v"(st0), "+v"(st1) :: "memory");
        for (int sp = 0; __any(st0.y != tag || st1.y != tag) && sp < SPIN_MAX; ++sp) {
            GLD2(st0, (32 * rg + fl) * 8, ws + WS_STAT + rp * 1024, "sc1"); GLD2(st1, (32 * rg + fl) * 8, ws + WS_STAT + rp * 1024, "offset:128 sc1");
            asm volatile("s_waitcnt vmcnt(0)" : "+v"(st0), "+v"(st1) :: "memory");
        }
        f4 cs[4];
#pragma unroll
        for (int c = 0; c < 4; ++c) { const u32 a = L_CS + ((64 * ch + 16 * c + 4 * g) * 4 ^ (128 * (rg >> 1))); cs[c] = LDS(f4, a) + LDS(f4, a + 512); }
#pragma unroll
        for (int tt = 0; tt < 2; ++tt) {
            const u32 sv = tt ? st1.x : st0.x;
            const float m = bflo(sv), rs = __builtin_amdgcn_rcpf(bfhi(sv));
#pragma unroll
            for (int k = 0; k < 2; ++k) {
                u4 o;
#pragma unroll
                for (int i = 0; i < 4; ++i) {
                    const int c = 2 * k + (i >> 1), r = 2 * (i & 1);
                    const float v0 = (acc[c][tt][r] - m * cs[c][r]) * rs, v1 = (acc[c][tt][r + 1] - m * cs[c][r + 1]) * rs;
                    o[i] = pkbf(fmaxf(v0, 0.f), fmaxf(v1, 0.f)) & 0x7fff7fffu;
                }
                const char* hp = hb + (k ^ (rg >> 1)) * 4096;
                if (tt) GST4(lane * 16, o, hp, "offset:1024 sc1"); else GST4(lane * 16, o, hp, "sc1");
            }
        }
    }
    STAMP(5);
    {
        asm volatile("s_waitcnt vmcnt(21)" : "+v"(xg0), "+v"(xg1) :: "memory");
        for (int sp = 0; __any(xg0.y != tag || xg0.w != tag || xg1.y != tag || xg1.w != tag) && sp < SPIN_MAX; ++sp) {
            GLD4(xg0, (tid >> 2) * 14336 + (tid & 3) * 16, ws + WS_XG + dp * 64, "sc1");
            GLD4(xg1, (tid >> 2) * 14336 + (tid & 3) * 16, ws + WS_XG + dp * 64 + 128 * 14336, "sc1");
            asm volatile("s_waitcnt vmcnt(0)" : "+v"(xg0), "+v"(xg1) :: "memory");
        }
        const u32 a = L_U + 480 + tid * 4;
        LDS(u32, a) = __builtin_amdgcn_perm(xg0.z, xg0.x, 0x05040100u); LDS(u32, a + 4608) = __builtin_amdgcn_perm(xg0.z, xg0.x, 0x07060302u);
        LDS(u32, a + 2048) = __builtin_amdgcn_perm(xg1.z, xg1.x, 0x05040100u); LDS(u32, a + 2048 + 4608) = __builtin_amdgcn_perm(xg1.z, xg1.x, 0x07060302u);
    }
    BARRIER();
    if (b == 0 && tid == 0) { u2 e = {tag, tag}; GST2(0, e, ws + WS_EPOCH, "sc1"); }
    STAMP(6);
    u4 Bw[7][4];
    const u32 aw2 = reg16 + (4 * g + (fl >> 2)) * 128 + 16 * (((lane & 3) >> 1) ^ (((2 * g + (fl >> 3)) & 3) << 1)) + 8 * (lane & 1);
#define W2_READ(j, slot) _Pragma("unroll") for (int c = 0; c < 4; ++c) { \
        u2 lo = trread((aw2 ^ (32 * c)) + (slot) * 4096), hi = trread((aw2 ^ (32 * c)) + (slot) * 4096 + 2048); Bw[j][c] = u4{lo.x, lo.y, hi.x, hi.y}; }
    f4 cacc[4];
    u4 hA0, hA1, hA2, hA3, hA4, hA5, hA6, hB0, hB1, hB2, hB3, hB4, hB5, hB6;
    const char* hrd = ws + WS_H + (P * 56 + wave * 7) * 4096;
#define H_LOAD(h, rho) do { const char* p_ = hrd + (rho) * 1024; GLD4(h##0, lane * 16, p_, "sc1"); GLD4(h##1, lane * 16, p_ + 4096, "sc1"); \
        GLD4(h##2, lane * 16, p_ + 8192, "sc1"); GLD4(h##3, lane * 16, p_ + 12288, "sc1"); GLD4(h##4, lane * 16, p_ + 16384, "sc1"); \
        GLD4(h##5, lane * 16, p_ + 20480, "sc1"); GLD4(h##6, lane * 16, p_ + 24576, "sc1"); } while (0)
#define H_UNDEF(h) do { UNDEF(h##0); UNDEF(h##1); UNDEF(h##2); UNDEF(h##3); UNDEF(h##4); UNDEF(h##5); UNDEF(h##6); } while (0)
    {
        const int kh = (wave >> 1) & 1, ts = wave & 1;
        const u32 tb = L_TAB + dsel * 4096;
        const u32 ub = L_U + dsel * 4608 + 2 * (224 + 16 * fl + 8 * g);
        const u32 la = tb + 4 * ((fl & 1) * 304 + ((16 - fl + 8 * g) >> 1));
        const int x2 = fl & 7, ml2 = x2 < 2 ? -2 : x2 < 5 ? -1 : 0;
        const u32 q2 = tb + 2432 + 8 * (2 * (2 + (fl >> 3) - g) + ml2 - 4);
        const int x3 = (fl + 8 * g) & 15, ml3 = x3 < 3 ? -1 : x3 < 12 ? 0 : 1;
        const u32 p3 = tb + 3456 + 4 * (2 * (1 + ((fl - 8 * g) >> 4)) + ml3 - 1);
        u4 sel2, sel3;
#pragma unroll
        for (int q = 0; q < 4; ++q) {
            u32 s2 = 0, s3 = 0;
#pragma unroll
            for (int hh = 0; hh < 2; ++hh) {
                const int e2 = x2 - 2 * q - hh, v2 = (e2 >= -5) + (e2 >= -2) + (e2 >= 3) + (e2 >= 6) - 2 - ml2;
                const int e3 = x3 - 2 * q - hh, v3 = (e3 >= -4) + (e3 >= 5) + (e3 >= 12) - 1 - ml3;
                s2 |= (u32)(v2 * 0x0202 + 0x0100) << (16 * hh); s3 |= (u32)(v3 * 0x0202 + 0x0100) << (16 * hh);
            }
            sel2[q] = s2; sel3[q] = s3;
        }
#pragma unroll
        for (int i = 0; i < 4; ++i) cacc[i] = f4{0, 0, 0, 0};
        if (ts) conv_pair<1>(cacc, 4 * kh, ub, la, q2, p3, sel2, sel3); else conv_pair<0>(cacc, 4 * kh, ub, la, q2, p3, sel2, sel3);
        WAITV(4);
        W2_READ(0, 0); W2_READ(1, 1); W2_READ(2, 2); W2_READ(3, 3);
        asm volatile("s_waitcnt lgkmcnt(0)" ::: "memory");
#pragma unroll 1
        for (int i = 0; i < 12; ++i) dma16(vw2, w2b + (16 + i) * 7168, reg16 + i * 1024);
        if (ts) conv_pair<1>(cacc, 4 * kh + 2, ub, la, q2, p3, sel2, sel3); else conv_pair<0>(cacc, 4 * kh + 2, ub, la, q2, p3, sel2, sel3);
        STAMP(7);
        PIN(clf);
        for (int sp = 0; __any(clf.x != tag) && sp < SPIN_MAX; ++sp) {
            GLD2(clf, min(lane, 55) % 14 * 64 + min(lane, 55) / 14 * 8, ws + WS_CL + (P >> 1) * 896 + (P & 1) * 32, "sc1");
            asm volatile("s_waitcnt vmcnt(0)" : "+v"(clf) :: "memory");
        }
        H_UNDEF(hA); H_UNDEF(hB);
        H_LOAD(hA, 0); H_LOAD(hB, 1);
        BARRIER();
        LDS(f4, L_EX + wave * 2048 + lane * 16) = self4(kh, cacc[0], cacc[2]);
        LDS(f4, L_EX + wave * 2048 + 1024 + lane * 16) = self4(kh, cacc[1], cacc[3]);
        BARRIER();
        const float dk = dsel ? bfhi(dsk2) : bflo(dsk2);
#pragma unroll
        for (int i = 0; i < 2; ++i) {
            const int tau = kh ? (ts ? 5 + i : 4 + 3 * i) : (ts ? 1 + i : 3 * i);
            const f4 y = self4(kh, cacc[2 + i], cacc[i]) + LDS(f4, L_EX + (wave ^ 2) * 2048 + i * 1024 + lane * 16);
            const u2 uu = LDS(u2, L_U + dsel * 4608 + 480 + (256 * tau + 16 * fl + 4 * g) * 2);
            const u32 y01 = pkbf(y.x, y.y), y23 = pkbf(y.z, y.w);
            const u32 s01 = pkbf(bflo(uu.x) * dk, bfhi(uu.x) * dk), s23 = pkbf(bflo(uu.y) * dk, bfhi(uu.y) * dk);
            const u4 o = {pkbf(bflo(y01) + bflo(s01), bfhi(y01) + bfhi(s01)), tag, pkbf(bflo(y23) + bflo(s23), bfhi(y23) + bfhi(s23)), tag};
            GST4((fl >> 2) * 114688 + ((fl & 3) + 4 * g) * 16, o, ws + WS_YG + (4 * tau * 448 + 2 * dp + dsel) * 256, "sc1");
        }
    }
    STAMP(8);
    WAITV(16);
    W2_READ(4, 0); W2_READ(5, 1); W2_READ(6, 2);
    u2 yv[8], xv[2];
    const int rho = wave >> 1;
#define OR4(v) ((v.x | v.y) | (v.z | v.w))
#define H_WAIT(h, NW) asm volatile("s_waitcnt vmcnt(" #NW ")" : "+v"(h##0), "+v"(h##1), "+v"(h##2), "+v"(h##3), "+v"(h##4), "+v"(h##5), "+v"(h##6) :: "memory")
#define H_OR(h) (OR4(h##0) | OR4(h##1) | OR4(h##2) | OR4(h##3) | OR4(h##4) | OR4(h##5) | OR4(h##6))
#define H_MMA(h, r) do { _Pragma("unroll") for (int c = 0; c < 4; ++c) { f4 a_ = mfma(Bw[0][c], h##0, f4{0, 0, 0, 0}); \
        a_ = mfma(Bw[1][c], h##1, a_); a_ = mfma(Bw[2][c], h##2, a_); a_ = mfma(Bw[3][c], h##3, a_); a_ = mfma(Bw[4][c], h##4, a_); \
        a_ = mfma(Bw[5][c], h##5, a_); a_ = mfma(Bw[6][c], h##6, a_); LDS(f4, reg16 + (4 * (r) + c) * 1024 + lane * 16) = a_; } } while (0)
#define H_TILE(h, r, NW) do { H_WAIT(h, NW); hbad |= H_OR(h); H_MMA(h, r); } while (0)
    u32 hbad = 0;
    H_TILE(hA, 0, 9);
    STAMP(9);
    H_LOAD(hA, 2);
    H_TILE(hB, 1, 9);
    H_LOAD(hB, 3);
    H_TILE(hA, 2, 7);
    const u32 yo = (32 * ch + 4 * g) * 256 + (rho + 4 * (fl >> 2)) * 16 + ((fl >> 1) & 1) * 8;
    const char* yb = ws + WS_YG + (P * 448 + 64 * ct) * 256;
#define Y_LOAD() do { GLD2(yv[0], yo, yb, "sc1"); GLD2(yv[1], yo, yb, "offset:256 sc1"); GLD2(yv[2], yo, yb, "offset:512 sc1"); GLD2(yv[3], yo, yb, "offset:768 sc1"); \
        GLD2(yv[4], yo, yb + 4096, "sc1"); GLD2(yv[5], yo, yb + 4096, "offset:256 sc1"); GLD2(yv[6], yo, yb + 4096, "offset:512 sc1"); GLD2(yv[7], yo, yb + 4096, "offset:768 sc1"); } while (0)
    const u32 xo = (16 * rho + fl) * 896 + (32 * ch + 4 * g) * 2;
#pragma unroll
    for (int i = 0; i < 8; ++i) UNDEF(yv[i]);
    UNDEF(xv[0]); UNDEF(xv[1]);
    Y_LOAD();
    GLD2(xv[0], xo, (const char*)x + P * 57344 + ct * 128, ""); GLD2(xv[1], xo, (const char*)x + P * 57344 + ct * 128, "offset:32");
    H_TILE(hB, 3, 10);
    if (__any(hbad & 0x80008000u)) {
#pragma unroll 1
        for (int r = 0; r < 4; ++r) {
            u4 t0, t1, t2, t3, t4, t5, t6;
            for (int sp = 0; sp < SPIN_MAX; ++sp) { H_UNDEF(t); H_LOAD(t, r); H_WAIT(t, 0); if (!__any(H_OR(t) & 0x80008000u)) break; }
            H_MMA(t, r);
        }
    }
    BARRIER();
    STAMP(10);
    f4 r2[2];
#pragma unroll
    for (int i = 0; i < 2; ++i) {
        const u32 a = (4 * rho + 2 * ch + i) * 1024 + lane * 16;
        r2[i] = ((LDS(f4, a) + LDS(f4, a + 16384)) + (LDS(f4, a + 32768) + LDS(f4, a + 49152))) +
                ((LDS(f4, a + 65536) + LDS(f4, a + 81920)) + (LDS(f4, a + 98304) + LDS(f4, a + 114688)));
    }
    asm volatile("s_waitcnt vmcnt(0)" : "+v"(yv[0]), "+v"(yv[1]), "+v"(yv[2]), "+v"(yv[3]), "+v"(yv[4]), "+v"(yv[5]), "+v"(yv[6]), "+v"(yv[7]), "+v"(xv[0]), "+v"(xv[1]) :: "memory");
    for (int sp = 0; sp < SPIN_MAX; ++sp) {
        u32 bad = 0;
#pragma unroll
        for (int i = 0; i < 8; ++i) bad |= yv[i].y ^ tag;
        if (!__any(bad)) break;
        Y_LOAD();
        asm volatile("s_waitcnt vmcnt(0)" : "+v"(yv[0]), "+v"(yv[1]), "+v"(yv[2]), "+v"(yv[3]), "+v"(yv[4]), "+v"(yv[5]), "+v"(yv[6]), "+v"(yv[7]) :: "memory");
    }
#pragma unroll
    for (int i = 0; i < 2; ++i) {
        float yy[4];
#pragma unroll
        for (int r = 0; r < 4; ++r) yy[r] = (lane & 1) ? bfhi(yv[4 * i + r].x) : bflo(yv[4 * i + r].x);
        const u32 a01 = pkbf(bflo(xv[i].x) + yy[0], bfhi(xv[i].x) + yy[1]), a23 = pkbf(bflo(xv[i].y) + yy[2], bfhi(xv[i].y) + yy[3]);
        const u32 f01 = pkbf(r2[i].x, r2[i].y), f23 = pkbf(r2[i].z, r2[i].w);
        const u2 o = {pkbf(bflo(a01) + bflo(f01), bfhi(a01) + bfhi(f01)), pkbf(bflo(a23) + bflo(f23), bfhi(a23) + bfhi(f23))};
        *(u2*)((char*)out + P * 57344 + ct * 128 + xo + i * 32) = o;
    }
    STAMP(11);
    STAMP_FLUSH(12);
}

extern "C" void kernel_launch(void* const* d_in, const int* in_sizes, int n_in, void* d_out, int out_size, void* d_ws, size_t ws_size,
                              hipStream_t stream) {
    (void)hipFuncSetAttribute((const void*)s4ffn_kernel, hipFuncAttributeMaxDynamicSharedMemorySize, LDS_BYTES);
    s4ffn_kernel<<<256, 512, LDS_BYTES, stream>>>((const u16*)d_in[0], (const u16*)d_in[1], (const u16*)d_in[2], (const u16*)d_in[3],
        (const u16*)d_in[4], (const u16*)d_in[5], (const u16*)d_in[6], (const u16*)d_in[7], (const u16*)d_in[8],
        (const u16*)d_in[10], (const u16*)d_in[11], (u16*)d_out, (char*)d_ws);
}
```

```cpp
#include <hip/hip_runtime.h>
#include <stdint.h>

typedef unsigned short u16;
typedef unsigned int u32;
typedef unsigned long long u64;
typedef __attribute__((ext_vector_type(8))) __bf16 bf8;
typedef __attribute__((ext_vector_type(2))) __bf16 bf2;
typedef __attribute__((ext_vector_type(4))) short s4;
typedef __attribute__((ext_vector_type(4))) float f4;
typedef __attribute__((ext_vector_type(16))) float f16v;
typedef __attribute__((ext_vector_type(2))) float f2;
typedef __attribute__((ext_vector_type(4))) u32 u4;
typedef __attribute__((ext_vector_type(2))) u32 u2;
#define LAS __attribute__((address_space(3)))
#define DI __device__ __forceinline__
#define LDS(T, off) (*(LAS T*)(uintptr_t)(u32)(off))

constexpr u32 WS_EPOCH = 0;
constexpr u32 WS_STAT = 4096;
constexpr u32 WS_AR = WS_STAT + 16384;
constexpr u32 WS_XG = 65536;
constexpr u32 WS_YG = WS_XG + 3670016;
constexpr u32 WS_H = WS_YG + 3670016;
constexpr u32 WS_STAMP = 32u << 20;
constexpr u32 L_TAB = 0;
constexpr u32 L_KC = 8192;
constexpr u32 L_CS = 11264;
constexpr u32 L_ST = 11776;
constexpr u32 L_SCR = 15360;
constexpr u32 L_U = 23552;
constexpr u32 L_RING = 32768;
constexpr u32 LDS_BYTES = 163840;
constexpr int SPIN_MAX = 1 << 16;

#define STAMP(i)
#define STAMPC(i)
#define ACC_BEGIN()
#define ACC_END(i)
#define STAMP_DECL
#define STAMP_FLUSH(n)

DI float bflo(u32 v) { return __uint_as_float(v << 16); }
DI float bfhi(u32 v) { return __uint_as_float(v & 0xffff0000u); }
DI u32 pkbf(float a, float b) { f2 v = {a, b}; return __builtin_bit_cast(u32, __builtin_convertvector(v, bf2)); }
DI float rbf(float a) { return bflo(pkbf(a, a)); }
DI f4 mfma(u4 a, u4 b, f4 c) { return __builtin_amdgcn_mfma_f32_16x16x32_bf16(__builtin_bit_cast(bf8, a), __builtin_bit_cast(bf8, b), c, 0, 0, 0); }
DI f16v mfma32(u4 a, u4 b, f16v c) { return __builtin_amdgcn_mfma_f32_32x32x16_bf16(__builtin_bit_cast(bf8, a), __builtin_bit_cast(bf8, b), c, 0, 0, 0); }
DI u2 trread(u32 addr) { return __builtin_bit_cast(u2, __builtin_amdgcn_ds_read_tr16_b64_v4i16((LAS s4*)(uintptr_t)addr)); }
DI u4 trread2(u32 a, u32 b) { const u2 l = trread(a), h = trread(b); return u4{l.x, l.y, h.x, h.y}; }
template <int C> DI float dpp(float x) { return __builtin_bit_cast(float, __builtin_amdgcn_mov_dpp(__builtin_bit_cast(int, x), C, 0xf, 0xf, true)); }
DI float rsum(float x) { x += dpp<0xB1>(x); x += dpp<0x4E>(x); x += dpp<0x141>(x); x += dpp<0x140>(x); return x; }
DI float sw32(float a, float b) { auto s = __builtin_amdgcn_permlane32_swap(__float_as_uint(a), __float_as_uint(b), false, false); return __uint_as_float(s[0]) + __uint_as_float(s[1]); }
DI float sw16(float a, float b) { auto s = __builtin_amdgcn_permlane16_swap(__float_as_uint(a), __float_as_uint(b), false, false); return __uint_as_float(s[0]) + __uint_as_float(s[1]); }
DI float dot2(u32 a, u32 b, float c) { return __builtin_amdgcn_fdot2_f32_bf16(__builtin_bit_cast(bf2, a), __builtin_bit_cast(bf2, b), c, false); }
DI f4 self4(int c, f4 a, f4 b) { return c ? a : b; }
DI f2 cmul(f2 a, f2 b) { float x, y;
    asm("v_mul_f32 %0, %2, %4\n\tv_mul_f32 %1, %3, %4\n\tv_fma_f32 %0, -%3, %5, %0\n\tv_fmac_f32 %1, %2, %5" : "=&v"(x), "=&v"(y) : "v"(a.x), "v"(a.y), "v"(b.x), "v"(b.y));
    return f2{x, y}; }
DI f2 cmulc(f2 a, f2 b) { float x, y;
    asm("v_mul_f32 %0, %2, %4\n\tv_mul_f32 %1, %3, %4\n\tv_fmac_f32 %0, %3, %5\n\tv_fma_f32 %1, -%2, %5, %1" : "=&v"(x), "=&v"(y) : "v"(a.x), "v"(a.y), "v"(b.x), "v"(b.y));
    return f2{x, y}; }
DI float fmul1(float a, float b) { float r; asm("v_mul_f32 %0, %1, %2" : "=v"(r) : "v"(a), "v"(b)); return r; }
DI u32 relu2(u32 v) { typedef __attribute__((ext_vector_type(2))) short s2; const s2 z = {0, 0}; return __builtin_bit_cast(u32, __builtin_elementwise_max(__builtin_bit_cast(s2, v), z)); }
DI f2 zpow(float a2, float rev, float p) {
    float e = __builtin_amdgcn_exp2f(a2 * p), r = __builtin_amdgcn_fractf(rev * p);
    float x, y;
    asm("s_nop 0\n\tv_mul_f32 %0, %2, %3\n\tv_mul_f32 %1, %2, %4" : "=&v"(x), "=&v"(y) : "v"(e), "v"(__builtin_amdgcn_cosf(r)), "v"(__builtin_amdgcn_sinf(r)));
    return f2{x, y};
}
DI f4 quad(const f16v& v, int q) { return f4{v[4 * q], v[4 * q + 1], v[4 * q + 2], v[4 * q + 3]}; }

#define SB() __builtin_amdgcn_sched_barrier(0)
#define BARRIER() asm volatile("s_waitcnt lgkmcnt(0)\n\ts_barrier" ::: "memory")
#define WAITV(n) asm volatile("s_waitcnt vmcnt(" #n ")" ::: "memory")
DI void dma16(u32 voff, const void* base, u32 lds) {
    asm volatile("s_mov_b32 m0, %2\n\ts_nop 0\n\tglobal_load_lds_dwordx4 %0, %1" :: "v"(voff), "s"(base), "s"(lds) : "memory");
}
#define GLD4(dst, voff, base, mod) asm volatile("global_load_dwordx4 %0, %1, %2 " mod : "+v"(dst) : "v"(voff), "s"(base) : "memory")
#define GLD2(dst, voff, base, mod) asm volatile("global_load_dwordx2 %0, %1, %2 " mod : "+v"(dst) : "v"(voff), "s"(base) : "memory")
#define GLD1(dst, voff, base) asm volatile("global_load_dword %0, %1, %2" : "+v"(dst) : "v"(voff), "s"(base) : "memory")
#define GST4(voff, val, base, mod) asm volatile("global_store_dwordx4 %0, %1, %2 " mod "\n\ts_nop 1" :: "v"(voff), "v"(val), "s"(base) : "memory")
#define GST2(voff, val, base, mod) asm volatile("global_store_dwordx2 %0, %1, %2 " mod :: "v"(voff), "v"(val), "s"(base) : "memory")
#define RLD2(dst, voff, base) asm volatile("global_load_dwordx2 %0, %1, %2 sc1\n\ts_waitcnt vmcnt(0)" : "=&v"(dst) : "v"(voff), "s"(base) : "memory")
#define RLD4(dst, voff, base) asm volatile("global_load_dwordx4 %0, %1, %2 sc1\n\ts_waitcnt vmcnt(0)" : "=&v"(dst) : "v"(voff), "s"(base) : "memory")
#define PIN(x) asm volatile("" : "+v"(x))
#define UNDEF(x) asm volatile("" : "=v"(x))

DI int cidx(int l) {
    int s = (l >= 256) + (l >= 512) + (l >= 1024);
    int r = (l + ((1 << s) >> 1) - (s > 0) + ((l >> s) & (s > 0))) >> s;
    return l < 0 ? 641 : 128 * s + r;
}

template <int TS>
DI void conv_pair(f4 (&acc)[4], int k0, u32 ub, u32 la, u32 q2, u32 p3, const u4& sel2, const u4& sel3) {
    constexpr int T[2][4] = {{0, 3, 4, 7}, {1, 2, 5, 6}};
    constexpr int NT = T[TS][3] + 1;
#pragma unroll 1
    for (int kk = 0; kk < 2; ++kk, ++k0) {
        u4 A[NT], B[NT];
        u32 u = ub - 64 * k0, l = la + 608 - 64 * k0, q = q2 + 64 * k0, p = p3 + 16 * k0;
        PIN(u); PIN(l); PIN(q); PIN(p);
        auto lin = [&](u32 a) { u4 r = {LDS(u32, a), LDS(u32, a + 4), LDS(u32, a + 8), LDS(u32, a + 12)}; return r; };
        auto seg2 = [&](u2 v) { u4 r;
            r.x = __builtin_amdgcn_perm(v.y, v.x, sel2.x); r.y = __builtin_amdgcn_perm(v.y, v.x, sel2.y);
            r.z = __builtin_amdgcn_perm(v.y, v.x, sel2.z); r.w = __builtin_amdgcn_perm(v.y, v.x, sel2.w); return r; };
        auto seg3 = [&](u32 v) { u4 r;
            r.x = __builtin_amdgcn_perm(v, v, sel3.x); r.y = __builtin_amdgcn_perm(v, v, sel3.y);
            r.z = __builtin_amdgcn_perm(v, v, sel3.z); r.w = __builtin_amdgcn_perm(v, v, sel3.w); return r; };
        A[0] = lin(l + 512);
#pragma unroll
        for (int i = 0; i < 4; ++i) B[T[TS][i]] = LDS(u4, u + 512 * T[TS][i]);
        A[1] = lin(l);
#pragma unroll
        for (int s = 0; s < NT; ++s) if (s != T[TS][0] && s != T[TS][1] && s != T[TS][2] && s != T[TS][3]) B[s] = LDS(u4, u + 512 * s);
        const u2 v2 = LDS(u2, q), v3 = LDS(u2, q + 512);
        u32 v4[4];
#pragma unroll
        for (int d = 4; d < NT; ++d) v4[d - 4] = LDS(u32, p + 128 * (d - 4));
        SB();
        A[2] = seg2(v2); A[3] = seg2(v3);
#pragma unroll
        for (int d = 4; d < NT; ++d) A[d] = seg3(v4[d - 4]);
        if (k0 == 0) { A[2] = lin(la + 96); A[4] = lin(la); }
#pragma unroll
        for (int d = 0; d < NT; ++d)
#pragma unroll
            for (int i = 0; i < 4; ++i)
                if (T[TS][i] >= d) acc[i] = mfma(A[d], B[T[TS][i] - d], acc[i]);
    }
}

__global__ __launch_bounds__(512, 2) void s4ffn_kernel(const u16* __restrict__ x, const u16* __restrict__ Are, const u16* __restrict__ Aim,
        const u16* __restrict__ Bre, const u16* __restrict__ Bim, const u16* __restrict__ Cre, const u16* __restrict__ Cim,
        const u16* __restrict__ logdt, const u16* __restrict__ Dsk, const u16* __restrict__ w1, const u16* __restrict__ w2,
        u16* __restrict__ out, char* __restrict__ ws) {
    const int tid = threadIdx.x, lane = tid & 63, wave = __builtin_amdgcn_readfirstlane(tid >> 6);
    const u32 b = blockIdx.x, xcd = b & 7, idx = b >> 3;
    const int fl = lane & 15, g = lane >> 4, l31 = lane & 31, hh = lane >> 5;
    u32 pfd = 0;
    STAMP_DECL;
    {
        u64 pc; asm volatile("s_getpc_b64 %0" : "=s"(pc));
        pc &= ~127ull;
        if (tid < 5) asm volatile("global_load_dword %0, %1, %2" : "+v"(pfd) : "v"((idx * 5 + tid) * 128), "s"(pc) : "memory");
    }
    STAMP(0); STAMPC(22);
    asm volatile("" :: "s"(x), "s"(Are), "s"(Aim), "s"(Bre), "s"(Bim), "s"(Cre), "s"(Cim), "s"(logdt), "s"(Dsk), "s"(w1), "s"(w2), "s"(out), "s"(ws));
    u32 tag;
    if (idx >= 28) {
        asm volatile("s_load_dword %0, %1, 0x0" : "=s"(tag) : "s"(ws));
        const u32 lb = (idx - 28) * 8 + xcd;
        u4 xr[8];
#pragma unroll
        for (int r = 0; r < 8; ++r) xr[r] = u4{0, 0, 0, 0};
        if (lane < 56) {
            const char* xp = (const char*)x + (lb * 64 + wave * 8) * 896;
            GLD4(xr[0], lane * 16, xp, ""); GLD4(xr[1], lane * 16, xp, "offset:896"); GLD4(xr[2], lane * 16, xp, "offset:1792"); GLD4(xr[3], lane * 16, xp, "offset:2688");
            GLD4(xr[4], lane * 16, xp, "offset:3584"); GLD4(xr[5], lane * 16, xp + 4480, ""); GLD4(xr[6], lane * 16, xp + 4480, "offset:896"); GLD4(xr[7], lane * 16, xp + 4480, "offset:1792");
        }
        auto prefetch = [&](u32 k) {
            const u32 n = ((k * 4 + idx - 28) * 8 + wave) * 64 + lane, m = n - 7168;
            const u32 o = n < 7168 ? ((n / 896 + 4) & 7) * 200704 + n % 896 * 128 : m / 672 * 200704 + 114688 + m % 672 * 128;
            asm volatile("global_load_dword %0, %1, %2" : "+v"(pfd) : "v"(min(o, 1605504u)), "s"(w2) : "memory");
        };
        prefetch(0);
        asm volatile("s_waitcnt vmcnt(1) lgkmcnt(0)" : "+v"(xr[0]), "+v"(xr[1]), "+v"(xr[2]), "+v"(xr[3]), "+v"(xr[4]), "+v"(xr[5]), "+v"(xr[6]), "+v"(xr[7]), "+s"(tag) :: "memory");
        tag += 1;
        prefetch(1);
        float sm[8], sq[8];
#pragma unroll
        for (int r = 0; r < 8; ++r) {
            sm[r] = 0; sq[r] = 0;
#pragma unroll
            for (int i = 0; i < 4; ++i) { sm[r] = dot2(xr[r][i], 0x3f803f80u, sm[r]); sq[r] = dot2(xr[r][i], xr[r][i], sq[r]); }
        }
        float a_[2], c_[2]; u32 st_[2];
#pragma unroll
        for (int j = 0; j < 2; ++j) {
            const float mean = rsum(sw16(sw32(sm[j], sm[j + 4]), sw32(sm[j + 2], sm[j + 6]))) * (1.f / 448.f);
            const float var = fmaxf(rsum(sw16(sw32(sq[j], sq[j + 4]), sw32(sq[j + 2], sq[j + 6]))) * (1.f / 448.f) - mean * mean, 0.f);
            const float mb = rbf(mean), sd_ = rbf(__builtin_sqrtf(rbf(rbf(var) + 1e-5f)));
            a_[j] = 1.f / sd_; c_[j] = -mb * a_[j];
            st_[j] = (__float_as_uint(mb) >> 16) | __float_as_uint(sd_);
        }
        if (fl < 2) { const u2 gr = {fl ? st_[1] : st_[0], tag}; GST2((fl + 2 * g) * 8, gr, ws + WS_STAT + (lb * 64 + wave * 8) * 8, "sc1"); }
#pragma unroll
        for (int r = 0; r < 8; ++r) {
            const float av = __builtin_bit_cast(float, __builtin_amdgcn_readlane(__builtin_bit_cast(int, a_[r & 1]), 16 * (r >> 1)));
            const float cv = __builtin_bit_cast(float, __builtin_amdgcn_readlane(__builtin_bit_cast(int, c_[r & 1]), 16 * (r >> 1)));
            u4 o;
#pragma unroll
            for (int i = 0; i < 4; ++i) o[i] = pkbf(fmaf(bflo(xr[r][i]), av, cv), fmaf(bfhi(xr[r][i]), av, cv));
            if (lane < 56) LDS(u4, (wave * 8 + r) * 928 + lane * 16) = o;
        }
        BARRIER();
#pragma unroll 2
        for (u32 i = 0; i < 14; ++i) {
            const u32 n = wave * 14 + i;
            const u2 v = trread((16 * (n & 3) + 4 * g + (fl >> 2)) * 928 + (n >> 2) * 32 + 8 * (lane & 3));
            const u4 gr = {v.x, tag, v.y, tag};
            GST4(lane * 16, gr, ws + WS_XG + lb * 114688 + n * 1024, "sc1");
        }
        if (tid == 0) { const u2 ar = {tag, tag}; GST2(0, ar, ws + WS_AR + b * 8, "sc1"); }
#pragma unroll 1
        for (u32 k = 2; k < 7; ++k) prefetch(k);
        if (lb == 0 && wave == 0) {
            u4 t0, t1;
#pragma unroll 1
            for (int sp = 0; sp < SPIN_MAX; ++sp) {
                RLD4(t0, lane * 16, ws + WS_AR); RLD4(t1, lane * 16, ws + WS_AR + 1024);
                if (!__any(((t0.x ^ tag) | (t0.z ^ tag) | (t1.x ^ tag) | (t1.z ^ tag)) != 0)) break;
            }
            if (lane == 0) { const u2 e = {tag, tag}; GST2(0, e, ws + WS_EPOCH, "sc1"); }
        }
        asm volatile("s_waitcnt vmcnt(0)" :: "v"(pfd) : "memory");
        return;
    }
    const bool isF = wave < 4;
    const u32 q7 = idx / 7, r7 = idx - 7 * q7;
    const u32 rp = 4 * (xcd >> 1) + q7, cc = 7 * (xcd & 1) + r7, P = 4 * xcd + q7, ct = r7, dp = 28 * xcd + idx;
    const u32 w4 = wave & 3, rg = w4 >> 1, ch = wave & 1, dsel = wave >> 2;
    u32 kt = (r7 + 2 * q7) % 7;
    const int r8 = lane >> 3, p8 = lane & 7, rw = (lane >> 2) & 7;
    u32 vo0 = (32 * w4 + r8) * 896 + 16 * (p8 ^ (r8 >> 1)), vo1 = (vo0 + 8 * 896) ^ 64, vo2 = vo0 + 16 * 896, vo3 = vo1 + 16 * 896;
    u32 vo4 = (16 * w4 + rw) * 3584 + 64 * hh + 16 * ((lane & 3) ^ (rw >> 2)), vo5 = vo4 + 128, vo6 = (vo4 + 8 * 3584) ^ 32, vo7 = vo6 + 128;
    const char *xb = (const char*)x + rp * 114688, *wb = (const char*)w1 + cc * 256;
    const char *pa, *pw;
#define RING_SET() do { asm volatile("" : "+s"(kt)); pa = xb + kt * 128; pw = wb + kt * 229376; kt = kt == 6 ? 0 : kt + 1; } while (0)
#define DMA_I(i, l_) do { if ((i) == 0) dma16(vo0, pa, l_); if ((i) == 1) dma16(vo1, pa, l_ + 1024); \
        if ((i) == 2) dma16(vo2, pa, l_ + 2048); if ((i) == 3) dma16(vo3, pa, l_ + 3072); \
        if ((i) == 4) dma16(vo4, pw, l_ + 16384); if ((i) == 5) dma16(vo5, pw, l_ + 17408); \
        if ((i) == 6) dma16(vo6, pw, l_ + 18432); if ((i) == 7) dma16(vo7, pw, l_ + 19456); } while (0)
#define DMA_ALL(l_) do { DMA_I(0, l_); DMA_I(1, l_); DMA_I(2, l_); DMA_I(3, l_); DMA_I(4, l_); DMA_I(5, l_); DMA_I(6, l_); DMA_I(7, l_); } while (0)
    const u32 lw = L_RING + w4 * 4096;
    u4 sel2, sel3; u32 cv_ub, cv_la, cv_q2, cv_p3, dsk2, hm0, hm1, hm2, hm3;
    auto setup = [&]() {
        const u32 tb = L_TAB + dsel * 4096;
        const int x2 = fl & 7, x3 = (fl + 8 * g) & 15;
        u32 w2_ = (u32)(0x001112222233344ull >> (28 - 4 * x2));
        u32 w3_ = (u32)(0x00000055556aaaffull >> (30 - 2 * x3)) & 0xffff;
        const int m2 = w2_ >> 28, m3 = w3_ >> 14;
        w2_ -= m2 * 0x11111111u; w3_ -= m3 * 0x5555u;
#pragma unroll
        for (int q = 0; q < 4; ++q) {
            sel2[q] = ((w2_ >> (8 * q) & 15) | (w2_ >> (8 * q + 4) & 15) << 16) * 0x0202 + 0x01000100;
            sel3[q] = ((w3_ >> (4 * q) & 3) | (w3_ >> (4 * q + 2) & 3) << 16) * 0x0202 + 0x01000100;
        }
        cv_ub = L_U + dsel * 4608 + 2 * (224 + 16 * fl + 8 * g);
        cv_la = tb + 4 * ((fl & 1) * 304 + ((16 - fl + 8 * g) >> 1));
        cv_q2 = tb + 2432 + 8 * (2 * (2 + (fl >> 3) - g) + m2 - 6);
        cv_p3 = tb + 3456 + 4 * (2 * (1 + ((fl - 8 * g) >> 4)) + m3 - 2);
        PIN(sel2); PIN(sel3); PIN(cv_ub); PIN(cv_la); PIN(cv_q2); PIN(cv_p3);
    };
#define SCALARS_ISSUE() do { asm volatile("s_load_dword %0, %1, 0x0" : "=s"(tag) : "s"(ws)); asm volatile("s_load_dword %0, %1, 0x0" : "=s"(dsk2) : "s"((const char*)Dsk + dp * 4)); } while (0)
#define SCALARS_DONE() do { tag += 1; const u32 pat = 1 + tag % 254; hm0 = (pat & 1) << 15 | (pat & 2) << 30; hm1 = (pat & 4) << 13 | (pat & 8) << 28; \
        hm2 = (pat & 16) << 11 | (pat & 32) << 26; hm3 = (pat & 64) << 9 | (pat & 128) << 24; } while (0)
    const char* hb = ws + WS_H + ((2 * rp + rg) * 112 + 8 * cc + 4 * ch) * 2048;
    const u32 vw2 = r8 * 896 + 16 * (p8 ^ (4 * ((r8 >> 1) & 1)));
    const char* w2b = (const char*)w2 + wave * (224 * 896) + ct * 128;
    if (isF) {
        SCALARS_ISSUE();
        __builtin_amdgcn_s_setprio(2); RING_SET(); DMA_ALL(lw); __builtin_amdgcn_s_setprio(1); RING_SET(); DMA_ALL(lw + 32768);
        __builtin_amdgcn_s_setprio(0); RING_SET(); DMA_ALL(lw + 65536);
        setup();
        asm volatile("s_waitcnt lgkmcnt(0)" : "+s"(tag), "+s"(dsk2) :: "memory");
        SCALARS_DONE();
        STAMP(1);
        f16v acc[2][2]; f4 cs = {0, 0, 0, 0};
#pragma unroll
        for (int i = 0; i < 16; ++i) { acc[0][0][i] = 0; acc[0][1][i] = 0; acc[1][0][i] = 0; acc[1][1][i] = 0; }
        const u32 o1 = ((g ^ lane) & 1) ? 0 : 0x3f803f80u; const u4 ones = {o1, o1, o1, o1};
        u32 ax0 = L_RING + (64 * rg + l31) * 128 + 16 * (hh ^ ((l31 >> 1) & 7)), ax1 = ax0 ^ 32, ax2 = ax0 ^ 64, ax3 = ax0 ^ 96;
        u32 aw0 = L_RING + 16384 + 2048 * hh + 1024 * ch + 512 * rg + 64 * (fl >> 2) + 16 * ((2 * (g & 1) + ((lane & 3) >> 1)) ^ (2 * hh)) + 8 * (lane & 1);
        u32 aw1 = aw0 ^ 272, aw2 = aw0 ^ 512, aw3 = aw0 ^ 784;
        u4 X0[4], W0[4], X1[4], W1[4];
#define G1_RD(i, X, W, a0, a1, o) do { if ((i) == 0) { X[0] = LDS(u4, a0); W[0] = trread2(aw0 + (o), aw1 + (o)); } \
        if ((i) == 1) { X[1] = LDS(u4, a0 + 4096); W[1] = trread2(aw2 + (o), aw3 + (o)); } \
        if ((i) == 2) { X[2] = LDS(u4, a1); W[2] = trread2(aw0 + (o) + 4096, aw1 + (o) + 4096); } \
        if ((i) == 3) { X[3] = LDS(u4, a1 + 4096); W[3] = trread2(aw2 + (o) + 4096, aw3 + (o) + 4096); } } while (0)
#define G1_PHASE(X, W, RD, D0) do { \
        acc[0][0] = mfma32(W[0], X[0], acc[0][0]); RD(0); SB(); \
        acc[0][1] = mfma32(W[0], X[1], acc[0][1]); RD(1); SB(); \
        acc[1][0] = mfma32(W[1], X[0], acc[1][0]); DMA_I(D0, ld); SB(); \
        acc[1][1] = mfma32(W[1], X[1], acc[1][1]); RD(2); SB(); \
        cs = mfma(W[0], ones, cs); DMA_I(D0 + 1, ld); SB(); \
        acc[0][0] = mfma32(W[2], X[2], acc[0][0]); RD(3); SB(); \
        acc[0][1] = mfma32(W[2], X[3], acc[0][1]); DMA_I(D0 + 2, ld); SB(); \
        acc[1][0] = mfma32(W[3], X[2], acc[1][0]); SB(); \
        acc[1][1] = mfma32(W[3], X[3], acc[1][1]); DMA_I(D0 + 3, ld); SB(); \
        cs = mfma(W[2], ones, cs); SB(); } while (0)
#define RDA(i) G1_RD(i, X1, W1, ax2, ax3, 8192)
#define RDB(i) G1_RD(i, X0, W0, ax0, ax1, 0)
        asm volatile("s_waitcnt vmcnt(16)" : "+v"(pfd) :: "memory");
        BARRIER();
        RDB(0); RDB(1); RDB(2); RDB(3);
        STAMP(3);
#pragma unroll 1
        for (u32 s = 0; s < 7; ++s) {
            RING_SET();
            if (s >= 4) pa = pw = w2b + (s < 6 ? 4 * 224 * 896 + (s - 4) * 57344 : 0);
            if (s == 4) { vo0 = vw2; vo1 = vw2 + 7168; vo2 = vw2 + 14336; vo3 = vw2 + 21504; vo4 = vw2 + 28672; vo5 = vw2 + 35840; vo6 = vw2 + 43008; vo7 = vw2 + 50176; }
            const u32 ld = lw + ((s + 3) & 3) * 32768;
            SB();
            G1_PHASE(X0, W0, RDA, 0);
            { const u32 m_ = (0x8000u * (((s & 3) + 1) ^ (((s + 1) & 3) + 1)));
              ax0 ^= m_; ax1 ^= m_; ax2 ^= m_; ax3 ^= m_; aw0 ^= m_; aw1 ^= m_; aw2 ^= m_; aw3 ^= m_; }
            if (s < 6) { ACC_BEGIN(); WAITV(12); ACC_END(20); BARRIER(); ACC_END(21); }
            SB();
            G1_PHASE(X1, W1, RDB, 4);
        }
        if (fl < 2) LDS(f4, L_CS + (64 * ch + 32 * rg + 16 * fl + 4 * g) * 4) = cs;
        STAMP(4);
        BARRIER();
        pa = pw = w2b + 57344;
        { const u32 ld = lw + 65536; DMA_ALL(ld); }
#pragma unroll
        for (int tt = 0; tt < 2; ++tt) {
            const u32 sv = LDS(u32, L_ST + (64 * rg + 32 * tt + l31) * 4);
            const float m = -bflo(sv), rs = __builtin_amdgcn_rcpf(bfhi(sv));
            const f2 m2 = {m, m}, rs2 = {rs, rs};
#pragma unroll
            for (int c = 0; c < 2; ++c)
#pragma unroll
                for (int k = 0; k < 2; ++k) {
                    u4 o;
#pragma unroll
                    for (int q = 0; q < 2; ++q) {
                        const f4 cv = LDS(f4, L_CS + ((64 * ch + 32 * rg + 4 * hh) * 4 ^ (128 * c)) + (2 * k + q) * 32), av = quad(acc[c][tt], 2 * k + q);
                        const f2 v0 = __builtin_elementwise_fma(m2, f2{cv.x, cv.y}, f2{av.x, av.y}) * rs2, v1 = __builtin_elementwise_fma(m2, f2{cv.z, cv.w}, f2{av.z, av.w}) * rs2;
                        o[2 * q] = relu2(pkbf(v0.x, v0.y)) | (q ? hm2 : hm0); o[2 * q + 1] = relu2(pkbf(v1.x, v1.y)) | (q ? hm3 : hm1);
                    }
                    const char* hp = hb + (2 * (c ^ rg) + k) * 2048;
                    if (tt) GST4(lane * 16, o, hp, "offset:1024 sc1"); else GST4(lane * 16, o, hp, "sc1");
                }
        }
        STAMP(5);
        WAITV(24);
    } else {
        __builtin_amdgcn_s_setprio(3);
        u32 pq0, pq1, pq2, pq3, pq4, pq5, ldt2;
        UNDEF(pq0); UNDEF(pq1); UNDEF(pq2); UNDEF(pq3); UNDEF(pq4); UNDEF(pq5);
        { const u32 po = (2 * dp + rg) * 512 + ch * 256 + lane * 4;
          GLD1(pq0, po, Are); GLD1(pq1, po, Aim); GLD1(pq2, po, Bre); GLD1(pq3, po, Bim); GLD1(pq4, po, Cre); GLD1(pq5, po, Cim); }
        SCALARS_ISSUE();
        asm volatile("s_load_dword %0, %1, 0x0" : "=s"(ldt2) : "s"((const char*)logdt + dp * 4));
        __builtin_amdgcn_s_setprio(1);
        setup();
        u32 tix[8];
        {
            const int t = tid & 127;
#pragma unroll
            for (int i = 0; i < 5; ++i) {
                const int u = min(t + 128 * i, 607), pi = u >= 304, ip = u - 304 * pi, top = (ip < 24 ? 1056 : 592) - pi - 2 * ip;
                tix[i] = cidx(top) * 2 | cidx(top - 1) << 17;
            }
            tix[5] = (513 + min(t, 126)) * 0x20002 + 0x20000;
            tix[6] = (388 + (t >> 1) + 2 * (t & 1)) * 0x20002 + 0x20000;
            tix[7] = tix[6] + 64 * 0x20002;
#pragma unroll
            for (int i = 0; i < 8; ++i) PIN(tix[i]);
        }
        asm volatile("s_waitcnt lgkmcnt(0)" : "+s"(tag), "+s"(dsk2), "+s"(ldt2) :: "memory");
        SCALARS_DONE();
        STAMP(1);
        const float dt = rbf(expf(rg ? bfhi(ldt2) : bflo(ldt2)));
        u32 sr = L_SCR + w4 * 4352 + fl * 272 + g * 16; PIN(sr);
        asm volatile("s_mov_b32 m0, %0" :: "s"(L_SCR + w4 * 4352));
        asm volatile("s_waitcnt vmcnt(0)" : "+v"(pq0), "+v"(pq1), "+v"(pq2), "+v"(pq3), "+v"(pq4), "+v"(pq5) :: "memory");
        if (tid == 256) { const u2 ar = {tag, tag}; GST2(0, ar, ws + WS_AR + b * 8, "sc1"); }
        STAMP(2);
        f4 dk[4];
#pragma unroll
        for (int t = 0; t < 4; ++t) dk[t] = f4{0, 0, 0, 0};
        u2 stv; u4 xg0, xg1, xg2, xg3;
        const u32 xo = ((tid & 255) >> 5) * 114688 + ((tid >> 1) & 15) * 256 + (tid & 1) * 16;
        const char* xgb = ws + WS_XG + (dp >> 3) * 4096 + (dp & 7) * 32;
#define SBAR() do { SB(); asm volatile("s_barrier" ::: "memory"); } while (0)
#pragma unroll
        for (int ps = 0; ps < 2; ++ps) {
#define PAR(v) (ps ? bfhi(v) : bflo(v))
            const float a2 = fmul1(dt, PAR(pq0)) * 1.44269504089f, rev = fmul1(dt, PAR(pq1)) * 0.15915494309f;
            const f2 bb = {fmul1(dt, PAR(pq2)), fmul1(dt, PAR(pq3))}, cv = {PAR(pq4), -PAR(pq5)};
            const f2 c = cmulc(cv, bb);
            const f2 z1 = zpow(a2, rev, 1.f), z8 = zpow(a2, rev, 8.f), z64 = zpow(a2, rev, 64.f), z256 = zpow(a2, rev, 256.f);
            const f2 z2 = cmul(z1, z1), z4 = cmul(z2, z2);
            const f2 z32 = zpow(a2, rev, 32.f), z128 = cmul(z64, z64);
            f2 e1 = cmulc(c, z256), e2 = cmulc(e1, z256), e3 = cmulc(e2, z256);
#define PUT(e, v) do { const u32 p_ = pkbf((v).x, (v).y); asm volatile("ds_write_addtid_b32 %0 offset:%1" :: "v"(p_), "i"((e) * 272) : "memory"); } while (0)
            f2 t = c, t4 = cmulc(c, z4), t8 = e1; PUT(0, t); PUT(4, t4); PUT(8, t8);
            t = cmulc(t, z1); t4 = cmulc(t4, z1); t8 = cmulc(t8, z2); PUT(1, t); PUT(5, t4); PUT(9, t8);
            if (ps == 1) { STAMP(17); SBAR(); }
            t = cmulc(t, z1); t4 = cmulc(t4, z1); t8 = cmulc(t8, z2); PUT(2, t); PUT(6, t4); PUT(10, t8);
            t = cmulc(t, z1); t4 = cmulc(t4, z1); t8 = cmulc(t8, z2); PUT(3, t); PUT(7, t4); PUT(11, t8);
            t = cmulc(e2, z4); t4 = cmulc(e3, z4); PUT(12, e2); PUT(14, e3); e3 = cmulc(e3, z256); PUT(13, t); PUT(15, t4);
            u4 B0[4], B1[4];
#pragma unroll
            for (int s = 0; s < 4; ++s) B0[s] = LDS(u4, sr + s * 64);
            const f2 z512 = cmul(z256, z256);
            e2 = cmulc(e3, z256); PUT(0, e3); PUT(1, e2); e3 = cmulc(e3, z512); e2 = cmulc(e2, z512); PUT(2, e3); PUT(3, e2); e3 = cmulc(e3, z512); PUT(4, e3);
#pragma unroll
            for (int s = 0; s < 4; ++s) B1[s] = LDS(u4, sr + s * 64);
            if (ps == 0) STAMP(3); else STAMP(18);
            SBAR();
            f2 r0 = {1.f, 0.f}, r1 = z32, r2 = z64, r3 = cmul(z64, z32);
#pragma unroll
            for (int h = 0; h < 2; ++h) {
                f2 t0 = r0, t1 = r1, t2 = r2, t3 = r3;
                PUT(0, t0); PUT(4, t1); PUT(8, t2); PUT(12, t3);
#define STEP(j) t0 = cmul(t0, z8); t1 = cmul(t1, z8); t2 = cmul(t2, z8); t3 = cmul(t3, z8); PUT(j, t0); PUT(4 + j, t1); PUT(8 + j, t2); PUT(12 + j, t3);
                STEP(1) STEP(2) STEP(3)
                u4 A[4];
#pragma unroll
                for (int s = 0; s < 4; ++s) A[s] = LDS(u4, sr + s * 64);
                asm volatile("" ::: "memory");
                if (h == 0) { r0 = z128; r1 = cmul(r1, z128); r2 = cmul(r2, z128); r3 = cmul(r3, z128); }
#pragma unroll
                for (int s = 0; s < 4; ++s) { dk[2 * h] = mfma(A[s], B0[s], dk[2 * h]); dk[2 * h + 1] = mfma(A[s], B1[s], dk[2 * h + 1]); }
                if (ps == 1 && h == 0) {
                    UNDEF(stv); UNDEF(xg0); UNDEF(xg1); UNDEF(xg2); UNDEF(xg3);
                    GLD2(stv, (tid & 127) * 8, ws + WS_STAT + rp * 1024, "sc1");
                    GLD4(xg0, xo, xgb, "sc1"); GLD4(xg1, xo, xgb + 8 * 114688, "sc1"); GLD4(xg2, xo, xgb + 16 * 114688, "sc1"); GLD4(xg3, xo, xgb + 24 * 114688, "sc1");
                }
                if (ps == 0) STAMP(15 + h); else if (h == 0) STAMP(19);
                if (ps == 1 && h == 1) {
                    const u32 sw = L_SCR + w4 * 4352 + lane * 16;
                    LDS(f4, sw) = self4(ch, dk[0], dk[2]); LDS(f4, sw + 1024) = self4(ch, dk[1], dk[3]);
                    SB(); BARRIER();
                } else SBAR();
            }
        }
        STAMP(12);
        {
            const u32 a = L_SCR + (w4 ^ 1) * 4352 + lane * 16;
#pragma unroll
            for (int jt = 0; jt < 2; ++jt) {
                const f4 s = self4(ch, dk[2 + jt], dk[jt]) + LDS(f4, a + jt * 1024);
                const int j = jt * 16 + fl, i0 = ch * 16 + 4 * g;
                const int mul = jt ? 1 : j < 8 ? 8 : j < 12 ? 4 : 2;
                const int base = jt ? 512 + 32 * fl : j < 8 ? j : j < 12 ? 248 + j : 384 + (j & 1) + 64 * ((j - 12) >> 1);
                const u32 o = L_KC + rg * 1536 + (base + mul * i0) * 2;
                const u32 p01 = pkbf(s.x, s.y), p23 = pkbf(s.z, s.w);
                if (jt == 0) { LDS(u16, o) = (u16)p01; LDS(u16, o + mul * 2) = (u16)(p01 >> 16); LDS(u16, o + mul * 4) = (u16)p23; LDS(u16, o + mul * 6) = (u16)(p23 >> 16); }
                else if (fl < 4) LDS(u2, o) = u2{p01, p23};
                else if (fl == 4 && i0 == 0) LDS(u32, o) = p01 & 0xffff;
            }
            asm volatile("s_waitcnt vmcnt(4)" : "+v"(stv) :: "memory");
#pragma unroll 1
            for (int sp = 0; __any(stv.y != tag) && sp < SPIN_MAX; ++sp) RLD2(stv, (tid & 127) * 8, ws + WS_STAT + rp * 1024);
            if (w4 < 2) LDS(u32, L_ST + (tid & 127) * 4) = stv.x;
        }
        STAMP(4);
        BARRIER();
        {
            u32 kc = L_KC + rg * 1536, td = L_TAB + rg * 4096 + (tid & 127) * 4; PIN(td);
            u32 v[8];
#pragma unroll
            for (int i = 0; i < 8; ++i) v[i] = (u32)LDS(u16, kc + (tix[i] & 0xffff)) | ((u32)LDS(u16, kc + (tix[i] >> 16)) << 16);
#pragma unroll
            for (int i = 0; i < 4; ++i) LDS(u32, td + 512 * i) = v[i];
            if ((tid & 127) < 96) LDS(u32, td + 2048) = v[4];
            LDS(u32, td + 3456) = v[5]; LDS(u32, td + 2432) = v[6]; LDS(u32, td + 2944) = v[7];
        }
        {
            asm volatile("s_waitcnt vmcnt(0)" : "+v"(xg0), "+v"(xg1), "+v"(xg2), "+v"(xg3) :: "memory");
#define XG_FIX(v, i) _Pragma("unroll 1") for (int sp = 0; __any(v.y != tag || v.w != tag) && sp < SPIN_MAX; ++sp) RLD4(v, xo, xgb + (i) * 8 * 114688)
            if (__any(((xg0.y ^ tag) | (xg0.w ^ tag) | (xg1.y ^ tag) | (xg1.w ^ tag) | (xg2.y ^ tag) | (xg2.w ^ tag) | (xg3.y ^ tag) | (xg3.w ^ tag)) != 0)) {
                XG_FIX(xg0, 0); XG_FIX(xg1, 1); XG_FIX(xg2, 2); XG_FIX(xg3, 3);
            }
            u32 a = L_U + 480 + (tid & 1) * 4608 + ((tid & 255) >> 1) * 8; PIN(a);
            LDS(u2, a) = u2{xg0.x, xg0.z}; LDS(u2, a + 1024) = u2{xg1.x, xg1.z}; LDS(u2, a + 2048) = u2{xg2.x, xg2.z}; LDS(u2, a + 3072) = u2{xg3.x, xg3.z};
            if ((tid & 255) < 120) LDS(u2, a - 480) = u2{0, 0};
        }
        __builtin_amdgcn_s_setprio(0);
        STAMP(5);
    }
    BARRIER();
    STAMP(6);
    const u32 pb0 = lw + (isF ? 32768 : 98304), pb1 = lw + (isF ? 65536 : 0);
    u4 Bw[14][2];
    u32 aw2a = pb0 + (4 * hh + (fl >> 2)) * 128 + 16 * ((2 * (g & 1) + ((lane & 3) >> 1)) ^ (4 * ((fl >> 3) & 1))) + 8 * (lane & 1);
    u32 aw2b = aw2a ^ 64, aw2c = aw2a - pb0 + pb1, aw2d = aw2c ^ 64;
#define W2_RD(j, p, n) do { const u32 o_ = ((n) >> 1) * 16384 + ((n) & 1) * 2048; \
        Bw[j][0] = trread2(((p) ? aw2c : aw2a) + o_, ((p) ? aw2c : aw2a) + o_ + 1024); Bw[j][1] = trread2(((p) ? aw2d : aw2b) + o_, ((p) ? aw2d : aw2b) + o_ + 1024); } while (0)
    f4 cacc[4];
    u4 hA0, hA1, hA2, hA3, hA4, hA5, hA6, hB0, hB1, hB2, hB3, hB4, hB5, hB6;
    const char* hrd = ws + WS_H + (P * 112 + wave * 14) * 2048;
#define H_LOAD(h, n) do { const char* p_ = hrd + ((n) & 1) * 14336 + ((n) >> 1) * 1024; GLD4(h##0, lane * 16, p_, "sc1"); GLD4(h##1, lane * 16, p_, "offset:2048 sc1"); \
        GLD4(h##2, lane * 16, p_ + 4096, "sc1"); GLD4(h##3, lane * 16, p_ + 4096, "offset:2048 sc1"); GLD4(h##4, lane * 16, p_ + 8192, "sc1"); \
        GLD4(h##5, lane * 16, p_ + 8192, "offset:2048 sc1"); GLD4(h##6, lane * 16, p_ + 12288, "sc1"); } while (0)
#define H_UNDEF(h) do { UNDEF(h##0); UNDEF(h##1); UNDEF(h##2); UNDEF(h##3); UNDEF(h##4); UNDEF(h##5); UNDEF(h##6); } while (0)
    {
        const int kh = (wave >> 1) & 1, ts = wave & 1;
#pragma unroll
        for (int i = 0; i < 4; ++i) cacc[i] = f4{0, 0, 0, 0};
        if (ts) conv_pair<1>(cacc, 4 * kh, cv_ub, cv_la, cv_q2, cv_p3, sel2, sel3); else conv_pair<0>(cacc, 4 * kh, cv_ub, cv_la, cv_q2, cv_p3, sel2, sel3);
        STAMP(13);
        WAITV(8);
        W2_RD(0, 0, 0); W2_RD(1, 0, 1); W2_RD(2, 0, 2); W2_RD(3, 0, 3); W2_RD(4, 1, 0); W2_RD(5, 1, 1); W2_RD(6, 1, 2); W2_RD(7, 1, 3);
        asm volatile("s_waitcnt lgkmcnt(0)" ::: "memory");
#pragma unroll 1
        for (u32 i = 0; i < 12; ++i) dma16(vw2, w2b + (16 + i) * 7168, (i < 8 ? pb0 : pb1) + ((i >> 2) & 1) * 16384 + (i & 3) * 1024);
        STAMP(14);
        if (ts) conv_pair<1>(cacc, 4 * kh + 2, cv_ub, cv_la, cv_q2, cv_p3, sel2, sel3); else conv_pair<0>(cacc, 4 * kh + 2, cv_ub, cv_la, cv_q2, cv_p3, sel2, sel3);
        STAMP(7);
        H_UNDEF(hA); H_UNDEF(hB);
        H_LOAD(hA, 0); H_LOAD(hB, 1);
        BARRIER();
        u32 ra = wave * 2048 + lane * 16; PIN(ra);
        LDS(f4, ra) = self4(kh, cacc[0], cacc[2]);
        LDS(f4, ra + 1024) = self4(kh, cacc[1], cacc[3]);
        ra ^= 4096;
        BARRIER();
        const float dk = dsel ? bfhi(dsk2) : bflo(dsk2);
#pragma unroll
        for (int i = 0; i < 2; ++i) {
            const int tau = kh ? (ts ? 5 + i : 4 + 3 * i) : (ts ? 1 + i : 3 * i);
            const f4 y = self4(kh, cacc[2 + i], cacc[i]) + LDS(f4, ra + i * 1024);
            const u2 uu = LDS(u2, L_U + dsel * 4608 + 480 + (256 * tau + 16 * fl + 4 * g) * 2);
            const u32 y01 = pkbf(y.x, y.y), y23 = pkbf(y.z, y.w);
            const u32 s01 = pkbf(bflo(uu.x) * dk, bfhi(uu.x) * dk), s23 = pkbf(bflo(uu.y) * dk, bfhi(uu.y) * dk);
            const u4 o = {pkbf(bflo(y01) + bflo(s01), bfhi(y01) + bfhi(s01)), tag, pkbf(bflo(y23) + bflo(s23), bfhi(y23) + bfhi(s23)), tag};
            GST4((fl >> 2) * 114688 + ((fl & 3) + 4 * g) * 16, o, ws + WS_YG + (4 * tau * 448 + 2 * dp + dsel) * 256, "sc1");
        }
    }
    STAMP(8);
    WAITV(16);
    W2_RD(8, 0, 0); W2_RD(9, 0, 1); W2_RD(10, 0, 2); W2_RD(11, 0, 3); W2_RD(12, 1, 0); W2_RD(13, 1, 1);
    u2 yv[8], xv[2];
    u32 pw0 = pb0 + lane * 16, pw1 = pb1 + lane * 16; PIN(pw0); PIN(pw1);
#define OR4(v) ((v.x | v.y) | (v.z | v.w))
#define H_WAIT(h, NW) asm volatile("s_waitcnt vmcnt(" #NW ")" : "+v"(h##0), "+v"(h##1), "+v"(h##2), "+v"(h##3), "+v"(h##4), "+v"(h##5), "+v"(h##6) :: "memory")
#define UNM(v) do { v.x ^= hm0; v.y ^= hm1; v.z ^= hm2; v.w ^= hm3; } while (0)
#define H_UNM(h) do { UNM(h##0); UNM(h##1); UNM(h##2); UNM(h##3); UNM(h##4); UNM(h##5); UNM(h##6); } while (0)
#define H_OR(h) (OR4(h##0) | OR4(h##1) | OR4(h##2) | OR4(h##3) | OR4(h##4) | OR4(h##5) | OR4(h##6))
#define H_MM1(h, j, o) do { a0 = mfma32(Bw[(o) + j][0], h##j, a0); a1 = mfma32(Bw[(o) + j][1], h##j, a1); } while (0)
#define H_MMA(h, o) do { H_MM1(h, 0, o); H_MM1(h, 1, o); H_MM1(h, 2, o); H_MM1(h, 3, o); H_MM1(h, 4, o); H_MM1(h, 5, o); H_MM1(h, 6, o); } while (0)
#define H_ZERO() _Pragma("unroll") for (int i = 0; i < 16; ++i) { a0[i] = 0; a1[i] = 0; }
#define H_PUT(pw) _Pragma("unroll") for (int q = 0; q < 4; ++q) { LDS(f4, (pw) + q * 1024) = quad(a0, q); LDS(f4, (pw) + 16384 + q * 1024) = quad(a1, q); }
    u32 hbad = 0;
    f16v a0, a1;
    H_ZERO();
    H_WAIT(hA, 9); H_UNM(hA); hbad |= H_OR(hA); PIN(hbad); H_MMA(hA, 0);
    STAMP(9);
    H_LOAD(hA, 2);
    H_WAIT(hB, 9); H_UNM(hB); hbad |= H_OR(hB); PIN(hbad); H_MMA(hB, 7); H_PUT(pw0);
    H_LOAD(hB, 3);
    H_ZERO();
    H_WAIT(hA, 7); H_UNM(hA); hbad |= H_OR(hA); PIN(hbad); H_MMA(hA, 0);
    const u32 dq = 32 * rg + 16 * ch + 4 * hh;
    const u32 yo = dq * 256 + (2 * dsel + (g & 1) + 4 * ((lane >> 2) & 3)) * 16 + ((lane >> 1) & 1) * 8;
    const char* yb = ws + WS_YG + (P * 448 + 64 * ct) * 256;
#define Y_LOAD() do { GLD2(yv[0], yo, yb, "sc1"); GLD2(yv[1], yo, yb, "offset:256 sc1"); GLD2(yv[2], yo, yb, "offset:512 sc1"); GLD2(yv[3], yo, yb, "offset:768 sc1"); \
        GLD2(yv[4], yo, yb, "offset:2048 sc1"); GLD2(yv[5], yo, yb, "offset:2304 sc1"); GLD2(yv[6], yo, yb, "offset:2560 sc1"); GLD2(yv[7], yo, yb, "offset:2816 sc1"); } while (0)
    const u32 xo = (32 * dsel + l31) * 896 + dq * 2;
#pragma unroll
    for (int i = 0; i < 8; ++i) UNDEF(yv[i]);
    UNDEF(xv[0]); UNDEF(xv[1]);
    Y_LOAD();
    GLD2(xv[0], xo, (const char*)x + P * 57344 + ct * 128, ""); GLD2(xv[1], xo, (const char*)x + P * 57344 + ct * 128, "offset:16");
    H_WAIT(hB, 10); H_UNM(hB); hbad |= H_OR(hB); PIN(hbad); H_MMA(hB, 7); H_PUT(pw1);
    if (__any(hbad & 0x80008000u)) {
        do { u64 e_ = __builtin_amdgcn_s_memrealtime() + 20000; while (__builtin_amdgcn_s_memrealtime() < e_) __builtin_amdgcn_s_sleep(8); } while (0);
#pragma unroll 1
        for (int r = 0; r < 2; ++r) {
            H_ZERO();
#define H_FIX(j) { u4 t; _Pragma("unroll 1") for (int sp = 0; sp < SPIN_MAX; ++sp) { RLD4(t, lane * 16, hrd + r * 1024 + (j) * 2048); UNM(t); if (!__any(OR4(t) & 0x80008000u)) break; } \
                a0 = mfma32(Bw[j][0], t, a0); a1 = mfma32(Bw[j][1], t, a1); }
            H_FIX(0) H_FIX(1) H_FIX(2) H_FIX(3) H_FIX(4) H_FIX(5) H_FIX(6) H_FIX(7) H_FIX(8) H_FIX(9) H_FIX(10) H_FIX(11) H_FIX(12) H_FIX(13)
            H_PUT(r ? pw1 : pw0);
        }
    }
    BARRIER();
    STAMP(10);
    f4 r2[2];
    {
        u32 aF = L_RING + (dsel ? 65536 : 32768) + rg * 16384 + ch * 2048 + lane * 16, aS = L_RING + (dsel ? 0 : 98304) + rg * 16384 + ch * 2048 + lane * 16;
        PIN(aF); PIN(aS);
#pragma unroll
        for (int i = 0; i < 2; ++i)
            r2[i] = ((LDS(f4, aF + i * 1024) + LDS(f4, aF + i * 1024 + 4096)) + (LDS(f4, aF + i * 1024 + 8192) + LDS(f4, aF + i * 1024 + 12288))) +
                    ((LDS(f4, aS + i * 1024) + LDS(f4, aS + i * 1024 + 4096)) + (LDS(f4, aS + i * 1024 + 8192) + LDS(f4, aS + i * 1024 + 12288)));
    }
    asm volatile("s_waitcnt vmcnt(0)" : "+v"(yv[0]), "+v"(yv[1]), "+v"(yv[2]), "+v"(yv[3]), "+v"(yv[4]), "+v"(yv[5]), "+v"(yv[6]), "+v"(yv[7]), "+v"(xv[0]), "+v"(xv[1]) :: "memory");
    if (__any(((yv[0].y ^ tag) | (yv[1].y ^ tag) | (yv[2].y ^ tag) | (yv[3].y ^ tag) | (yv[4].y ^ tag) | (yv[5].y ^ tag) | (yv[6].y ^ tag) | (yv[7].y ^ tag)) != 0)) {
#define Y_FIX(i) _Pragma("unroll 1") for (int sp = 0; __any(yv[i].y != tag) && sp < SPIN_MAX; ++sp) RLD2(yv[i], yo, yb + ((i) & 3) * 256 + ((i) >> 2) * 2048)
        Y_FIX(0); Y_FIX(1); Y_FIX(2); Y_FIX(3); Y_FIX(4); Y_FIX(5); Y_FIX(6); Y_FIX(7);
    }
#pragma unroll
    for (int i = 0; i < 2; ++i) {
        float yy[4];
#pragma unroll
        for (int r = 0; r < 4; ++r) yy[r] = (lane & 1) ? bfhi(yv[4 * i + r].x) : bflo(yv[4 * i + r].x);
        const u32 a01 = pkbf(bflo(xv[i].x) + yy[0], bfhi(xv[i].x) + yy[1]), a23 = pkbf(bflo(xv[i].y) + yy[2], bfhi(xv[i].y) + yy[3]);
        const u32 f01 = pkbf(r2[i].x, r2[i].y), f23 = pkbf(r2[i].z, r2[i].w);
        const u2 o = {pkbf(bflo(a01) + bflo(f01), bfhi(a01) + bfhi(f01)), pkbf(bflo(a23) + bflo(f23), bfhi(a23) + bfhi(f23))};
        *(u2*)((char*)out + P * 57344 + ct * 128 + xo + i * 16) = o;
    }
    STAMP(11); STAMPC(23);
    STAMP_FLUSH(24);
}

extern "C" void kernel_launch(void* const* d_in, const int* in_sizes, int n_in, void* d_out, int out_size, void* d_ws, size_t ws_size,
                              hipStream_t stream) {
    (void)hipFuncSetAttribute((const void*)s4ffn_kernel, hipFuncAttributeMaxDynamicSharedMemorySize, LDS_BYTES);
    s4ffn_kernel<<<256, 512, LDS_BYTES, stream>>>((const u16*)d_in[0], (const u16*)d_in[1], (const u16*)d_in[2], (const u16*)d_in[3],
        (const u16*)d_in[4], (const u16*)d_in[5], (const u16*)d_in[6], (const u16*)d_in[7], (const u16*)d_in[8],
        (const u16*)d_in[10], (const u16*)d_in[11], (u16*)d_out, (char*)d_ws);
}
```

```cpp
#include <hip/hip_runtime.h>
#include <stdint.h>

typedef unsigned short u16;
typedef unsigned int u32;
typedef unsigned long long u64;
typedef __attribute__((ext_vector_type(8))) __bf16 bf8;
typedef __attribute__((ext_vector_type(2))) __bf16 bf2;
typedef __attribute__((ext_vector_type(4))) short s4;
typedef __attribute__((ext_vector_type(4))) float f4;
typedef __attribute__((ext_vector_type(16))) float f16v;
typedef __attribute__((ext_vector_type(2))) float f2;
typedef __attribute__((ext_vector_type(4))) u32 u4;
typedef __attribute__((ext_vector_type(2))) u32 u2;
#define LAS __attribute__((address_space(3)))
#define DI __device__ __forceinline__
#define LDS(T, off) (*(LAS T*)(uintptr_t)(u32)(off))

constexpr u32 WS_EPOCH = 0;
constexpr u32 WS_STAT = 4096;
constexpr u32 WS_AR = WS_STAT + 16384;
constexpr u32 WS_XG = 65536;
constexpr u32 WS_YG = WS_XG + 3670016;
constexpr u32 WS_H = WS_YG + 3670016;
constexpr u32 WS_STAMP = 32u << 20;
constexpr u32 L_TAB = 0;
constexpr u32 L_KC = 8192;
constexpr u32 L_CS = 11264;
constexpr u32 L_ST = 11776;
constexpr u32 L_SCR = 15360;
constexpr u32 L_U = 23552;
constexpr u32 L_RING = 32768;
constexpr u32 LDS_BYTES = 163840;
constexpr int SPIN_MAX = 1 << 16;

#define STAMP(i)
#define STAMPC(i)
#define ACC_BEGIN()
#define ACC_END(i)
#define STAMP_DECL
#define STAMP_FLUSH(n)

DI float bflo(u32 v) { return __uint_as_float(v << 16); }
DI float bfhi(u32 v) { return __uint_as_float(v & 0xffff0000u); }
DI u32 pkbf(float a, float b) { f2 v = {a, b}; return __builtin_bit_cast(u32, __builtin_convertvector(v, bf2)); }
DI float rbf(float a) { return bflo(pkbf(a, a)); }
DI f4 mfma(u4 a, u4 b, f4 c) { return __builtin_amdgcn_mfma_f32_16x16x32_bf16(__builtin_bit_cast(bf8, a), __builtin_bit_cast(bf8, b), c, 0, 0, 0); }
DI f16v mfma32(u4 a, u4 b, f16v c) { return __builtin_amdgcn_mfma_f32_32x32x16_bf16(__builtin_bit_cast(bf8, a), __builtin_bit_cast(bf8, b), c, 0, 0, 0); }
DI u2 trread(u32 addr) { return __builtin_bit_cast(u2, __builtin_amdgcn_ds_read_tr16_b64_v4i16((LAS s4*)(uintptr_t)addr)); }
DI u4 trread2(u32 a, u32 b) { const u2 l = trread(a), h = trread(b); return u4{l.x, l.y, h.x, h.y}; }
template <int C> DI float dpp(float x) { return __builtin_bit_cast(float, __builtin_amdgcn_mov_dpp(__builtin_bit_cast(int, x), C, 0xf, 0xf, true)); }
DI float rsum(float x) { x += dpp<0xB1>(x); x += dpp<0x4E>(x); x += dpp<0x141>(x); x += dpp<0x140>(x); return x; }
DI float sw32(float a, float b) { auto s = __builtin_amdgcn_permlane32_swap(__float_as_uint(a), __float_as_uint(b), false, false); return __uint_as_float(s[0]) + __uint_as_float(s[1]); }
DI float sw16(float a, float b) { auto s = __builtin_amdgcn_permlane16_swap(__float_as_uint(a), __float_as_uint(b), false, false); return __uint_as_float(s[0]) + __uint_as_float(s[1]); }
DI float dot2(u32 a, u32 b, float c) { return __builtin_amdgcn_fdot2_f32_bf16(__builtin_bit_cast(bf2, a), __builtin_bit_cast(bf2, b), c, false); }
DI f4 self4(int c, f4 a, f4 b) { return c ? a : b; }
DI f2 cmul(f2 a, f2 b) { float x, y;
    asm("v_mul_f32 %0, %2, %4\n\tv_mul_f32 %1, %3, %4\n\tv_fma_f32 %0, -%3, %5, %0\n\tv_fmac_f32 %1, %2, %5" : "=&v"(x), "=&v"(y) : "v"(a.x), "v"(a.y), "v"(b.x), "v"(b.y));
    return f2{x, y}; }
DI f2 cmulc(f2 a, f2 b) { float x, y;
    asm("v_mul_f32 %0, %2, %4\n\tv_mul_f32 %1, %3, %4\n\tv_fmac_f32 %0, %3, %5\n\tv_fma_f32 %1, -%2, %5, %1" : "=&v"(x), "=&v"(y) : "v"(a.x), "v"(a.y), "v"(b.x), "v"(b.y));
    return f2{x, y}; }
DI float fmul1(float a, float b) { float r; asm("v_mul_f32 %0, %1, %2" : "=v"(r) : "v"(a), "v"(b)); return r; }
DI u32 relu2(u32 v) { typedef __attribute__((ext_vector_type(2))) short s2; const s2 z = {0, 0}; return __builtin_bit_cast(u32, __builtin_elementwise_max(__builtin_bit_cast(s2, v), z)); }
DI f2 zpow(float a2, float rev, float p) {
    float e = __builtin_amdgcn_exp2f(a2 * p), r = __builtin_amdgcn_fractf(rev * p);
    float x, y;
    asm("s_nop 0\n\tv_mul_f32 %0, %2, %3\n\tv_mul_f32 %1, %2, %4" : "=&v"(x), "=&v"(y) : "v"(e), "v"(__builtin_amdgcn_cosf(r)), "v"(__builtin_amdgcn_sinf(r)));
    return f2{x, y};
}
DI f4 quad(const f16v& v, int q) { return f4{v[4 * q], v[4 * q + 1], v[4 * q + 2], v[4 * q + 3]}; }

#define SB() __builtin_amdgcn_sched_barrier(0)
#define BARRIER() asm volatile("s_waitcnt lgkmcnt(0)\n\ts_barrier" ::: "memory")
#define WAITV(n) asm volatile("s_waitcnt vmcnt(" #n ")" ::: "memory")
DI void dma16(u32 voff, const void* base, u32 lds) {
    asm volatile("s_mov_b32 m0, %2\n\ts_nop 0\n\tglobal_load_lds_dwordx4 %0, %1" :: "v"(voff), "s"(base), "s"(lds) : "memory");
}
#define GLD4(dst, voff, base, mod) asm volatile("global_load_dwordx4 %0, %1, %2 " mod : "+v"(dst) : "v"(voff), "s"(base) : "memory")
#define GLD2(dst, voff, base, mod) asm volatile("global_load_dwordx2 %0, %1, %2 " mod : "+v"(dst) : "v"(voff), "s"(base) : "memory")
#define GLD1(dst, voff, base) asm volatile("global_load_dword %0, %1, %2" : "+v"(dst) : "v"(voff), "s"(base) : "memory")
#define GST4(voff, val, base, mod) asm volatile("global_store_dwordx4 %0, %1, %2 " mod "\n\ts_nop 1" :: "v"(voff), "v"(val), "s"(base) : "memory")
#define GST2(voff, val, base, mod) asm volatile("global_store_dwordx2 %0, %1, %2 " mod :: "v"(voff), "v"(val), "s"(base) : "memory")
#define RLD2(dst, voff, base) asm volatile("global_load_dwordx2 %0, %1, %2 sc1\n\ts_waitcnt vmcnt(0)" : "=&v"(dst) : "v"(voff), "s"(base) : "memory")
#define RLD4(dst, voff, base) asm volatile("global_load_dwordx4 %0, %1, %2 sc1\n\ts_waitcnt vmcnt(0)" : "=&v"(dst) : "v"(voff), "s"(base) : "memory")
#define PIN(x) asm volatile("" : "+v"(x))
#define UNDEF(x) asm volatile("" : "=v"(x))

DI int cidx(int l) {
    int s = (l >= 256) + (l >= 512) + (l >= 1024);
    int r = (l + ((1 << s) >> 1) - (s > 0) + ((l >> s) & (s > 0))) >> s;
    return l < 0 ? 641 : 128 * s + r;
}

template <int TS>
DI void conv_pair(f4 (&acc)[4], int k0, u32 ub, u32 la, u32 q2, u32 p3, const u4& sel2, const u4& sel3) {
    constexpr int T[2][4] = {{0, 3, 4, 7}, {1, 2, 5, 6}};
    constexpr int NT = T[TS][3] + 1;
#pragma unroll 1
    for (int kk = 0; kk < 2; ++kk, ++k0) {
        u4 A[NT], B[NT];
        u32 u = ub - 64 * k0, l = la + 608 - 64 * k0, q = q2 + 64 * k0, p = p3 + 16 * k0;
        PIN(u); PIN(l); PIN(q); PIN(p);
        auto lin = [&](u32 a) { u4 r = {LDS(u32, a), LDS(u32, a + 4), LDS(u32, a + 8), LDS(u32, a + 12)}; return r; };
        auto seg2 = [&](u2 v) { u4 r;
            r.x = __builtin_amdgcn_perm(v.y, v.x, sel2.x); r.y = __builtin_amdgcn_perm(v.y, v.x, sel2.y);
            r.z = __builtin_amdgcn_perm(v.y, v.x, sel2.z); r.w = __builtin_amdgcn_perm(v.y, v.x, sel2.w); return r; };
        auto seg3 = [&](u32 v) { u4 r;
            r.x = __builtin_amdgcn_perm(v, v, sel3.x); r.y = __builtin_amdgcn_perm(v, v, sel3.y);
            r.z = __builtin_amdgcn_perm(v, v, sel3.z); r.w = __builtin_amdgcn_perm(v, v, sel3.w); return r; };
        A[0] = lin(l + 512);
#pragma unroll
        for (int i = 0; i < 4; ++i) B[T[TS][i]] = LDS(u4, u + 512 * T[TS][i]);
        A[1] = lin(l);
#pragma unroll
        for (int s = 0; s < NT; ++s) if (s != T[TS][0] && s != T[TS][1] && s != T[TS][2] && s != T[TS][3]) B[s] = LDS(u4, u + 512 * s);
        const u2 v2 = LDS(u2, q), v3 = LDS(u2, q + 512);
        u32 v4[4];
#pragma unroll
        for (int d = 4; d < NT; ++d) v4[d - 4] = LDS(u32, p + 128 * (d - 4));
        SB();
        A[2] = seg2(v2); A[3] = seg2(v3);
#pragma unroll
        for (int d = 4; d < NT; ++d) A[d] = seg3(v4[d - 4]);
        if (k0 == 0) { A[2] = lin(la + 96); A[4] = lin(la); }
#pragma unroll
        for (int d = 0; d < NT; ++d)
#pragma unroll
            for (int i = 0; i < 4; ++i)
                if (T[TS][i] >= d) acc[i] = mfma(A[d], B[T[TS][i] - d], acc[i]);
    }
}

__global__ __launch_bounds__(512, 2) void s4ffn_kernel(const u16* __restrict__ x, const u16* __restrict__ Are, const u16* __restrict__ Aim,
        const u16* __restrict__ Bre, const u16* __restrict__ Bim, const u16* __restrict__ Cre, const u16* __restrict__ Cim,
        const u16* __restrict__ logdt, const u16* __restrict__ Dsk, const u16* __restrict__ w1, const u16* __restrict__ w2,
        u16* __restrict__ out, char* __restrict__ ws) {
    const int tid = threadIdx.x, lane = tid & 63, wave = __builtin_amdgcn_readfirstlane(tid >> 6);
    const u32 b = blockIdx.x, xcd = b & 7, idx = b >> 3;
    const int fl = lane & 15, g = lane >> 4, l31 = lane & 31, hh = lane >> 5;
    u32 pfd = 0;
    const u32 tk0 = (u32)__builtin_amdgcn_s_memrealtime();
    STAMP_DECL;
    {
        u64 pc; asm volatile("s_getpc_b64 %0" : "=s"(pc));
        pc &= ~127ull;
        if (tid < 5) asm volatile("global_load_dword %0, %1, %2" : "+v"(pfd) : "v"((idx * 5 + tid) * 128), "s"(pc) : "memory");
    }
    STAMP(0); STAMPC(22);
    asm volatile("" :: "s"(x), "s"(Are), "s"(Aim), "s"(Bre), "s"(Bim), "s"(Cre), "s"(Cim), "s"(logdt), "s"(Dsk), "s"(w1), "s"(w2), "s"(out), "s"(ws));
    u32 tag;
    if (idx >= 28) {
        asm volatile("s_load_dword %0, %1, 0x0" : "=s"(tag) : "s"(ws));
        const u32 lb = (idx - 28) * 8 + xcd;
        u4 xr[8];
#pragma unroll
        for (int r = 0; r < 8; ++r) xr[r] = u4{0, 0, 0, 0};
        if (lane < 56) {
            const char* xp = (const char*)x + (lb * 64 + wave * 8) * 896;
            GLD4(xr[0], lane * 16, xp, ""); GLD4(xr[1], lane * 16, xp, "offset:896"); GLD4(xr[2], lane * 16, xp, "offset:1792"); GLD4(xr[3], lane * 16, xp, "offset:2688");
            GLD4(xr[4], lane * 16, xp, "offset:3584"); GLD4(xr[5], lane * 16, xp + 4480, ""); GLD4(xr[6], lane * 16, xp + 4480, "offset:896"); GLD4(xr[7], lane * 16, xp + 4480, "offset:1792");
        }
        auto prefetch = [&](u32 k) {
            const u32 n = ((k * 4 + idx - 28) * 8 + wave) * 64 + lane, m = n - 7168;
            const u32 o = n < 7168 ? ((n / 896 + 4) & 7) * 200704 + n % 896 * 128 : m / 672 * 200704 + 114688 + m % 672 * 128;
            asm volatile("global_load_dword %0, %1, %2" : "+v"(pfd) : "v"(min(o, 1605504u)), "s"(w2) : "memory");
        };
        prefetch(0);
        asm volatile("s_waitcnt vmcnt(1) lgkmcnt(0)" : "+v"(xr[0]), "+v"(xr[1]), "+v"(xr[2]), "+v"(xr[3]), "+v"(xr[4]), "+v"(xr[5]), "+v"(xr[6]), "+v"(xr[7]), "+s"(tag) :: "memory");
        tag += 1;
        prefetch(1);
        float sm[8], sq[8];
#pragma unroll
        for (int r = 0; r < 8; ++r) {
            sm[r] = 0; sq[r] = 0;
#pragma unroll
            for (int i = 0; i < 4; ++i) { sm[r] = dot2(xr[r][i], 0x3f803f80u, sm[r]); sq[r] = dot2(xr[r][i], xr[r][i], sq[r]); }
        }
        float a_[2], c_[2]; u32 st_[2];
#pragma unroll
        for (int j = 0; j < 2; ++j) {
            const float mean = rsum(sw16(sw32(sm[j], sm[j + 4]), sw32(sm[j + 2], sm[j + 6]))) * (1.f / 448.f);
            const float var = fmaxf(rsum(sw16(sw32(sq[j], sq[j + 4]), sw32(sq[j + 2], sq[j + 6]))) * (1.f / 448.f) - mean * mean, 0.f);
            const float mb = rbf(mean), sd_ = rbf(__builtin_sqrtf(rbf(rbf(var) + 1e-5f)));
            a_[j] = 1.f / sd_; c_[j] = -mb * a_[j];
            st_[j] = (__float_as_uint(mb) >> 16) | __float_as_uint(sd_);
        }
        if (fl < 2) { const u2 gr = {fl ? st_[1] : st_[0], tag}; GST2((fl + 2 * g) * 8, gr, ws + WS_STAT + (lb * 64 + wave * 8) * 8, "sc1"); }
#pragma unroll
        for (int r = 0; r < 8; ++r) {
            const float av = __builtin_bit_cast(float, __builtin_amdgcn_readlane(__builtin_bit_cast(int, a_[r & 1]), 16 * (r >> 1)));
            const float cv = __builtin_bit_cast(float, __builtin_amdgcn_readlane(__builtin_bit_cast(int, c_[r & 1]), 16 * (r >> 1)));
            u4 o;
#pragma unroll
            for (int i = 0; i < 4; ++i) o[i] = pkbf(fmaf(bflo(xr[r][i]), av, cv), fmaf(bfhi(xr[r][i]), av, cv));
            if (lane < 56) LDS(u4, (wave * 8 + r) * 928 + lane * 16) = o;
        }
        BARRIER();
#pragma unroll 2
        for (u32 i = 0; i < 14; ++i) {
            const u32 n = wave * 14 + i;
            const u2 v = trread((16 * (n & 3) + 4 * g + (fl >> 2)) * 928 + (n >> 2) * 32 + 8 * (lane & 3));
            const u4 gr = {v.x, tag, v.y, tag};
            GST4(lane * 16, gr, ws + WS_XG + lb * 114688 + n * 1024, "sc1");
        }
        if (tid == 0) { const u2 ar = {tag, tk0}; GST2(0, ar, ws + WS_AR + b * 8, "sc1"); }
#pragma unroll 1
        for (u32 k = 2; k < 7; ++k) prefetch(k);
        if (lb == 0 && wave == 0) {
            u4 t0, t1;
#pragma unroll 1
            for (int sp = 0; sp < SPIN_MAX; ++sp) {
                RLD4(t0, lane * 16, ws + WS_AR); RLD4(t1, lane * 16, ws + WS_AR + 1024);
                if (!__any(((t0.x ^ tag) | (t0.z ^ tag) | (t1.x ^ tag) | (t1.z ^ tag)) != 0)) break;
            }
            if (lane == 0) { const u2 e = {tag, tag}; GST2(0, e, ws + WS_EPOCH, "sc1"); }
            int mn = min(min((int)(t0.y - tk0), (int)(t0.w - tk0)), min((int)(t1.y - tk0), (int)(t1.w - tk0)));
            for (int sp = 0; sp < SPIN_MAX; ++sp) {
                RLD4(t0, lane * 16, ws + WS_AR + 4096); RLD4(t1, min(lane, 47) * 16, ws + WS_AR + 4096 + 1024);
                if (!__any(((t0.x ^ tag) | (t0.z ^ tag) | (t1.x ^ tag) | (t1.z ^ tag)) != 0)) break;
            }
            int mx = max(max((int)(t0.y - tk0), (int)(t0.w - tk0)), max((int)(t1.y - tk0), (int)(t1.w - tk0)));
            for (int o = 32; o; o >>= 1) { mx = max(mx, __shfl_xor(mx, o)); mn = min(mn, __shfl_xor(mn, o)); }
            const u32 sk = __builtin_amdgcn_readfirstlane(mx - mn);
            do { u64 e_ = __builtin_amdgcn_s_memrealtime() + (u64)(sk * 10); while (__builtin_amdgcn_s_memrealtime() < e_) __builtin_amdgcn_s_sleep(8); } while (0);
        }
        asm volatile("s_waitcnt vmcnt(0)" :: "v"(pfd) : "memory");
        return;
    }
    const bool isF = wave < 4;
    const u32 q7 = idx / 7, r7 = idx - 7 * q7;
    const u32 rp = 4 * (xcd >> 1) + q7, cc = 7 * (xcd & 1) + r7, P = 4 * xcd + q7, ct = r7, dp = 28 * xcd + idx;
    const u32 w4 = wave & 3, rg = w4 >> 1, ch = wave & 1, dsel = wave >> 2;
    u32 kt = (r7 + 2 * q7) % 7;
    const int r8 = lane >> 3, p8 = lane & 7, rw = (lane >> 2) & 7;
    u32 vo0 = (32 * w4 + r8) * 896 + 16 * (p8 ^ (r8 >> 1)), vo1 = (vo0 + 8 * 896) ^ 64, vo2 = vo0 + 16 * 896, vo3 = vo1 + 16 * 896;
    u32 vo4 = (16 * w4 + rw) * 3584 + 64 * hh + 16 * ((lane & 3) ^ (rw >> 2)), vo5 = vo4 + 128, vo6 = (vo4 + 8 * 3584) ^ 32, vo7 = vo6 + 128;
    const char *xb = (const char*)x + rp * 114688, *wb = (const char*)w1 + cc * 256;
    const char *pa, *pw;
#define RING_SET() do { asm volatile("" : "+s"(kt)); pa = xb + kt * 128; pw = wb + kt * 229376; kt = kt == 6 ? 0 : kt + 1; } while (0)
#define DMA_I(i, l_) do { if ((i) == 0) dma16(vo0, pa, l_); if ((i) == 1) dma16(vo1, pa, l_ + 1024); \
        if ((i) == 2) dma16(vo2, pa, l_ + 2048); if ((i) == 3) dma16(vo3, pa, l_ + 3072); \
        if ((i) == 4) dma16(vo4, pw, l_ + 16384); if ((i) == 5) dma16(vo5, pw, l_ + 17408); \
        if ((i) == 6) dma16(vo6, pw, l_ + 18432); if ((i) == 7) dma16(vo7, pw, l_ + 19456); } while (0)
#define DMA_ALL(l_) do { DMA_I(0, l_); DMA_I(1, l_); DMA_I(2, l_); DMA_I(3, l_); DMA_I(4, l_); DMA_I(5, l_); DMA_I(6, l_); DMA_I(7, l_); } while (0)
    const u32 lw = L_RING + w4 * 4096;
    u4 sel2, sel3; u32 cv_ub, cv_la, cv_q2, cv_p3, dsk2, hm0, hm1, hm2, hm3;
    auto setup = [&]() {
        const u32 tb = L_TAB + dsel * 4096;
        const int x2 = fl & 7, x3 = (fl + 8 * g) & 15;
        u32 w2_ = (u32)(0x001112222233344ull >> (28 - 4 * x2));
        u32 w3_ = (u32)(0x00000055556aaaffull >> (30 - 2 * x3)) & 0xffff;
        const int m2 = w2_ >> 28, m3 = w3_ >> 14;
        w2_ -= m2 * 0x11111111u; w3_ -= m3 * 0x5555u;
#pragma unroll
        for (int q = 0; q < 4; ++q) {
            sel2[q] = ((w2_ >> (8 * q) & 15) | (w2_ >> (8 * q + 4) & 15) << 16) * 0x0202 + 0x01000100;
            sel3[q] = ((w3_ >> (4 * q) & 3) | (w3_ >> (4 * q + 2) & 3) << 16) * 0x0202 + 0x01000100;
        }
        cv_ub = L_U + dsel * 4608 + 2 * (224 + 16 * fl + 8 * g);
        cv_la = tb + 4 * ((fl & 1) * 304 + ((16 - fl + 8 * g) >> 1));
        cv_q2 = tb + 2432 + 8 * (2 * (2 + (fl >> 3) - g) + m2 - 6);
        cv_p3 = tb + 3456 + 4 * (2 * (1 + ((fl - 8 * g) >> 4)) + m3 - 2);
        PIN(sel2); PIN(sel3); PIN(cv_ub); PIN(cv_la); PIN(cv_q2); PIN(cv_p3);
    };
#define SCALARS_ISSUE() do { asm volatile("s_load_dword %0, %1, 0x0" : "=s"(tag) : "s"(ws)); asm volatile("s_load_dword %0, %1, 0x0" : "=s"(dsk2) : "s"((const char*)Dsk + dp * 4)); } while (0)
#define SCALARS_DONE() do { tag += 1; const u32 pat = 1 + tag % 254; hm0 = (pat & 1) << 15 | (pat & 2) << 30; hm1 = (pat & 4) << 13 | (pat & 8) << 28; \
        hm2 = (pat & 16) << 11 | (pat & 32) << 26; hm3 = (pat & 64) << 9 | (pat & 128) << 24; } while (0)
    const char* hb = ws + WS_H + ((2 * rp + rg) * 112 + 8 * cc + 4 * ch) * 2048;
    const u32 vw2 = r8 * 896 + 16 * (p8 ^ (4 * ((r8 >> 1) & 1)));
    const char* w2b = (const char*)w2 + wave * (224 * 896) + ct * 128;
    if (isF) {
        SCALARS_ISSUE();
        __builtin_amdgcn_s_setprio(2); RING_SET(); DMA_ALL(lw); __builtin_amdgcn_s_setprio(1); RING_SET(); DMA_ALL(lw + 32768);
        __builtin_amdgcn_s_setprio(0); RING_SET(); DMA_ALL(lw + 65536);
        setup();
        asm volatile("s_waitcnt lgkmcnt(0)" : "+s"(tag), "+s"(dsk2) :: "memory");
        SCALARS_DONE();
        STAMP(1);
        f16v acc[2][2]; f4 cs = {0, 0, 0, 0};
#pragma unroll
        for (int i = 0; i < 16; ++i) { acc[0][0][i] = 0; acc[0][1][i] = 0; acc[1][0][i] = 0; acc[1][1][i] = 0; }
        const u32 o1 = ((g ^ lane) & 1) ? 0 : 0x3f803f80u; const u4 ones = {o1, o1, o1, o1};
        u32 ax0 = L_RING + (64 * rg + l31) * 128 + 16 * (hh ^ ((l31 >> 1) & 7)), ax1 = ax0 ^ 32, ax2 = ax0 ^ 64, ax3 = ax0 ^ 96;
        u32 aw0 = L_RING + 16384 + 2048 * hh + 1024 * ch + 512 * rg + 64 * (fl >> 2) + 16 * ((2 * (g & 1) + ((lane & 3) >> 1)) ^ (2 * hh)) + 8 * (lane & 1);
        u32 aw1 = aw0 ^ 272, aw2 = aw0 ^ 512, aw3 = aw0 ^ 784;
        u4 X0[4], W0[4], X1[4], W1[4];
#define G1_RD(i, X, W, a0, a1, o) do { if ((i) == 0) { X[0] = LDS(u4, a0); W[0] = trread2(aw0 + (o), aw1 + (o)); } \
        if ((i) == 1) { X[1] = LDS(u4, a0 + 4096); W[1] = trread2(aw2 + (o), aw3 + (o)); } \
        if ((i) == 2) { X[2] = LDS(u4, a1); W[2] = trread2(aw0 + (o) + 4096, aw1 + (o) + 4096); } \
        if ((i) == 3) { X[3] = LDS(u4, a1 + 4096); W[3] = trread2(aw2 + (o) + 4096, aw3 + (o) + 4096); } } while (0)
#define G1_PHASE(X, W, RD, D0) do { \
        acc[0][0] = mfma32(W[0], X[0], acc[0][0]); RD(0); SB(); \
        acc[0][1] = mfma32(W[0], X[1], acc[0][1]); RD(1); SB(); \
        acc[1][0] = mfma32(W[1], X[0], acc[1][0]); DMA_I(D0, ld); SB(); \
        acc[1][1] = mfma32(W[1], X[1], acc[1][1]); RD(2); SB(); \
        cs = mfma(W[0], ones, cs); DMA_I(D0 + 1, ld); SB(); \
        acc[0][0] = mfma32(W[2], X[2], acc[0][0]); RD(3); SB(); \
        acc[0][1] = mfma32(W[2], X[3], acc[0][1]); DMA_I(D0 + 2, ld); SB(); \
        acc[1][0] = mfma32(W[3], X[2], acc[1][0]); SB(); \
        acc[1][1] = mfma32(W[3], X[3], acc[1][1]); DMA_I(D0 + 3, ld); SB(); \
        cs = mfma(W[2], ones, cs); SB(); } while (0)
#define RDA(i) G1_RD(i, X1, W1, ax2, ax3, 8192)
#define RDB(i) G1_RD(i, X0, W0, ax0, ax1, 0)
        asm volatile("s_waitcnt vmcnt(16)" : "+v"(pfd) :: "memory");
        BARRIER();
        RDB(0); RDB(1); RDB(2); RDB(3);
        STAMP(3);
#pragma unroll 1
        for (u32 s = 0; s < 7; ++s) {
            RING_SET();
            if (s >= 4) pa = pw = w2b + (s < 6 ? 4 * 224 * 896 + (s - 4) * 57344 : 0);
            if (s == 4) { vo0 = vw2; vo1 = vw2 + 7168; vo2 = vw2 + 14336; vo3 = vw2 + 21504; vo4 = vw2 + 28672; vo5 = vw2 + 35840; vo6 = vw2 + 43008; vo7 = vw2 + 50176; }
            const u32 ld = lw + ((s + 3) & 3) * 32768;
            SB();
            G1_PHASE(X0, W0, RDA, 0);
            { const u32 m_ = (0x8000u * (((s & 3) + 1) ^ (((s + 1) & 3) + 1)));
              ax0 ^= m_; ax1 ^= m_; ax2 ^= m_; ax3 ^= m_; aw0 ^= m_; aw1 ^= m_; aw2 ^= m_; aw3 ^= m_; }
            if (s < 6) { ACC_BEGIN(); WAITV(12); ACC_END(20); BARRIER(); ACC_END(21); }
            SB();
            G1_PHASE(X1, W1, RDB, 4);
        }
        if (fl < 2) LDS(f4, L_CS + (64 * ch + 32 * rg + 16 * fl + 4 * g) * 4) = cs;
        STAMP(4);
        BARRIER();
        pa = pw = w2b + 57344;
        { const u32 ld = lw + 65536; DMA_ALL(ld); }
#pragma unroll
        for (int tt = 0; tt < 2; ++tt) {
            const u32 sv = LDS(u32, L_ST + (64 * rg + 32 * tt + l31) * 4);
            const float m = -bflo(sv), rs = __builtin_amdgcn_rcpf(bfhi(sv));
            const f2 m2 = {m, m}, rs2 = {rs, rs};
#pragma unroll
            for (int c = 0; c < 2; ++c)
#pragma unroll
                for (int k = 0; k < 2; ++k) {
                    u4 o;
#pragma unroll
                    for (int q = 0; q < 2; ++q) {
                        const f4 cv = LDS(f4, L_CS + ((64 * ch + 32 * rg + 4 * hh) * 4 ^ (128 * c)) + (2 * k + q) * 32), av = quad(acc[c][tt], 2 * k + q);
                        const f2 v0 = __builtin_elementwise_fma(m2, f2{cv.x, cv.y}, f2{av.x, av.y}) * rs2, v1 = __builtin_elementwise_fma(m2, f2{cv.z, cv.w}, f2{av.z, av.w}) * rs2;
                        o[2 * q] = relu2(pkbf(v0.x, v0.y)) | (q ? hm2 : hm0); o[2 * q + 1] = relu2(pkbf(v1.x, v1.y)) | (q ? hm3 : hm1);
                    }
                    const char* hp = hb + (2 * (c ^ rg) + k) * 2048;
                    if (tt) GST4(lane * 16, o, hp, "offset:1024 sc1"); else GST4(lane * 16, o, hp, "sc1");
                }
        }
        STAMP(5);
        WAITV(24);
    } else {
        __builtin_amdgcn_s_setprio(3);
        u32 pq0, pq1, pq2, pq3, pq4, pq5, ldt2;
        UNDEF(pq0); UNDEF(pq1); UNDEF(pq2); UNDEF(pq3); UNDEF(pq4); UNDEF(pq5);
        { const u32 po = (2 * dp + rg) * 512 + ch * 256 + lane * 4;
          GLD1(pq0, po, Are); GLD1(pq1, po, Aim); GLD1(pq2, po, Bre); GLD1(pq3, po, Bim); GLD1(pq4, po, Cre); GLD1(pq5, po, Cim); }
        SCALARS_ISSUE();
        asm volatile("s_load_dword %0, %1, 0x0" : "=s"(ldt2) : "s"((const char*)logdt + dp * 4));
        __builtin_amdgcn_s_setprio(1);
        setup();
        u32 tix[8];
        {
            const int t = tid & 127;
#pragma unroll
            for (int i = 0; i < 5; ++i) {
                const int u = min(t + 128 * i, 607), pi = u >= 304, ip = u - 304 * pi, top = (ip < 24 ? 1056 : 592) - pi - 2 * ip;
                tix[i] = cidx(top) * 2 | cidx(top - 1) << 17;
            }
            tix[5] = (513 + min(t, 126)) * 0x20002 + 0x20000;
            tix[6] = (388 + (t >> 1) + 2 * (t & 1)) * 0x20002 + 0x20000;
            tix[7] = tix[6] + 64 * 0x20002;
#pragma unroll
            for (int i = 0; i < 8; ++i) PIN(tix[i]);
        }
        asm volatile("s_waitcnt lgkmcnt(0)" : "+s"(tag), "+s"(dsk2), "+s"(ldt2) :: "memory");
        SCALARS_DONE();
        STAMP(1);
        const float dt = rbf(expf(rg ? bfhi(ldt2) : bflo(ldt2)));
        u32 sr = L_SCR + w4 * 4352 + fl * 272 + g * 16; PIN(sr);
        asm volatile("s_mov_b32 m0, %0" :: "s"(L_SCR + w4 * 4352));
        asm volatile("s_waitcnt vmcnt(0)" : "+v"(pq0), "+v"(pq1), "+v"(pq2), "+v"(pq3), "+v"(pq4), "+v"(pq5) :: "memory");
        if (tid == 256) { const u2 ar = {tag, tk0}; GST2(0, ar, ws + WS_AR + b * 8, "sc1"); }
        STAMP(2);
        f4 dk[4];
#pragma unroll
        for (int t = 0; t < 4; ++t) dk[t] = f4{0, 0, 0, 0};
        u2 stv; u4 xg0, xg1, xg2, xg3;
        const u32 xo = ((tid & 255) >> 5) * 114688 + ((tid >> 1) & 15) * 256 + (tid & 1) * 16;
        const char* xgb = ws + WS_XG + (dp >> 3) * 4096 + (dp & 7) * 32;
#define SBAR() do { SB(); asm volatile("s_barrier" ::: "memory"); } while (0)
#pragma unroll
        for (int ps = 0; ps < 2; ++ps) {
#define PAR(v) (ps ? bfhi(v) : bflo(v))
            const float a2 = fmul1(dt, PAR(pq0)) * 1.44269504089f, rev = fmul1(dt, PAR(pq1)) * 0.15915494309f;
            const f2 bb = {fmul1(dt, PAR(pq2)), fmul1(dt, PAR(pq3))}, cv = {PAR(pq4), -PAR(pq5)};
            const f2 c = cmulc(cv, bb);
            const f2 z1 = zpow(a2, rev, 1.f), z8 = zpow(a2, rev, 8.f), z64 = zpow(a2, rev, 64.f), z256 = zpow(a2, rev, 256.f);
            const f2 z2 = cmul(z1, z1), z4 = cmul(z2, z2);
            const f2 z32 = zpow(a2, rev, 32.f), z128 = cmul(z64, z64);
            f2 e1 = cmulc(c, z256), e2 = cmulc(e1, z256), e3 = cmulc(e2, z256);
#define PUT(e, v) do { const u32 p_ = pkbf((v).x, (v).y); asm volatile("ds_write_addtid_b32 %0 offset:%1" :: "v"(p_), "i"((e) * 272) : "memory"); } while (0)
            f2 t = c, t4 = cmulc(c, z4), t8 = e1; PUT(0, t); PUT(4, t4); PUT(8, t8);
            t = cmulc(t, z1); t4 = cmulc(t4, z1); t8 = cmulc(t8, z2); PUT(1, t); PUT(5, t4); PUT(9, t8);
            if (ps == 1) { STAMP(17); SBAR(); }
            t = cmulc(t, z1); t4 = cmulc(t4, z1); t8 = cmulc(t8, z2); PUT(2, t); PUT(6, t4); PUT(10, t8);
            t = cmulc(t, z1); t4 = cmulc(t4, z1); t8 = cmulc(t8, z2); PUT(3, t); PUT(7, t4); PUT(11, t8);
            t = cmulc(e2, z4); t4 = cmulc(e3, z4); PUT(12, e2); PUT(14, e3); e3 = cmulc(e3, z256); PUT(13, t); PUT(15, t4);
            u4 B0[4], B1[4];
#pragma unroll
            for (int s = 0; s < 4; ++s) B0[s] = LDS(u4, sr + s * 64);
            const f2 z512 = cmul(z256, z256);
            e2 = cmulc(e3, z256); PUT(0, e3); PUT(1, e2); e3 = cmulc(e3, z512); e2 = cmulc(e2, z512); PUT(2, e3); PUT(3, e2); e3 = cmulc(e3, z512); PUT(4, e3);
#pragma unroll
            for (int s = 0; s < 4; ++s) B1[s] = LDS(u4, sr + s * 64);
            if (ps == 0) STAMP(3); else STAMP(18);
            SBAR();
            f2 r0 = {1.f, 0.f}, r1 = z32, r2 = z64, r3 = cmul(z64, z32);
#pragma unroll
            for (int h = 0; h < 2; ++h) {
                f2 t0 = r0, t1 = r1, t2 = r2, t3 = r3;
                PUT(0, t0); PUT(4, t1); PUT(8, t2); PUT(12, t3);
#define STEP(j) t0 = cmul(t0, z8); t1 = cmul(t1, z8); t2 = cmul(t2, z8); t3 = cmul(t3, z8); PUT(j, t0); PUT(4 + j, t1); PUT(8 + j, t2); PUT(12 + j, t3);
                STEP(1) STEP(2) STEP(3)
                u4 A[4];
#pragma unroll
                for (int s = 0; s < 4; ++s) A[s] = LDS(u4, sr + s * 64);
                asm volatile("" ::: "memory");
                if (h == 0) { r0 = z128; r1 = cmul(r1, z128); r2 = cmul(r2, z128); r3 = cmul(r3, z128); }
#pragma unroll
                for (int s = 0; s < 4; ++s) { dk[2 * h] = mfma(A[s], B0[s], dk[2 * h]); dk[2 * h + 1] = mfma(A[s], B1[s], dk[2 * h + 1]); }
                if (ps == 1 && h == 0) {
                    UNDEF(stv); UNDEF(xg0); UNDEF(xg1); UNDEF(xg2); UNDEF(xg3);
                    GLD2(stv, (tid & 127) * 8, ws + WS_STAT + rp * 1024, "sc1");
                    GLD4(xg0, xo, xgb, "sc1"); GLD4(xg1, xo, xgb + 8 * 114688, "sc1"); GLD4(xg2, xo, xgb + 16 * 114688, "sc1"); GLD4(xg3, xo, xgb + 24 * 114688, "sc1");
                }
                if (ps == 0) STAMP(15 + h); else if (h == 0) STAMP(19);
                if (ps == 1 && h == 1) {
                    const u32 sw = L_SCR + w4 * 4352 + lane * 16;
                    LDS(f4, sw) = self4(ch, dk[0], dk[2]); LDS(f4, sw + 1024) = self4(ch, dk[1], dk[3]);
                    SB(); BARRIER();
                } else SBAR();
            }
        }
        STAMP(12);
        {
            const u32 a = L_SCR + (w4 ^ 1) * 4352 + lane * 16;
#pragma unroll
            for (int jt = 0; jt < 2; ++jt) {
                const f4 s = self4(ch, dk[2 + jt], dk[jt]) + LDS(f4, a + jt * 1024);
                const int j = jt * 16 + fl, i0 = ch * 16 + 4 * g;
                const int mul = jt ? 1 : j < 8 ? 8 : j < 12 ? 4 : 2;
                const int base = jt ? 512 + 32 * fl : j < 8 ? j : j < 12 ? 248 + j : 384 + (j & 1) + 64 * ((j - 12) >> 1);
                const u32 o = L_KC + rg * 1536 + (base + mul * i0) * 2;
                const u32 p01 = pkbf(s.x, s.y), p23 = pkbf(s.z, s.w);
                if (jt == 0) { LDS(u16, o) = (u16)p01; LDS(u16, o + mul * 2) = (u16)(p01 >> 16); LDS(u16, o + mul * 4) = (u16)p23; LDS(u16, o + mul * 6) = (u16)(p23 >> 16); }
                else if (fl < 4) LDS(u2, o) = u2{p01, p23};
                else if (fl == 4 && i0 == 0) LDS(u32, o) = p01 & 0xffff;
            }
            asm volatile("s_waitcnt vmcnt(4)" : "+v"(stv) :: "memory");
#pragma unroll 1
            for (int sp = 0; __any(stv.y != tag) && sp < SPIN_MAX; ++sp) RLD2(stv, (tid & 127) * 8, ws + WS_STAT + rp * 1024);
            if (w4 < 2) LDS(u32, L_ST + (tid & 127) * 4) = stv.x;
        }
        STAMP(4);
        BARRIER();
        {
            u32 kc = L_KC + rg * 1536, td = L_TAB + rg * 4096 + (tid & 127) * 4; PIN(td);
            u32 v[8];
#pragma unroll
            for (int i = 0; i < 8; ++i) v[i] = (u32)LDS(u16, kc + (tix[i] & 0xffff)) | ((u32)LDS(u16, kc + (tix[i] >> 16)) << 16);
#pragma unroll
            for (int i = 0; i < 4; ++i) LDS(u32, td + 512 * i) = v[i];
            if ((tid & 127) < 96) LDS(u32, td + 2048) = v[4];
            LDS(u32, td + 3456) = v[5]; LDS(u32, td + 2432) = v[6]; LDS(u32, td + 2944) = v[7];
        }
        {
            asm volatile("s_waitcnt vmcnt(0)" : "+v"(xg0), "+v"(xg1), "+v"(xg2), "+v"(xg3) :: "memory");
#define XG_FIX(v, i) _Pragma("unroll 1") for (int sp = 0; __any(v.y != tag || v.w != tag) && sp < SPIN_MAX; ++sp) RLD4(v, xo, xgb + (i) * 8 * 114688)
            if (__any(((xg0.y ^ tag) | (xg0.w ^ tag) | (xg1.y ^ tag) | (xg1.w ^ tag) | (xg2.y ^ tag) | (xg2.w ^ tag) | (xg3.y ^ tag) | (xg3.w ^ tag)) != 0)) {
                XG_FIX(xg0, 0); XG_FIX(xg1, 1); XG_FIX(xg2, 2); XG_FIX(xg3, 3);
            }
            u32 a = L_U + 480 + (tid & 1) * 4608 + ((tid & 255) >> 1) * 8; PIN(a);
            LDS(u2, a) = u2{xg0.x, xg0.z}; LDS(u2, a + 1024) = u2{xg1.x, xg1.z}; LDS(u2, a + 2048) = u2{xg2.x, xg2.z}; LDS(u2, a + 3072) = u2{xg3.x, xg3.z};
            if ((tid & 255) < 120) LDS(u2, a - 480) = u2{0, 0};
        }
        __builtin_amdgcn_s_setprio(0);
        STAMP(5);
    }
    BARRIER();
    STAMP(6);
    const u32 pb0 = lw + (isF ? 32768 : 98304), pb1 = lw + (isF ? 65536 : 0);
    u4 Bw[14][2];
    u32 aw2a = pb0 + (4 * hh + (fl >> 2)) * 128 + 16 * ((2 * (g & 1) + ((lane & 3) >> 1)) ^ (4 * ((fl >> 3) & 1))) + 8 * (lane & 1);
    u32 aw2b = aw2a ^ 64, aw2c = aw2a - pb0 + pb1, aw2d = aw2c ^ 64;
#define W2_RD(j, p, n) do { const u32 o_ = ((n) >> 1) * 16384 + ((n) & 1) * 2048; \
        Bw[j][0] = trread2(((p) ? aw2c : aw2a) + o_, ((p) ? aw2c : aw2a) + o_ + 1024); Bw[j][1] = trread2(((p) ? aw2d : aw2b) + o_, ((p) ? aw2d : aw2b) + o_ + 1024); } while (0)
    f4 cacc[4];
    u4 hA0, hA1, hA2, hA3, hA4, hA5, hA6, hB0, hB1, hB2, hB3, hB4, hB5, hB6;
    const char* hrd = ws + WS_H + (P * 112 + wave * 14) * 2048;
#define H_LOAD(h, n) do { const char* p_ = hrd + ((n) & 1) * 14336 + ((n) >> 1) * 1024; GLD4(h##0, lane * 16, p_, "sc1"); GLD4(h##1, lane * 16, p_, "offset:2048 sc1"); \
        GLD4(h##2, lane * 16, p_ + 4096, "sc1"); GLD4(h##3, lane * 16, p_ + 4096, "offset:2048 sc1"); GLD4(h##4, lane * 16, p_ + 8192, "sc1"); \
        GLD4(h##5, lane * 16, p_ + 8192, "offset:2048 sc1"); GLD4(h##6, lane * 16, p_ + 12288, "sc1"); } while (0)
#define H_UNDEF(h) do { UNDEF(h##0); UNDEF(h##1); UNDEF(h##2); UNDEF(h##3); UNDEF(h##4); UNDEF(h##5); UNDEF(h##6); } while (0)
    {
        const int kh = (wave >> 1) & 1, ts = wave & 1;
#pragma unroll
        for (int i = 0; i < 4; ++i) cacc[i] = f4{0, 0, 0, 0};
        if (ts) conv_pair<1>(cacc, 4 * kh, cv_ub, cv_la, cv_q2, cv_p3, sel2, sel3); else conv_pair<0>(cacc, 4 * kh, cv_ub, cv_la, cv_q2, cv_p3, sel2, sel3);
        STAMP(13);
        WAITV(8);
        W2_RD(0, 0, 0); W2_RD(1, 0, 1); W2_RD(2, 0, 2); W2_RD(3, 0, 3); W2_RD(4, 1, 0); W2_RD(5, 1, 1); W2_RD(6, 1, 2); W2_RD(7, 1, 3);
        asm volatile("s_waitcnt lgkmcnt(0)" ::: "memory");
#pragma unroll 1
        for (u32 i = 0; i < 12; ++i) dma16(vw2, w2b + (16 + i) * 7168, (i < 8 ? pb0 : pb1) + ((i >> 2) & 1) * 16384 + (i & 3) * 1024);
        STAMP(14);
        if (ts) conv_pair<1>(cacc, 4 * kh + 2, cv_ub, cv_la, cv_q2, cv_p3, sel2, sel3); else conv_pair<0>(cacc, 4 * kh + 2, cv_ub, cv_la, cv_q2, cv_p3, sel2, sel3);
        STAMP(7);
        H_UNDEF(hA); H_UNDEF(hB);
        H_LOAD(hA, 0); H_LOAD(hB, 1);
        BARRIER();
        u32 ra = wave * 2048 + lane * 16; PIN(ra);
        LDS(f4, ra) = self4(kh, cacc[0], cacc[2]);
        LDS(f4, ra + 1024) = self4(kh, cacc[1], cacc[3]);
        ra ^= 4096;
        BARRIER();
        const float dk = dsel ? bfhi(dsk2) : bflo(dsk2);
#pragma unroll
        for (int i = 0; i < 2; ++i) {
            const int tau = kh ? (ts ? 5 + i : 4 + 3 * i) : (ts ? 1 + i : 3 * i);
            const f4 y = self4(kh, cacc[2 + i], cacc[i]) + LDS(f4, ra + i * 1024);
            const u2 uu = LDS(u2, L_U + dsel * 4608 + 480 + (256 * tau + 16 * fl + 4 * g) * 2);
            const u32 y01 = pkbf(y.x, y.y), y23 = pkbf(y.z, y.w);
            const u32 s01 = pkbf(bflo(uu.x) * dk, bfhi(uu.x) * dk), s23 = pkbf(bflo(uu.y) * dk, bfhi(uu.y) * dk);
            const u4 o = {pkbf(bflo(y01) + bflo(s01), bfhi(y01) + bfhi(s01)), tag, pkbf(bflo(y23) + bflo(s23), bfhi(y23) + bfhi(s23)), tag};
            GST4((fl >> 2) * 114688 + ((fl & 3) + 4 * g) * 16, o, ws + WS_YG + (4 * tau * 448 + 2 * dp + dsel) * 256, "sc1");
        }
    }
    STAMP(8);
    WAITV(16);
    W2_RD(8, 0, 0); W2_RD(9, 0, 1); W2_RD(10, 0, 2); W2_RD(11, 0, 3); W2_RD(12, 1, 0); W2_RD(13, 1, 1);
    u2 yv[8], xv[2];
    u32 pw0 = pb0 + lane * 16, pw1 = pb1 + lane * 16; PIN(pw0); PIN(pw1);
#define OR4(v) ((v.x | v.y) | (v.z | v.w))
#define H_WAIT(h, NW) asm volatile("s_waitcnt vmcnt(" #NW ")" : "+v"(h##0), "+v"(h##1), "+v"(h##2), "+v"(h##3), "+v"(h##4), "+v"(h##5), "+v"(h##6) :: "memory")
#define UNM(v) do { v.x ^= hm0; v.y ^= hm1; v.z ^= hm2; v.w ^= hm3; } while (0)
#define H_UNM(h) do { UNM(h##0); UNM(h##1); UNM(h##2); UNM(h##3); UNM(h##4); UNM(h##5); UNM(h##6); } while (0)
#define H_OR(h) (OR4(h##0) | OR4(h##1) | OR4(h##2) | OR4(h##3) | OR4(h##4) | OR4(h##5) | OR4(h##6))
#define H_MM1(h, j, o) do { a0 = mfma32(Bw[(o) + j][0], h##j, a0); a1 = mfma32(Bw[(o) + j][1], h##j, a1); } while (0)
#define H_MMA(h, o) do { H_MM1(h, 0, o); H_MM1(h, 1, o); H_MM1(h, 2, o); H_MM1(h, 3, o); H_MM1(h, 4, o); H_MM1(h, 5, o); H_MM1(h, 6, o); } while (0)
#define H_ZERO() _Pragma("unroll") for (int i = 0; i < 16; ++i) { a0[i] = 0; a1[i] = 0; }
#define H_PUT(pw) _Pragma("unroll") for (int q = 0; q < 4; ++q) { LDS(f4, (pw) + q * 1024) = quad(a0, q); LDS(f4, (pw) + 16384 + q * 1024) = quad(a1, q); }
    u32 hbad = 0;
    f16v a0, a1;
    H_ZERO();
    H_WAIT(hA, 9); H_UNM(hA); hbad |= H_OR(hA); PIN(hbad); H_MMA(hA, 0);
    STAMP(9);
    H_LOAD(hA, 2);
    H_WAIT(hB, 9); H_UNM(hB); hbad |= H_OR(hB); PIN(hbad); H_MMA(hB, 7); H_PUT(pw0);
    H_LOAD(hB, 3);
    H_ZERO();
    H_WAIT(hA, 7); H_UNM(hA); hbad |= H_OR(hA); PIN(hbad); H_MMA(hA, 0);
    const u32 dq = 32 * rg + 16 * ch + 4 * hh;
    const u32 yo = dq * 256 + (2 * dsel + (g & 1) + 4 * ((lane >> 2) & 3)) * 16 + ((lane >> 1) & 1) * 8;
    const char* yb = ws + WS_YG + (P * 448 + 64 * ct) * 256;
#define Y_LOAD() do { GLD2(yv[0], yo, yb, "sc1"); GLD2(yv[1], yo, yb, "offset:256 sc1"); GLD2(yv[2], yo, yb, "offset:512 sc1"); GLD2(yv[3], yo, yb, "offset:768 sc1"); \
        GLD2(yv[4], yo, yb, "offset:2048 sc1"); GLD2(yv[5], yo, yb, "offset:2304 sc1"); GLD2(yv[6], yo, yb, "offset:2560 sc1"); GLD2(yv[7], yo, yb, "offset:2816 sc1"); } while (0)
    const u32 xo = (32 * dsel + l31) * 896 + dq * 2;
#pragma unroll
    for (int i = 0; i < 8; ++i) UNDEF(yv[i]);
    UNDEF(xv[0]); UNDEF(xv[1]);
    Y_LOAD();
    GLD2(xv[0], xo, (const char*)x + P * 57344 + ct * 128, ""); GLD2(xv[1], xo, (const char*)x + P * 57344 + ct * 128, "offset:16");
    H_WAIT(hB, 10); H_UNM(hB); hbad |= H_OR(hB); PIN(hbad); H_MMA(hB, 7); H_PUT(pw1);
    if (__any(hbad & 0x80008000u)) {
#pragma unroll 1
        for (int r = 0; r < 2; ++r) {
            H_ZERO();
#define H_FIX(j) { u4 t; _Pragma("unroll 1") for (int sp = 0; sp < SPIN_MAX; ++sp) { RLD4(t, lane * 16, hrd + r * 1024 + (j) * 2048); UNM(t); if (!__any(OR4(t) & 0x80008000u)) break; } \
                a0 = mfma32(Bw[j][0], t, a0); a1 = mfma32(Bw[j][1], t, a1); }
            H_FIX(0) H_FIX(1) H_FIX(2) H_FIX(3) H_FIX(4) H_FIX(5) H_FIX(6) H_FIX(7) H_FIX(8) H_FIX(9) H_FIX(10) H_FIX(11) H_FIX(12) H_FIX(13)
            H_PUT(r ? pw1 : pw0);
        }
    }
    BARRIER();
    STAMP(10);
    f4 r2[2];
    {
        u32 aF = L_RING + (dsel ? 65536 : 32768) + rg * 16384 + ch * 2048 + lane * 16, aS = L_RING + (dsel ? 0 : 98304) + rg * 16384 + ch * 2048 + lane * 16;
        PIN(aF); PIN(aS);
#pragma unroll
        for (int i = 0; i < 2; ++i)
            r2[i] = ((LDS(f4, aF + i * 1024) + LDS(f4, aF + i * 1024 + 4096)) + (LDS(f4, aF + i * 1024 + 8192) + LDS(f4, aF + i * 1024 + 12288))) +
                    ((LDS(f4, aS + i * 1024) + LDS(f4, aS + i * 1024 + 4096)) + (LDS(f4, aS + i * 1024 + 8192) + LDS(f4, aS + i * 1024 + 12288)));
    }
    asm volatile("s_waitcnt vmcnt(0)" : "+v"(yv[0]), "+v"(yv[1]), "+v"(yv[2]), "+v"(yv[3]), "+v"(yv[4]), "+v"(yv[5]), "+v"(yv[6]), "+v"(yv[7]), "+v"(xv[0]), "+v"(xv[1]) :: "memory");
    if (__any(((yv[0].y ^ tag) | (yv[1].y ^ tag) | (yv[2].y ^ tag) | (yv[3].y ^ tag) | (yv[4].y ^ tag) | (yv[5].y ^ tag) | (yv[6].y ^ tag) | (yv[7].y ^ tag)) != 0)) {
#define Y_FIX(i) _Pragma("unroll 1") for (int sp = 0; __any(yv[i].y != tag) && sp < SPIN_MAX; ++sp) RLD2(yv[i], yo, yb + ((i) & 3) * 256 + ((i) >> 2) * 2048)
        Y_FIX(0); Y_FIX(1); Y_FIX(2); Y_FIX(3); Y_FIX(4); Y_FIX(5); Y_FIX(6); Y_FIX(7);
    }
#pragma unroll
    for (int i = 0; i < 2; ++i) {
        float yy[4];
#pragma unroll
        for (int r = 0; r < 4; ++r) yy[r] = (lane & 1) ? bfhi(yv[4 * i + r].x) : bflo(yv[4 * i + r].x);
        const u32 a01 = pkbf(bflo(xv[i].x) + yy[0], bfhi(xv[i].x) + yy[1]), a23 = pkbf(bflo(xv[i].y) + yy[2], bfhi(xv[i].y) + yy[3]);
        const u32 f01 = pkbf(r2[i].x, r2[i].y), f23 = pkbf(r2[i].z, r2[i].w);
        const u2 o = {pkbf(bflo(a01) + bflo(f01), bfhi(a01) + bfhi(f01)), pkbf(bflo(a23) + bflo(f23), bfhi(a23) + bfhi(f23))};
        *(u2*)((char*)out + P * 57344 + ct * 128 + xo + i * 16) = o;
    }
    WAITV(0); BARRIER(); if (tid == 0) { const u2 dn = {tag, (u32)__builtin_amdgcn_s_memrealtime()}; GST2(0, dn, ws + WS_AR + 4096 + b * 8, "sc1"); }
    STAMP(11); STAMPC(23);
    STAMP_FLUSH(24);
}

extern "C" void kernel_launch(void* const* d_in, const int* in_sizes, int n_in, void* d_out, int out_size, void* d_ws, size_t ws_size,
                              hipStream_t stream) {
    (void)hipFuncSetAttribute((const void*)s4ffn_kernel, hipFuncAttributeMaxDynamicSharedMemorySize, LDS_BYTES);
    s4ffn_kernel<<<256, 512, LDS_BYTES, stream>>>((const u16*)d_in[0], (const u16*)d_in[1], (const u16*)d_in[2], (const u16*)d_in[3],
        (const u16*)d_in[4], (const u16*)d_in[5], (const u16*)d_in[6], (const u16*)d_in[7], (const u16*)d_in[8],
        (const u16*)d_in[10], (const u16*)d_in[11], (u16*)d_out, (char*)d_ws);
}
```

```cpp
#include <hip/hip_runtime.h>
#include <stdint.h>

typedef unsigned short u16;
typedef unsigned int u32;
typedef unsigned long long u64;
typedef __attribute__((ext_vector_type(8))) __bf16 bf8;
typedef __attribute__((ext_vector_type(2))) __bf16 bf2;
typedef __attribute__((ext_vector_type(4))) short s4;
typedef __attribute__((ext_vector_type(4))) float f4;
typedef __attribute__((ext_vector_type(16))) float f16v;
typedef __attribute__((ext_vector_type(2))) float f2;
typedef __attribute__((ext_vector_type(4))) u32 u4;
typedef __attribute__((ext_vector_type(2))) u32 u2;
#define LAS __attribute__((address_space(3)))
#define DI __device__ __forceinline__
#define LDS(T, off) (*(LAS T*)(uintptr_t)(u32)(off))

constexpr u32 WS_EPOCH = 0;
constexpr u32 WS_STAT = 4096;
constexpr u32 WS_AR = WS_STAT + 16384;
constexpr u32 WS_XG = 65536;
constexpr u32 WS_YG = WS_XG + 3670016;
constexpr u32 WS_H = WS_YG + 3670016;
constexpr u32 WS_STAMP = 32u << 20;
constexpr u32 L_TAB = 0;
constexpr u32 L_KC = 8192;
constexpr u32 L_CS = 11264;
constexpr u32 L_ST = 11776;
constexpr u32 L_SCR = 15360;
constexpr u32 L_U = 23552;
constexpr u32 L_RING = 32768;
constexpr u32 LDS_BYTES = 163840;
constexpr int SPIN_MAX = 1 << 16;

#define STAMP(i)
#define STAMPC(i)
#define ACC_BEGIN()
#define ACC_END(i)
#define STAMP_DECL
#define STAMP_FLUSH(n)

DI float bflo(u32 v) { return __uint_as_float(v << 16); }
DI float bfhi(u32 v) { return __uint_as_float(v & 0xffff0000u); }
DI u32 pkbf(float a, float b) { f2 v = {a, b}; return __builtin_bit_cast(u32, __builtin_convertvector(v, bf2)); }
DI float rbf(float a) { return bflo(pkbf(a, a)); }
DI f4 mfma(u4 a, u4 b, f4 c) { return __builtin_amdgcn_mfma_f32_16x16x32_bf16(__builtin_bit_cast(bf8, a), __builtin_bit_cast(bf8, b), c, 0, 0, 0); }
DI f16v mfma32(u4 a, u4 b, f16v c) { return __builtin_amdgcn_mfma_f32_32x32x16_bf16(__builtin_bit_cast(bf8, a), __builtin_bit_cast(bf8, b), c, 0, 0, 0); }
DI u2 trread(u32 addr) { return __builtin_bit_cast(u2, __builtin_amdgcn_ds_read_tr16_b64_v4i16((LAS s4*)(uintptr_t)addr)); }
DI u4 trread2(u32 a, u32 b) { const u2 l = trread(a), h = trread(b); return u4{l.x, l.y, h.x, h.y}; }
template <int C> DI float dpp(float x) { return __builtin_bit_cast(float, __builtin_amdgcn_mov_dpp(__builtin_bit_cast(int, x), C, 0xf, 0xf, true)); }
DI float rsum(float x) { x += dpp<0xB1>(x); x += dpp<0x4E>(x); x += dpp<0x141>(x); x += dpp<0x140>(x); return x; }
DI float sw32(float a, float b) { auto s = __builtin_amdgcn_permlane32_swap(__float_as_uint(a), __float_as_uint(b), false, false); return __uint_as_float(s[0]) + __uint_as_float(s[1]); }
DI float sw16(float a, float b) { auto s = __builtin_amdgcn_permlane16_swap(__float_as_uint(a), __float_as_uint(b), false, false); return __uint_as_float(s[0]) + __uint_as_float(s[1]); }
DI float dot2(u32 a, u32 b, float c) { return __builtin_amdgcn_fdot2_f32_bf16(__builtin_bit_cast(bf2, a), __builtin_bit_cast(bf2, b), c, false); }
DI f4 self4(int c, f4 a, f4 b) { return c ? a : b; }
DI f2 cmul(f2 a, f2 b) { float x, y;
    asm("v_mul_f32 %0, %2, %4\n\tv_mul_f32 %1, %3, %4\n\tv_fma_f32 %0, -%3, %5, %0\n\tv_fmac_f32 %1, %2, %5" : "=&v"(x), "=&v"(y) : "v"(a.x), "v"(a.y), "v"(b.x), "v"(b.y));
    return f2{x, y}; }
DI f2 cmulc(f2 a, f2 b) { float x, y;
    asm("v_mul_f32 %0, %2, %4\n\tv_mul_f32 %1, %3, %4\n\tv_fmac_f32 %0, %3, %5\n\tv_fma_f32 %1, -%2, %5, %1" : "=&v"(x), "=&v"(y) : "v"(a.x), "v"(a.y), "v"(b.x), "v"(b.y));
    return f2{x, y}; }
DI float fmul1(float a, float b) { float r; asm("v_mul_f32 %0, %1, %2" : "=v"(r) : "v"(a), "v"(b)); return r; }
DI u32 relu2(u32 v) { typedef __attribute__((ext_vector_type(2))) short s2; const s2 z = {0, 0}; return __builtin_bit_cast(u32, __builtin_elementwise_max(__builtin_bit_cast(s2, v), z)); }
DI f2 zpow(float a2, float rev, float p) {
    float e = __builtin_amdgcn_exp2f(a2 * p), r = __builtin_amdgcn_fractf(rev * p);
    float x, y;
    asm("s_nop 0\n\tv_mul_f32 %0, %2, %3\n\tv_mul_f32 %1, %2, %4" : "=&v"(x), "=&v"(y) : "v"(e), "v"(__builtin_amdgcn_cosf(r)), "v"(__builtin_amdgcn_sinf(r)));
    return f2{x, y};
}
DI f4 quad(const f16v& v, int q) { return f4{v[4 * q], v[4 * q + 1], v[4 * q + 2], v[4 * q + 3]}; }

#define SB() __builtin_amdgcn_sched_barrier(0)
#define BARRIER() asm volatile("s_waitcnt lgkmcnt(0)\n\ts_barrier" ::: "memory")
#define WAITV(n) asm volatile("s_waitcnt vmcnt(" #n ")" ::: "memory")
DI void dma16(u32 voff, const void* base, u32 lds) {
    asm volatile("s_mov_b32 m0, %2\n\ts_nop 0\n\tglobal_load_lds_dwordx4 %0, %1" :: "v"(voff), "s"(base), "s"(lds) : "memory");
}
#define GLD4(dst, voff, base, mod) asm volatile("global_load_dwordx4 %0, %1, %2 " mod : "+v"(dst) : "v"(voff), "s"(base) : "memory")
#define GLD2(dst, voff, base, mod) asm volatile("global_load_dwordx2 %0, %1, %2 " mod : "+v"(dst) : "v"(voff), "s"(base) : "memory")
#define GLD1(dst, voff, base) asm volatile("global_load_dword %0, %1, %2" : "+v"(dst) : "v"(voff), "s"(base) : "memory")
#define GST4(voff, val, base, mod) asm volatile("global_store_dwordx4 %0, %1, %2 " mod "\n\ts_nop 1" :: "v"(voff), "v"(val), "s"(base) : "memory")
#define GST2(voff, val, base, mod) asm volatile("global_store_dwordx2 %0, %1, %2 " mod :: "v"(voff), "v"(val), "s"(base) : "memory")
#define RLD2(dst, voff, base) asm volatile("global_load_dwordx2 %0, %1, %2 sc1\n\ts_waitcnt vmcnt(0)" : "=&v"(dst) : "v"(voff), "s"(base) : "memory")
#define RLD4(dst, voff, base) asm volatile("global_load_dwordx4 %0, %1, %2 sc1\n\ts_waitcnt vmcnt(0)" : "=&v"(dst) : "v"(voff), "s"(base) : "memory")
#define PIN(x) asm volatile("" : "+v"(x))
#define UNDEF(x) asm volatile("" : "=v"(x))

DI int cidx(int l) {
    int s = (l >= 256) + (l >= 512) + (l >= 1024);
    int r = (l + ((1 << s) >> 1) - (s > 0) + ((l >> s) & (s > 0))) >> s;
    return l < 0 ? 641 : 128 * s + r;
}

template <int TS>
DI void conv_pair(f4 (&acc)[4], int k0, u32 ub, u32 la, u32 q2, u32 p3, const u4& sel2, const u4& sel3) {
    constexpr int T[2][4] = {{0, 3, 4, 7}, {1, 2, 5, 6}};
    constexpr int NT = T[TS][3] + 1;
#pragma unroll 1
    for (int kk = 0; kk < 2; ++kk, ++k0) {
        u4 A[NT], B[NT];
        u32 u = ub - 64 * k0, l = la + 608 - 64 * k0, q = q2 + 64 * k0, p = p3 + 16 * k0;
        PIN(u); PIN(l); PIN(q); PIN(p);
        auto lin = [&](u32 a) { u4 r = {LDS(u32, a), LDS(u32, a + 4), LDS(u32, a + 8), LDS(u32, a + 12)}; return r; };
        auto seg2 = [&](u2 v) { u4 r;
            r.x = __builtin_amdgcn_perm(v.y, v.x, sel2.x); r.y = __builtin_amdgcn_perm(v.y, v.x, sel2.y);
            r.z = __builtin_amdgcn_perm(v.y, v.x, sel2.z); r.w = __builtin_amdgcn_perm(v.y, v.x, sel2.w); return r; };
        auto seg3 = [&](u32 v) { u4 r;
            r.x = __builtin_amdgcn_perm(v, v, sel3.x); r.y = __builtin_amdgcn_perm(v, v, sel3.y);
            r.z = __builtin_amdgcn_perm(v, v, sel3.z); r.w = __builtin_amdgcn_perm(v, v, sel3.w); return r; };
        A[0] = lin(l + 512);
#pragma unroll
        for (int i = 0; i < 4; ++i) B[T[TS][i]] = LDS(u4, u + 512 * T[TS][i]);
        A[1] = lin(l);
#pragma unroll
        for (int s = 0; s < NT; ++s) if (s != T[TS][0] && s != T[TS][1] && s != T[TS][2] && s != T[TS][3]) B[s] = LDS(u4, u + 512 * s);
        const u2 v2 = LDS(u2, q), v3 = LDS(u2, q + 512);
        u32 v4[4];
#pragma unroll
        for (int d = 4; d < NT; ++d) v4[d - 4] = LDS(u32, p + 128 * (d - 4));
        SB();
        A[2] = seg2(v2); A[3] = seg2(v3);
#pragma unroll
        for (int d = 4; d < NT; ++d) A[d] = seg3(v4[d - 4]);
        if (k0 == 0) { A[2] = lin(la + 96); A[4] = lin(la); }
#pragma unroll
        for (int d = 0; d < NT; ++d)
#pragma unroll
            for (int i = 0; i < 4; ++i)
                if (T[TS][i] >= d) acc[i] = mfma(A[d], B[T[TS][i] - d], acc[i]);
    }
}

__global__ __launch_bounds__(512, 2) void s4ffn_kernel(const u16* __restrict__ x, const u16* __restrict__ Are, const u16* __restrict__ Aim,
        const u16* __restrict__ Bre, const u16* __restrict__ Bim, const u16* __restrict__ Cre, const u16* __restrict__ Cim,
        const u16* __restrict__ logdt, const u16* __restrict__ Dsk, const u16* __restrict__ w1, const u16* __restrict__ w2,
        u16* __restrict__ out, char* __restrict__ ws) {
    const int tid = threadIdx.x, lane = tid & 63, wave = __builtin_amdgcn_readfirstlane(tid >> 6);
    const u32 b = blockIdx.x, xcd = b & 7, idx = b >> 3;
    const int fl = lane & 15, g = lane >> 4, l31 = lane & 31, hh = lane >> 5;
    u32 pfd = 0;
    const u32 tk0 = (u32)__builtin_amdgcn_s_memrealtime();
    STAMP_DECL;
    {
        u64 pc; asm volatile("s_getpc_b64 %0" : "=s"(pc));
        pc &= ~127ull;
        if (tid < 5) asm volatile("global_load_dword %0, %1, %2" : "+v"(pfd) : "v"((idx * 5 + tid) * 128), "s"(pc) : "memory");
    }
    STAMP(0); STAMPC(22);
    asm volatile("" :: "s"(x), "s"(Are), "s"(Aim), "s"(Bre), "s"(Bim), "s"(Cre), "s"(Cim), "s"(logdt), "s"(Dsk), "s"(w1), "s"(w2), "s"(out), "s"(ws));
    u32 tag;
    if (idx >= 28) {
        asm volatile("s_load_dword %0, %1, 0x0" : "=s"(tag) : "s"(ws));
        const u32 lb = (idx - 28) * 8 + xcd;
        u4 xr[8];
#pragma unroll
        for (int r = 0; r < 8; ++r) xr[r] = u4{0, 0, 0, 0};
        if (lane < 56) {
            const char* xp = (const char*)x + (lb * 64 + wave * 8) * 896;
            GLD4(xr[0], lane * 16, xp, ""); GLD4(xr[1], lane * 16, xp, "offset:896"); GLD4(xr[2], lane * 16, xp, "offset:1792"); GLD4(xr[3], lane * 16, xp, "offset:2688");
            GLD4(xr[4], lane * 16, xp, "offset:3584"); GLD4(xr[5], lane * 16, xp + 4480, ""); GLD4(xr[6], lane * 16, xp + 4480, "offset:896"); GLD4(xr[7], lane * 16, xp + 4480, "offset:1792");
        }
        auto prefetch = [&](u32 k) {
            const u32 n = ((k * 4 + idx - 28) * 8 + wave) * 64 + lane, m = n - 7168;
            const u32 o = n < 7168 ? ((n / 896 + 4) & 7) * 200704 + n % 896 * 128 : m / 672 * 200704 + 114688 + m % 672 * 128;
            asm volatile("global_load_dword %0, %1, %2" : "+v"(pfd) : "v"(min(o, 1605504u)), "s"(w2) : "memory");
        };
        prefetch(0);
        asm volatile("s_waitcnt vmcnt(1) lgkmcnt(0)" : "+v"(xr[0]), "+v"(xr[1]), "+v"(xr[2]), "+v"(xr[3]), "+v"(xr[4]), "+v"(xr[5]), "+v"(xr[6]), "+v"(xr[7]), "+s"(tag) :: "memory");
        tag += 1;
        prefetch(1);
        float sm[8], sq[8];
#pragma unroll
        for (int r = 0; r < 8; ++r) {
            sm[r] = 0; sq[r] = 0;
#pragma unroll
            for (int i = 0; i < 4; ++i) { sm[r] = dot2(xr[r][i], 0x3f803f80u, sm[r]); sq[r] = dot2(xr[r][i], xr[r][i], sq[r]); }
        }
        float a_[2], c_[2]; u32 st_[2];
#pragma unroll
        for (int j = 0; j < 2; ++j) {
            const float mean = rsum(sw16(sw32(sm[j], sm[j + 4]), sw32(sm[j + 2], sm[j + 6]))) * (1.f / 448.f);
            const float var = fmaxf(rsum(sw16(sw32(sq[j], sq[j + 4]), sw32(sq[j + 2], sq[j + 6]))) * (1.f / 448.f) - mean * mean, 0.f);
            const float mb = rbf(mean), sd_ = rbf(__builtin_sqrtf(rbf(rbf(var) + 1e-5f)));
            a_[j] = 1.f / sd_; c_[j] = -mb * a_[j];
            st_[j] = (__float_as_uint(mb) >> 16) | __float_as_uint(sd_);
        }
        if (fl < 2) { const u2 gr = {fl ? st_[1] : st_[0], tag}; GST2((fl + 2 * g) * 8, gr, ws + WS_STAT + (lb * 64 + wave * 8) * 8, "sc1"); }
#pragma unroll
        for (int r = 0; r < 8; ++r) {
            const float av = __builtin_bit_cast(float, __builtin_amdgcn_readlane(__builtin_bit_cast(int, a_[r & 1]), 16 * (r >> 1)));
            const float cv = __builtin_bit_cast(float, __builtin_amdgcn_readlane(__builtin_bit_cast(int, c_[r & 1]), 16 * (r >> 1)));
            u4 o;
#pragma unroll
            for (int i = 0; i < 4; ++i) o[i] = pkbf(fmaf(bflo(xr[r][i]), av, cv), fmaf(bfhi(xr[r][i]), av, cv));
            if (lane < 56) LDS(u4, (wave * 8 + r) * 928 + lane * 16) = o;
        }
        BARRIER();
#pragma unroll 2
        for (u32 i = 0; i < 14; ++i) {
            const u32 n = wave * 14 + i;
            const u2 v = trread((16 * (n & 3) + 4 * g + (fl >> 2)) * 928 + (n >> 2) * 32 + 8 * (lane & 3));
            const u4 gr = {v.x, tag, v.y, tag};
            GST4(lane * 16, gr, ws + WS_XG + lb * 114688 + n * 1024, "sc1");
        }
        if (tid == 0) { const u2 ar = {tag, tk0}; GST2(0, ar, ws + WS_AR + b * 8, "sc1"); }
#pragma unroll 1
        for (u32 k = 2; k < 7; ++k) prefetch(k);
        if (lb == 0 && wave == 0) {
            u4 t0, t1;
#pragma unroll 1
            for (int sp = 0; sp < SPIN_MAX; ++sp) {
                RLD4(t0, lane * 16, ws + WS_AR); RLD4(t1, lane * 16, ws + WS_AR + 1024);
                if (!__any(((t0.x ^ tag) | (t0.z ^ tag) | (t1.x ^ tag) | (t1.z ^ tag)) != 0)) break;
            }
            if (lane == 0) { const u2 e = {tag, tag}; GST2(0, e, ws + WS_EPOCH, "sc1"); }
        }
        asm volatile("s_waitcnt vmcnt(0)" :: "v"(pfd) : "memory");
        return;
    }
    const bool isF = wave < 4;
    const u32 q7 = idx / 7, r7 = idx - 7 * q7;
    const u32 rp = 4 * (xcd >> 1) + q7, cc = 7 * (xcd & 1) + r7, P = 4 * xcd + q7, ct = r7, dp = 28 * xcd + idx;
    const u32 w4 = wave & 3, rg = w4 >> 1, ch = wave & 1, dsel = wave >> 2;
    u32 kt = (r7 + 2 * q7) % 7;
    const int r8 = lane >> 3, p8 = lane & 7, rw = (lane >> 2) & 7;
    u32 vo0 = (32 * w4 + r8) * 896 + 16 * (p8 ^ (r8 >> 1)), vo1 = (vo0 + 8 * 896) ^ 64, vo2 = vo0 + 16 * 896, vo3 = vo1 + 16 * 896;
    u32 vo4 = (16 * w4 + rw) * 3584 + 64 * hh + 16 * ((lane & 3) ^ (rw >> 2)), vo5 = vo4 + 128, vo6 = (vo4 + 8 * 3584) ^ 32, vo7 = vo6 + 128;
    const char *xb = (const char*)x + rp * 114688, *wb = (const char*)w1 + cc * 256;
    const char *pa, *pw;
#define RING_SET() do { asm volatile("" : "+s"(kt)); pa = xb + kt * 128; pw = wb + kt * 229376; kt = kt == 6 ? 0 : kt + 1; } while (0)
#define DMA_I(i, l_) do { if ((i) == 0) dma16(vo0, pa, l_); if ((i) == 1) dma16(vo1, pa, l_ + 1024); \
        if ((i) == 2) dma16(vo2, pa, l_ + 2048); if ((i) == 3) dma16(vo3, pa, l_ + 3072); \
        if ((i) == 4) dma16(vo4, pw, l_ + 16384); if ((i) == 5) dma16(vo5, pw, l_ + 17408); \
        if ((i) == 6) dma16(vo6, pw, l_ + 18432); if ((i) == 7) dma16(vo7, pw, l_ + 19456); } while (0)
#define DMA_ALL(l_) do { DMA_I(0, l_); DMA_I(1, l_); DMA_I(2, l_); DMA_I(3, l_); DMA_I(4, l_); DMA_I(5, l_); DMA_I(6, l_); DMA_I(7, l_); } while (0)
    const u32 lw = L_RING + w4 * 4096;
    u4 sel2, sel3; u32 cv_ub, cv_la, cv_q2, cv_p3, dsk2, hm0, hm1, hm2, hm3;
    auto setup = [&]() {
        const u32 tb = L_TAB + dsel * 4096;
        const int x2 = fl & 7, x3 = (fl + 8 * g) & 15;
        u32 w2_ = (u32)(0x001112222233344ull >> (28 - 4 * x2));
        u32 w3_ = (u32)(0x00000055556aaaffull >> (30 - 2 * x3)) & 0xffff;
        const int m2 = w2_ >> 28, m3 = w3_ >> 14;
        w2_ -= m2 * 0x11111111u; w3_ -= m3 * 0x5555u;
#pragma unroll
        for (int q = 0; q < 4; ++q) {
            sel2[q] = ((w2_ >> (8 * q) & 15) | (w2_ >> (8 * q + 4) & 15) << 16) * 0x0202 + 0x01000100;
            sel3[q] = ((w3_ >> (4 * q) & 3) | (w3_ >> (4 * q + 2) & 3) << 16) * 0x0202 + 0x01000100;
        }
        cv_ub = L_U + dsel * 4608 + 2 * (224 + 16 * fl + 8 * g);
        cv_la = tb + 4 * ((fl & 1) * 304 + ((16 - fl + 8 * g) >> 1));
        cv_q2 = tb + 2432 + 8 * (2 * (2 + (fl >> 3) - g) + m2 - 6);
        cv_p3 = tb + 3456 + 4 * (2 * (1 + ((fl - 8 * g) >> 4)) + m3 - 2);
        PIN(sel2); PIN(sel3); PIN(cv_ub); PIN(cv_la); PIN(cv_q2); PIN(cv_p3);
    };
#define SCALARS_ISSUE() do { asm volatile("s_load_dword %0, %1, 0x0" : "=s"(tag) : "s"(ws)); asm volatile("s_load_dword %0, %1, 0x0" : "=s"(dsk2) : "s"((const char*)Dsk + dp * 4)); } while (0)
#define SCALARS_DONE() do { tag += 1; const u32 pat = 1 + tag % 254; hm0 = (pat & 1) << 15 | (pat & 2) << 30; hm1 = (pat & 4) << 13 | (pat & 8) << 28; \
        hm2 = (pat & 16) << 11 | (pat & 32) << 26; hm3 = (pat & 64) << 9 | (pat & 128) << 24; } while (0)
    const char* hb = ws + WS_H + ((2 * rp + rg) * 112 + 8 * cc + 4 * ch) * 2048;
    const u32 vw2 = r8 * 896 + 16 * (p8 ^ (4 * ((r8 >> 1) & 1)));
    const char* w2b = (const char*)w2 + wave * (224 * 896) + ct * 128;
    if (isF) {
        SCALARS_ISSUE();
        __builtin_amdgcn_s_setprio(2); RING_SET(); DMA_ALL(lw); __builtin_amdgcn_s_setprio(1); RING_SET(); DMA_ALL(lw + 32768);
        __builtin_amdgcn_s_setprio(0); RING_SET(); DMA_ALL(lw + 65536);
        setup();
        asm volatile("s_waitcnt lgkmcnt(0)" : "+s"(tag), "+s"(dsk2) :: "memory");
        SCALARS_DONE();
        STAMP(1);
        f16v acc[2][2]; f4 cs = {0, 0, 0, 0};
#pragma unroll
        for (int i = 0; i < 16; ++i) { acc[0][0][i] = 0; acc[0][1][i] = 0; acc[1][0][i] = 0; acc[1][1][i] = 0; }
        const u32 o1 = ((g ^ lane) & 1) ? 0 : 0x3f803f80u; const u4 ones = {o1, o1, o1, o1};
        u32 ax0 = L_RING + (64 * rg + l31) * 128 + 16 * (hh ^ ((l31 >> 1) & 7)), ax1 = ax0 ^ 32, ax2 = ax0 ^ 64, ax3 = ax0 ^ 96;
        u32 aw0 = L_RING + 16384 + 2048 * hh + 1024 * ch + 512 * rg + 64 * (fl >> 2) + 16 * ((2 * (g & 1) + ((lane & 3) >> 1)) ^ (2 * hh)) + 8 * (lane & 1);
        u32 aw1 = aw0 ^ 272, aw2 = aw0 ^ 512, aw3 = aw0 ^ 784;
        u4 X0[4], W0[4], X1[4], W1[4];
#define G1_RD(i, X, W, a0, a1, o) do { if ((i) == 0) { X[0] = LDS(u4, a0); W[0] = trread2(aw0 + (o), aw1 + (o)); } \
        if ((i) == 1) { X[1] = LDS(u4, a0 + 4096); W[1] = trread2(aw2 + (o), aw3 + (o)); } \
        if ((i) == 2) { X[2] = LDS(u4, a1); W[2] = trread2(aw0 + (o) + 4096, aw1 + (o) + 4096); } \
        if ((i) == 3) { X[3] = LDS(u4, a1 + 4096); W[3] = trread2(aw2 + (o) + 4096, aw3 + (o) + 4096); } } while (0)
#define G1_PHASE(X, W, RD, D0) do { \
        acc[0][0] = mfma32(W[0], X[0], acc[0][0]); RD(0); SB(); \
        acc[0][1] = mfma32(W[0], X[1], acc[0][1]); RD(1); SB(); \
        acc[1][0] = mfma32(W[1], X[0], acc[1][0]); DMA_I(D0, ld); SB(); \
        acc[1][1] = mfma32(W[1], X[1], acc[1][1]); RD(2); SB(); \
        cs = mfma(W[0], ones, cs); DMA_I(D0 + 1, ld); SB(); \
        acc[0][0] = mfma32(W[2], X[2], acc[0][0]); RD(3); SB(); \
        acc[0][1] = mfma32(W[2], X[3], acc[0][1]); DMA_I(D0 + 2, ld); SB(); \
        acc[1][0] = mfma32(W[3], X[2], acc[1][0]); SB(); \
        acc[1][1] = mfma32(W[3], X[3], acc[1][1]); DMA_I(D0 + 3, ld); SB(); \
        cs = mfma(W[2], ones, cs); SB(); } while (0)
#define RDA(i) G1_RD(i, X1, W1, ax2, ax3, 8192)
#define RDB(i) G1_RD(i, X0, W0, ax0, ax1, 0)
        asm volatile("s_waitcnt vmcnt(16)" : "+v"(pfd) :: "memory");
        BARRIER();
        RDB(0); RDB(1); RDB(2); RDB(3);
        STAMP(3);
#pragma unroll 1
        for (u32 s = 0; s < 7; ++s) {
            RING_SET();
            if (s >= 4) pa = pw = w2b + (s < 6 ? 4 * 224 * 896 + (s - 4) * 57344 : 0);
            if (s == 4) { vo0 = vw2; vo1 = vw2 + 7168; vo2 = vw2 + 14336; vo3 = vw2 + 21504; vo4 = vw2 + 28672; vo5 = vw2 + 35840; vo6 = vw2 + 43008; vo7 = vw2 + 50176; }
            const u32 ld = lw + ((s + 3) & 3) * 32768;
            SB();
            G1_PHASE(X0, W0, RDA, 0);
            { const u32 m_ = (0x8000u * (((s & 3) + 1) ^ (((s + 1) & 3) + 1)));
              ax0 ^= m_; ax1 ^= m_; ax2 ^= m_; ax3 ^= m_; aw0 ^= m_; aw1 ^= m_; aw2 ^= m_; aw3 ^= m_; }
            if (s < 6) { ACC_BEGIN(); WAITV(12); ACC_END(20); BARRIER(); ACC_END(21); }
            SB();
            G1_PHASE(X1, W1, RDB, 4);
        }
        if (fl < 2) LDS(f4, L_CS + (64 * ch + 32 * rg + 16 * fl + 4 * g) * 4) = cs;
        STAMP(4);
        BARRIER();
        pa = pw = w2b + 57344;
        { const u32 ld = lw + 65536; DMA_ALL(ld); }
#pragma unroll
        for (int tt = 0; tt < 2; ++tt) {
            const u32 sv = LDS(u32, L_ST + (64 * rg + 32 * tt + l31) * 4);
            const float m = -bflo(sv), rs = __builtin_amdgcn_rcpf(bfhi(sv));
            const f2 m2 = {m, m}, rs2 = {rs, rs};
#pragma unroll
            for (int c = 0; c < 2; ++c)
#pragma unroll
                for (int k = 0; k < 2; ++k) {
                    u4 o;
#pragma unroll
                    for (int q = 0; q < 2; ++q) {
                        const f4 cv = LDS(f4, L_CS + ((64 * ch + 32 * rg + 4 * hh) * 4 ^ (128 * c)) + (2 * k + q) * 32), av = quad(acc[c][tt], 2 * k + q);
                        const f2 v0 = __builtin_elementwise_fma(m2, f2{cv.x, cv.y}, f2{av.x, av.y}) * rs2, v1 = __builtin_elementwise_fma(m2, f2{cv.z, cv.w}, f2{av.z, av.w}) * rs2;
                        o[2 * q] = relu2(pkbf(v0.x, v0.y)) | (q ? hm2 : hm0); o[2 * q + 1] = relu2(pkbf(v1.x, v1.y)) | (q ? hm3 : hm1);
                    }
                    const char* hp = hb + (2 * (c ^ rg) + k) * 2048;
                    if (tt) GST4(lane * 16, o, hp, "offset:1024 sc1"); else GST4(lane * 16, o, hp, "sc1");
                }
        }
        STAMP(5);
        WAITV(24);
    } else {
        __builtin_amdgcn_s_setprio(3);
        u32 pq0, pq1, pq2, pq3, pq4, pq5, ldt2;
        UNDEF(pq0); UNDEF(pq1); UNDEF(pq2); UNDEF(pq3); UNDEF(pq4); UNDEF(pq5);
        { const u32 po = (2 * dp + rg) * 512 + ch * 256 + lane * 4;
          GLD1(pq0, po, Are); GLD1(pq1, po, Aim); GLD1(pq2, po, Bre); GLD1(pq3, po, Bim); GLD1(pq4, po, Cre); GLD1(pq5, po, Cim); }
        SCALARS_ISSUE();
        asm volatile("s_load_dword %0, %1, 0x0" : "=s"(ldt2) : "s"((const char*)logdt + dp * 4));
        __builtin_amdgcn_s_setprio(1);
        setup();
        u32 tix[8];
        {
            const int t = tid & 127;
#pragma unroll
            for (int i = 0; i < 5; ++i) {
                const int u = min(t + 128 * i, 607), pi = u >= 304, ip = u - 304 * pi, top = (ip < 24 ? 1056 : 592) - pi - 2 * ip;
                tix[i] = cidx(top) * 2 | cidx(top - 1) << 17;
            }
            tix[5] = (513 + min(t, 126)) * 0x20002 + 0x20000;
            tix[6] = (388 + (t >> 1) + 2 * (t & 1)) * 0x20002 + 0x20000;
            tix[7] = tix[6] + 64 * 0x20002;
#pragma unroll
            for (int i = 0; i < 8; ++i) PIN(tix[i]);
        }
        asm volatile("s_waitcnt lgkmcnt(0)" : "+s"(tag), "+s"(dsk2), "+s"(ldt2) :: "memory");
        SCALARS_DONE();
        STAMP(1);
        const float dt = rbf(expf(rg ? bfhi(ldt2) : bflo(ldt2)));
        u32 sr = L_SCR + w4 * 4352 + fl * 272 + g * 16; PIN(sr);
        asm volatile("s_mov_b32 m0, %0" :: "s"(L_SCR + w4 * 4352));
        asm volatile("s_waitcnt vmcnt(0)" : "+v"(pq0), "+v"(pq1), "+v"(pq2), "+v"(pq3), "+v"(pq4), "+v"(pq5) :: "memory");
        if (tid == 256) { const u2 ar = {tag, tk0}; GST2(0, ar, ws + WS_AR + b * 8, "sc1"); }
        STAMP(2);
        f4 dk[4];
#pragma unroll
        for (int t = 0; t < 4; ++t) dk[t] = f4{0, 0, 0, 0};
        u2 stv; u4 xg0, xg1, xg2, xg3;
        const u32 xo = ((tid & 255) >> 5) * 114688 + ((tid >> 1) & 15) * 256 + (tid & 1) * 16;
        const char* xgb = ws + WS_XG + (dp >> 3) * 4096 + (dp & 7) * 32;
#define SBAR() do { SB(); asm volatile("s_barrier" ::: "memory"); } while (0)
#pragma unroll
        for (int ps = 0; ps < 2; ++ps) {
#define PAR(v) (ps ? bfhi(v) : bflo(v))
            const float a2 = fmul1(dt, PAR(pq0)) * 1.44269504089f, rev = fmul1(dt, PAR(pq1)) * 0.15915494309f;
            const f2 bb = {fmul1(dt, PAR(pq2)), fmul1(dt, PAR(pq3))}, cv = {PAR(pq4), -PAR(pq5)};
            const f2 c = cmulc(cv, bb);
            const f2 z1 = zpow(a2, rev, 1.f), z8 = zpow(a2, rev, 8.f), z64 = zpow(a2, rev, 64.f), z256 = zpow(a2, rev, 256.f);
            const f2 z2 = cmul(z1, z1), z4 = cmul(z2, z2);
            const f2 z32 = zpow(a2, rev, 32.f), z128 = cmul(z64, z64);
            f2 e1 = cmulc(c, z256), e2 = cmulc(e1, z256), e3 = cmulc(e2, z256);
#define PUT(e, v) do { const u32 p_ = pkbf((v).x, (v).y); asm volatile("ds_write_addtid_b32 %0 offset:%1" :: "v"(p_), "i"((e) * 272) : "memory"); } while (0)
            f2 t = c, t4 = cmulc(c, z4), t8 = e1; PUT(0, t); PUT(4, t4); PUT(8, t8);
            t = cmulc(t, z1); t4 = cmulc(t4, z1); t8 = cmulc(t8, z2); PUT(1, t); PUT(5, t4); PUT(9, t8);
            if (ps == 1) { STAMP(17); SBAR(); }
            t = cmulc(t, z1); t4 = cmulc(t4, z1); t8 = cmulc(t8, z2); PUT(2, t); PUT(6, t4); PUT(10, t8);
            t = cmulc(t, z1); t4 = cmulc(t4, z1); t8 = cmulc(t8, z2); PUT(3, t); PUT(7, t4); PUT(11, t8);
            t = cmulc(e2, z4); t4 = cmulc(e3, z4); PUT(12, e2); PUT(14, e3); e3 = cmulc(e3, z256); PUT(13, t); PUT(15, t4);
            u4 B0[4], B1[4];
#pragma unroll
            for (int s = 0; s < 4; ++s) B0[s] = LDS(u4, sr + s * 64);
            const f2 z512 = cmul(z256, z256);
            e2 = cmulc(e3, z256); PUT(0, e3); PUT(1, e2); e3 = cmulc(e3, z512); e2 = cmulc(e2, z512); PUT(2, e3); PUT(3, e2); e3 = cmulc(e3, z512); PUT(4, e3);
#pragma unroll
            for (int s = 0; s < 4; ++s) B1[s] = LDS(u4, sr + s * 64);
            if (ps == 0) STAMP(3); else STAMP(18);
            SBAR();
            f2 r0 = {1.f, 0.f}, r1 = z32, r2 = z64, r3 = cmul(z64, z32);
#pragma unroll
            for (int h = 0; h < 2; ++h) {
                f2 t0 = r0, t1 = r1, t2 = r2, t3 = r3;
                PUT(0, t0); PUT(4, t1); PUT(8, t2); PUT(12, t3);
#define STEP(j) t0 = cmul(t0, z8); t1 = cmul(t1, z8); t2 = cmul(t2, z8); t3 = cmul(t3, z8); PUT(j, t0); PUT(4 + j, t1); PUT(8 + j, t2); PUT(12 + j, t3);
                STEP(1) STEP(2) STEP(3)
                u4 A[4];
#pragma unroll
                for (int s = 0; s < 4; ++s) A[s] = LDS(u4, sr + s * 64);
                asm volatile("" ::: "memory");
                if (h == 0) { r0 = z128; r1 = cmul(r1, z128); r2 = cmul(r2, z128); r3 = cmul(r3, z128); }
#pragma unroll
                for (int s = 0; s < 4; ++s) { dk[2 * h] = mfma(A[s], B0[s], dk[2 * h]); dk[2 * h + 1] = mfma(A[s], B1[s], dk[2 * h + 1]); }
                if (ps == 1 && h == 0) {
                    UNDEF(stv); UNDEF(xg0); UNDEF(xg1); UNDEF(xg2); UNDEF(xg3);
                    GLD2(stv, (tid & 127) * 8, ws + WS_STAT + rp * 1024, "sc1");
                    GLD4(xg0, xo, xgb, "sc1"); GLD4(xg1, xo, xgb + 8 * 114688, "sc1"); GLD4(xg2, xo, xgb + 16 * 114688, "sc1"); GLD4(xg3, xo, xgb + 24 * 114688, "sc1");
                }
                if (ps == 0) STAMP(15 + h); else if (h == 0) STAMP(19);
                if (ps == 1 && h == 1) {
                    const u32 sw = L_SCR + w4 * 4352 + lane * 16;
                    LDS(f4, sw) = self4(ch, dk[0], dk[2]); LDS(f4, sw + 1024) = self4(ch, dk[1], dk[3]);
                    SB(); BARRIER();
                } else SBAR();
            }
        }
        STAMP(12);
        {
            const u32 a = L_SCR + (w4 ^ 1) * 4352 + lane * 16;
#pragma unroll
            for (int jt = 0; jt < 2; ++jt) {
                const f4 s = self4(ch, dk[2 + jt], dk[jt]) + LDS(f4, a + jt * 1024);
                const int j = jt * 16 + fl, i0 = ch * 16 + 4 * g;
                const int mul = jt ? 1 : j < 8 ? 8 : j < 12 ? 4 : 2;
                const int base = jt ? 512 + 32 * fl : j < 8 ? j : j < 12 ? 248 + j : 384 + (j & 1) + 64 * ((j - 12) >> 1);
                const u32 o = L_KC + rg * 1536 + (base + mul * i0) * 2;
                const u32 p01 = pkbf(s.x, s.y), p23 = pkbf(s.z, s.w);
                if (jt == 0) { LDS(u16, o) = (u16)p01; LDS(u16, o + mul * 2) = (u16)(p01 >> 16); LDS(u16, o + mul * 4) = (u16)p23; LDS(u16, o + mul * 6) = (u16)(p23 >> 16); }
                else if (fl < 4) LDS(u2, o) = u2{p01, p23};
                else if (fl == 4 && i0 == 0) LDS(u32, o) = p01 & 0xffff;
            }
            asm volatile("s_waitcnt vmcnt(4)" : "+v"(stv) :: "memory");
#pragma unroll 1
            for (int sp = 0; __any(stv.y != tag) && sp < SPIN_MAX; ++sp) RLD2(stv, (tid & 127) * 8, ws + WS_STAT + rp * 1024);
            if (w4 < 2) LDS(u32, L_ST + (tid & 127) * 4) = stv.x;
        }
        STAMP(4);
        BARRIER();
        {
            u32 kc = L_KC + rg * 1536, td = L_TAB + rg * 4096 + (tid & 127) * 4; PIN(td);
            u32 v[8];
#pragma unroll
            for (int i = 0; i < 8; ++i) v[i] = (u32)LDS(u16, kc + (tix[i] & 0xffff)) | ((u32)LDS(u16, kc + (tix[i] >> 16)) << 16);
#pragma unroll
            for (int i = 0; i < 4; ++i) LDS(u32, td + 512 * i) = v[i];
            if ((tid & 127) < 96) LDS(u32, td + 2048) = v[4];
            LDS(u32, td + 3456) = v[5]; LDS(u32, td + 2432) = v[6]; LDS(u32, td + 2944) = v[7];
        }
        {
            asm volatile("s_waitcnt vmcnt(0)" : "+v"(xg0), "+v"(xg1), "+v"(xg2), "+v"(xg3) :: "memory");
#define XG_FIX(v, i) _Pragma("unroll 1") for (int sp = 0; __any(v.y != tag || v.w != tag) && sp < SPIN_MAX; ++sp) RLD4(v, xo, xgb + (i) * 8 * 114688)
            if (__any(((xg0.y ^ tag) | (xg0.w ^ tag) | (xg1.y ^ tag) | (xg1.w ^ tag) | (xg2.y ^ tag) | (xg2.w ^ tag) | (xg3.y ^ tag) | (xg3.w ^ tag)) != 0)) {
                XG_FIX(xg0, 0); XG_FIX(xg1, 1); XG_FIX(xg2, 2); XG_FIX(xg3, 3);
            }
            u32 a = L_U + 480 + (tid & 1) * 4608 + ((tid & 255) >> 1) * 8; PIN(a);
            LDS(u2, a) = u2{xg0.x, xg0.z}; LDS(u2, a + 1024) = u2{xg1.x, xg1.z}; LDS(u2, a + 2048) = u2{xg2.x, xg2.z}; LDS(u2, a + 3072) = u2{xg3.x, xg3.z};
            if ((tid & 255) < 120) LDS(u2, a - 480) = u2{0, 0};
        }
        __builtin_amdgcn_s_setprio(0);
        STAMP(5);
    }
    BARRIER();
    STAMP(6);
    const u32 pb0 = lw + (isF ? 32768 : 98304), pb1 = lw + (isF ? 65536 : 0);
    u4 Bw[14][2];
    u32 aw2a = pb0 + (4 * hh + (fl >> 2)) * 128 + 16 * ((2 * (g & 1) + ((lane & 3) >> 1)) ^ (4 * ((fl >> 3) & 1))) + 8 * (lane & 1);
    u32 aw2b = aw2a ^ 64, aw2c = aw2a - pb0 + pb1, aw2d = aw2c ^ 64;
#define W2_RD(j, p, n) do { const u32 o_ = ((n) >> 1) * 16384 + ((n) & 1) * 2048; \
        Bw[j][0] = trread2(((p) ? aw2c : aw2a) + o_, ((p) ? aw2c : aw2a) + o_ + 1024); Bw[j][1] = trread2(((p) ? aw2d : aw2b) + o_, ((p) ? aw2d : aw2b) + o_ + 1024); } while (0)
    f4 cacc[4];
    u4 hA0, hA1, hA2, hA3, hA4, hA5, hA6, hB0, hB1, hB2, hB3, hB4, hB5, hB6;
    const char* hrd = ws + WS_H + (P * 112 + wave * 14) * 2048;
#define H_LOAD(h, n) do { const char* p_ = hrd + ((n) & 1) * 14336 + ((n) >> 1) * 1024; GLD4(h##0, lane * 16, p_, "sc1"); GLD4(h##1, lane * 16, p_, "offset:2048 sc1"); \
        GLD4(h##2, lane * 16, p_ + 4096, "sc1"); GLD4(h##3, lane * 16, p_ + 4096, "offset:2048 sc1"); GLD4(h##4, lane * 16, p_ + 8192, "sc1"); \
        GLD4(h##5, lane * 16, p_ + 8192, "offset:2048 sc1"); GLD4(h##6, lane * 16, p_ + 12288, "sc1"); } while (0)
#define H_UNDEF(h) do { UNDEF(h##0); UNDEF(h##1); UNDEF(h##2); UNDEF(h##3); UNDEF(h##4); UNDEF(h##5); UNDEF(h##6); } while (0)
    {
        const int kh = (wave >> 1) & 1, ts = wave & 1;
#pragma unroll
        for (int i = 0; i < 4; ++i) cacc[i] = f4{0, 0, 0, 0};
        if (ts) conv_pair<1>(cacc, 4 * kh, cv_ub, cv_la, cv_q2, cv_p3, sel2, sel3); else conv_pair<0>(cacc, 4 * kh, cv_ub, cv_la, cv_q2, cv_p3, sel2, sel3);
        STAMP(13);
        WAITV(8);
        W2_RD(0, 0, 0); W2_RD(1, 0, 1); W2_RD(2, 0, 2); W2_RD(3, 0, 3); W2_RD(4, 1, 0); W2_RD(5, 1, 1); W2_RD(6, 1, 2); W2_RD(7, 1, 3);
        asm volatile("s_waitcnt lgkmcnt(0)" ::: "memory");
#pragma unroll 1
        for (u32 i = 0; i < 12; ++i) dma16(vw2, w2b + (16 + i) * 7168, (i < 8 ? pb0 : pb1) + ((i >> 2) & 1) * 16384 + (i & 3) * 1024);
        STAMP(14);
        if (ts) conv_pair<1>(cacc, 4 * kh + 2, cv_ub, cv_la, cv_q2, cv_p3, sel2, sel3); else conv_pair<0>(cacc, 4 * kh + 2, cv_ub, cv_la, cv_q2, cv_p3, sel2, sel3);
        STAMP(7);
        H_UNDEF(hA); H_UNDEF(hB);
        H_LOAD(hA, 0); H_LOAD(hB, 1);
        BARRIER();
        u32 ra = wave * 2048 + lane * 16; PIN(ra);
        LDS(f4, ra) = self4(kh, cacc[0], cacc[2]);
        LDS(f4, ra + 1024) = self4(kh, cacc[1], cacc[3]);
        ra ^= 4096;
        BARRIER();
        const float dk = dsel ? bfhi(dsk2) : bflo(dsk2);
#pragma unroll
        for (int i = 0; i < 2; ++i) {
            const int tau = kh ? (ts ? 5 + i : 4 + 3 * i) : (ts ? 1 + i : 3 * i);
            const f4 y = self4(kh, cacc[2 + i], cacc[i]) + LDS(f4, ra + i * 1024);
            const u2 uu = LDS(u2, L_U + dsel * 4608 + 480 + (256 * tau + 16 * fl + 4 * g) * 2);
            const u32 y01 = pkbf(y.x, y.y), y23 = pkbf(y.z, y.w);
            const u32 s01 = pkbf(bflo(uu.x) * dk, bfhi(uu.x) * dk), s23 = pkbf(bflo(uu.y) * dk, bfhi(uu.y) * dk);
            const u4 o = {pkbf(bflo(y01) + bflo(s01), bfhi(y01) + bfhi(s01)), tag, pkbf(bflo(y23) + bflo(s23), bfhi(y23) + bfhi(s23)), tag};
            GST4((fl >> 2) * 114688 + ((fl & 3) + 4 * g) * 16, o, ws + WS_YG + (4 * tau * 448 + 2 * dp + dsel) * 256, "sc1");
        }
    }
    STAMP(8);
    WAITV(16);
    W2_RD(8, 0, 0); W2_RD(9, 0, 1); W2_RD(10, 0, 2); W2_RD(11, 0, 3); W2_RD(12, 1, 0); W2_RD(13, 1, 1);
    u2 yv[8], xv[2];
    u32 pw0 = pb0 + lane * 16, pw1 = pb1 + lane * 16; PIN(pw0); PIN(pw1);
#define OR4(v) ((v.x | v.y) | (v.z | v.w))
#define H_WAIT(h, NW) asm volatile("s_waitcnt vmcnt(" #NW ")" : "+v"(h##0), "+v"(h##1), "+v"(h##2), "+v"(h##3), "+v"(h##4), "+v"(h##5), "+v"(h##6) :: "memory")
#define UNM(v) do { v.x ^= hm0; v.y ^= hm1; v.z ^= hm2; v.w ^= hm3; } while (0)
#define H_UNM(h) do { UNM(h##0); UNM(h##1); UNM(h##2); UNM(h##3); UNM(h##4); UNM(h##5); UNM(h##6); } while (0)
#define H_OR(h) (OR4(h##0) | OR4(h##1) | OR4(h##2) | OR4(h##3) | OR4(h##4) | OR4(h##5) | OR4(h##6))
#define H_MM1(h, j, o) do { a0 = mfma32(Bw[(o) + j][0], h##j, a0); a1 = mfma32(Bw[(o) + j][1], h##j, a1); } while (0)
#define H_MMA(h, o) do { H_MM1(h, 0, o); H_MM1(h, 1, o); H_MM1(h, 2, o); H_MM1(h, 3, o); H_MM1(h, 4, o); H_MM1(h, 5, o); H_MM1(h, 6, o); } while (0)
#define H_ZERO() _Pragma("unroll") for (int i = 0; i < 16; ++i) { a0[i] = 0; a1[i] = 0; }
#define H_PUT(pw) _Pragma("unroll") for (int q = 0; q < 4; ++q) { LDS(f4, (pw) + q * 1024) = quad(a0, q); LDS(f4, (pw) + 16384 + q * 1024) = quad(a1, q); }
    u32 hbad = 0;
    f16v a0, a1;
    H_ZERO();
    H_WAIT(hA, 9); H_UNM(hA); hbad |= H_OR(hA); PIN(hbad); H_MMA(hA, 0);
    STAMP(9);
    H_LOAD(hA, 2);
    H_WAIT(hB, 9); H_UNM(hB); hbad |= H_OR(hB); PIN(hbad); H_MMA(hB, 7); H_PUT(pw0);
    H_LOAD(hB, 3);
    H_ZERO();
    H_WAIT(hA, 7); H_UNM(hA); hbad |= H_OR(hA); PIN(hbad); H_MMA(hA, 0);
    const u32 dq = 32 * rg + 16 * ch + 4 * hh;
    const u32 yo = dq * 256 + (2 * dsel + (g & 1) + 4 * ((lane >> 2) & 3)) * 16 + ((lane >> 1) & 1) * 8;
    const char* yb = ws + WS_YG + (P * 448 + 64 * ct) * 256;
#define Y_LOAD() do { GLD2(yv[0], yo, yb, "sc1"); GLD2(yv[1], yo, yb, "offset:256 sc1"); GLD2(yv[2], yo, yb, "offset:512 sc1"); GLD2(yv[3], yo, yb, "offset:768 sc1"); \
        GLD2(yv[4], yo, yb, "offset:2048 sc1"); GLD2(yv[5], yo, yb, "offset:2304 sc1"); GLD2(yv[6], yo, yb, "offset:2560 sc1"); GLD2(yv[7], yo, yb, "offset:2816 sc1"); } while (0)
    const u32 xo = (32 * dsel + l31) * 896 + dq * 2;
#pragma unroll
    for (int i = 0; i < 8; ++i) UNDEF(yv[i]);
    UNDEF(xv[0]); UNDEF(xv[1]);
    Y_LOAD();
    GLD2(xv[0], xo, (const char*)x + P * 57344 + ct * 128, ""); GLD2(xv[1], xo, (const char*)x + P * 57344 + ct * 128, "offset:16");
    H_WAIT(hB, 10); H_UNM(hB); hbad |= H_OR(hB); PIN(hbad); H_MMA(hB, 7); H_PUT(pw1);
    if (__any(hbad & 0x80008000u)) {
#pragma unroll 1
        for (int r = 0; r < 2; ++r) {
            H_ZERO();
#define H_FIX(j) { u4 t; _Pragma("unroll 1") for (int sp = 0; sp < SPIN_MAX; ++sp) { RLD4(t, lane * 16, hrd + r * 1024 + (j) * 2048); UNM(t); if (!__any(OR4(t) & 0x80008000u)) break; } \
                a0 = mfma32(Bw[j][0], t, a0); a1 = mfma32(Bw[j][1], t, a1); }
            H_FIX(0) H_FIX(1) H_FIX(2) H_FIX(3) H_FIX(4) H_FIX(5) H_FIX(6) H_FIX(7) H_FIX(8) H_FIX(9) H_FIX(10) H_FIX(11) H_FIX(12) H_FIX(13)
            H_PUT(r ? pw1 : pw0);
        }
    }
    BARRIER();
    STAMP(10);
    f4 r2[2];
    {
        u32 aF = L_RING + (dsel ? 65536 : 32768) + rg * 16384 + ch * 2048 + lane * 16, aS = L_RING + (dsel ? 0 : 98304) + rg * 16384 + ch * 2048 + lane * 16;
        PIN(aF); PIN(aS);
#pragma unroll
        for (int i = 0; i < 2; ++i)
            r2[i] = ((LDS(f4, aF + i * 1024) + LDS(f4, aF + i * 1024 + 4096)) + (LDS(f4, aF + i * 1024 + 8192) + LDS(f4, aF + i * 1024 + 12288))) +
                    ((LDS(f4, aS + i * 1024) + LDS(f4, aS + i * 1024 + 4096)) + (LDS(f4, aS + i * 1024 + 8192) + LDS(f4, aS + i * 1024 + 12288)));
    }
    asm volatile("s_waitcnt vmcnt(0)" : "+v"(yv[0]), "+v"(yv[1]), "+v"(yv[2]), "+v"(yv[3]), "+v"(yv[4]), "+v"(yv[5]), "+v"(yv[6]), "+v"(yv[7]), "+v"(xv[0]), "+v"(xv[1]) :: "memory");
    if (__any(((yv[0].y ^ tag) | (yv[1].y ^ tag) | (yv[2].y ^ tag) | (yv[3].y ^ tag) | (yv[4].y ^ tag) | (yv[5].y ^ tag) | (yv[6].y ^ tag) | (yv[7].y ^ tag)) != 0)) {
#define Y_FIX(i) _Pragma("unroll 1") for (int sp = 0; __any(yv[i].y != tag) && sp < SPIN_MAX; ++sp) RLD2(yv[i], yo, yb + ((i) & 3) * 256 + ((i) >> 2) * 2048)
        Y_FIX(0); Y_FIX(1); Y_FIX(2); Y_FIX(3); Y_FIX(4); Y_FIX(5); Y_FIX(6); Y_FIX(7);
    }
#pragma unroll
    for (int i = 0; i < 2; ++i) {
        float yy[4];
#pragma unroll
        for (int r = 0; r < 4; ++r) yy[r] = (lane & 1) ? bfhi(yv[4 * i + r].x) : bflo(yv[4 * i + r].x);
        const u32 a01 = pkbf(bflo(xv[i].x) + yy[0], bfhi(xv[i].x) + yy[1]), a23 = pkbf(bflo(xv[i].y) + yy[2], bfhi(xv[i].y) + yy[3]);
        const u32 f01 = pkbf(r2[i].x, r2[i].y), f23 = pkbf(r2[i].z, r2[i].w);
        const u2 o = {pkbf(bflo(a01) + bflo(f01), bfhi(a01) + bfhi(f01)), pkbf(bflo(a23) + bflo(f23), bfhi(a23) + bfhi(f23))};
        *(u2*)((char*)out + P * 57344 + ct * 128 + xo + i * 16) = o;
    }
    BARRIER(); if (tid == 0) { const u2 dn = {tag, (u32)__builtin_amdgcn_s_memrealtime()}; GST2(0, dn, ws + WS_AR + 4096 + b * 8, "sc1"); }
    STAMP(11); STAMPC(23);
    STAMP_FLUSH(24);
}

__global__ void kA(char* ws) { if (threadIdx.x == 0) *(u32*)(ws + WS_AR + 8192) = (u32)__builtin_amdgcn_s_memrealtime(); }
__global__ void kB(char* ws) {
    const u32 now = (u32)__builtin_amdgcn_s_memrealtime();
    const u32 ta = *(u32*)(ws + WS_AR + 8192);
    int t = threadIdx.x;
    int st = (int)(((u32*)(ws + WS_AR))[t * 2 + 1] - ta), en = t < 224 ? (int)(((u32*)(ws + WS_AR + 4096))[t * 2 + 1] - ta) : 0;
    __shared__ int smn, smx; if (t == 0) { smn = 1 << 30; smx = 0; } __syncthreads();
    atomicMin(&smn, st); atomicMax(&smx, en); __syncthreads();
    const int v = 3 == 0 ? smn : 3 == 1 ? (int)(now - ta) - smx : smx - smn;
    u64 e_ = __builtin_amdgcn_s_memrealtime() + (u64)v * 0; while (__builtin_amdgcn_s_memrealtime() < e_) __builtin_amdgcn_s_sleep(8);
}

extern "C" void kernel_launch(void* const* d_in, const int* in_sizes, int n_in, void* d_out, int out_size, void* d_ws, size_t ws_size,
                              hipStream_t stream) {
    (void)hipFuncSetAttribute((const void*)s4ffn_kernel, hipFuncAttributeMaxDynamicSharedMemorySize, LDS_BYTES);
    kA<<<1, 64, 0, stream>>>((char*)d_ws);
    s4ffn_kernel<<<256, 512, LDS_BYTES, stream>>>((const u16*)d_in[0], (const u16*)d_in[1], (const u16*)d_in[2], (const u16*)d_in[3],
        (const u16*)d_in[4], (const u16*)d_in[5], (const u16*)d_in[6], (const u16*)d_in[7], (const u16*)d_in[8],
        (const u16*)d_in[10], (const u16*)d_in[11], (u16*)d_out, (char*)d_ws);
    kB<<<1, 256, 0, stream>>>((char*)d_ws);
}
```

```cpp
#include <hip/hip_runtime.h>
#include <stdint.h>

typedef unsigned short u16;
typedef unsigned int u32;
typedef unsigned long long u64;
typedef __attribute__((ext_vector_type(8))) __bf16 bf8;
typedef __attribute__((ext_vector_type(2))) __bf16 bf2;
typedef __attribute__((ext_vector_type(4))) short s4;
typedef __attribute__((ext_vector_type(4))) float f4;
typedef __attribute__((ext_vector_type(16))) float f16v;
typedef __attribute__((ext_vector_type(2))) float f2;
typedef __attribute__((ext_vector_type(4))) u32 u4;
typedef __attribute__((ext_vector_type(2))) u32 u2;
#define LAS __attribute__((address_space(3)))
#define DI __device__ __forceinline__
#define LDS(T, off) (*(LAS T*)(uintptr_t)(u32)(off))

constexpr u32 WS_EPOCH = 0;
constexpr u32 WS_STAT = 4096;
constexpr u32 WS_AR = WS_STAT + 16384;
constexpr u32 WS_XG = 65536;
constexpr u32 WS_YG = WS_XG + 3670016;
constexpr u32 WS_H = WS_YG + 3670016;
constexpr u32 WS_STAMP = 32u << 20;
constexpr u32 L_TAB = 0;
constexpr u32 L_KC = 8192;
constexpr u32 L_CS = 11264;
constexpr u32 L_ST = 11776;
constexpr u32 L_SCR = 15360;
constexpr u32 L_U = 23552;
constexpr u32 L_RING = 32768;
constexpr u32 LDS_BYTES = 163840;
constexpr int SPIN_MAX = 1 << 16;

#define STAMP(i)
#define STAMPC(i)
#define ACC_BEGIN()
#define ACC_END(i)
#define STAMP_DECL
#define STAMP_FLUSH(n)

DI float bflo(u32 v) { return __uint_as_float(v << 16); }
DI float bfhi(u32 v) { return __uint_as_float(v & 0xffff0000u); }
DI u32 pkbf(float a, float b) { f2 v = {a, b}; return __builtin_bit_cast(u32, __builtin_convertvector(v, bf2)); }
DI float rbf(float a) { return bflo(pkbf(a, a)); }
DI f4 mfma(u4 a, u4 b, f4 c) { return __builtin_amdgcn_mfma_f32_16x16x32_bf16(__builtin_bit_cast(bf8, a), __builtin_bit_cast(bf8, b), c, 0, 0, 0); }
DI f16v mfma32(u4 a, u4 b, f16v c) { return __builtin_amdgcn_mfma_f32_32x32x16_bf16(__builtin_bit_cast(bf8, a), __builtin_bit_cast(bf8, b), c, 0, 0, 0); }
DI u2 trread(u32 addr) { return __builtin_bit_cast(u2, __builtin_amdgcn_ds_read_tr16_b64_v4i16((LAS s4*)(uintptr_t)addr)); }
DI u4 trread2(u32 a, u32 b) { const u2 l = trread(a), h = trread(b); return u4{l.x, l.y, h.x, h.y}; }
template <int C> DI float dpp(float x) { return __builtin_bit_cast(float, __builtin_amdgcn_mov_dpp(__builtin_bit_cast(int, x), C, 0xf, 0xf, true)); }
DI float rsum(float x) { x += dpp<0xB1>(x); x += dpp<0x4E>(x); x += dpp<0x141>(x); x += dpp<0x140>(x); return x; }
DI float sw32(float a, float b) { auto s = __builtin_amdgcn_permlane32_swap(__float_as_uint(a), __float_as_uint(b), false, false); return __uint_as_float(s[0]) + __uint_as_float(s[1]); }
DI float sw16(float a, float b) { auto s = __builtin_amdgcn_permlane16_swap(__float_as_uint(a), __float_as_uint(b), false, false); return __uint_as_float(s[0]) + __uint_as_float(s[1]); }
DI float dot2(u32 a, u32 b, float c) { return __builtin_amdgcn_fdot2_f32_bf16(__builtin_bit_cast(bf2, a), __builtin_bit_cast(bf2, b), c, false); }
DI f4 self4(int c, f4 a, f4 b) { return c ? a : b; }
DI f2 cmul(f2 a, f2 b) { float x, y;
    asm("v_mul_f32 %0, %2, %4\n\tv_mul_f32 %1, %3, %4\n\tv_fma_f32 %0, -%3, %5, %0\n\tv_fmac_f32 %1, %2, %5" : "=&v"(x), "=&v"(y) : "v"(a.x), "v"(a.y), "v"(b.x), "v"(b.y));
    return f2{x, y}; }
DI f2 cmulc(f2 a, f2 b) { float x, y;
    asm("v_mul_f32 %0, %2, %4\n\tv_mul_f32 %1, %3, %4\n\tv_fmac_f32 %0, %3, %5\n\tv_fma_f32 %1, -%2, %5, %1" : "=&v"(x), "=&v"(y) : "v"(a.x), "v"(a.y), "v"(b.x), "v"(b.y));
    return f2{x, y}; }
DI float fmul1(float a, float b) { float r; asm("v_mul_f32 %0, %1, %2" : "=v"(r) : "v"(a), "v"(b)); return r; }
DI u32 relu2(u32 v) { typedef __attribute__((ext_vector_type(2))) short s2; const s2 z = {0, 0}; return __builtin_bit_cast(u32, __builtin_elementwise_max(__builtin_bit_cast(s2, v), z)); }
DI f2 zpow(float a2, float rev, float p) {
    float e = __builtin_amdgcn_exp2f(a2 * p), r = __builtin_amdgcn_fractf(rev * p);
    float x, y;
    asm("s_nop 0\n\tv_mul_f32 %0, %2, %3\n\tv_mul_f32 %1, %2, %4" : "=&v"(x), "=&v"(y) : "v"(e), "v"(__builtin_amdgcn_cosf(r)), "v"(__builtin_amdgcn_sinf(r)));
    return f2{x, y};
}
DI f4 quad(const f16v& v, int q) { return f4{v[4 * q], v[4 * q + 1], v[4 * q + 2], v[4 * q + 3]}; }

#define SB() __builtin_amdgcn_sched_barrier(0)
#define BARRIER() asm volatile("s_waitcnt lgkmcnt(0)\n\ts_barrier" ::: "memory")
#define WAITV(n) asm volatile("s_waitcnt vmcnt(" #n ")" ::: "memory")
DI void dma16(u32 voff, const void* base, u32 lds) {
    asm volatile("s_mov_b32 m0, %2\n\ts_nop 0\n\tglobal_load_lds_dwordx4 %0, %1" :: "v"(voff), "s"(base), "s"(lds) : "memory");
}
#define GLD4(dst, voff, base, mod) asm volatile("global_load_dwordx4 %0, %1, %2 " mod : "+v"(dst) : "v"(voff), "s"(base) : "memory")
#define GLD2(dst, voff, base, mod) asm volatile("global_load_dwordx2 %0, %1, %2 " mod : "+v"(dst) : "v"(voff), "s"(base) : "memory")
#define GLD1(dst, voff, base) asm volatile("global_load_dword %0, %1, %2" : "+v"(dst) : "v"(voff), "s"(base) : "memory")
#define GST4(voff, val, base, mod) asm volatile("global_store_dwordx4 %0, %1, %2 " mod "\n\ts_nop 1" :: "v"(voff), "v"(val), "s"(base) : "memory")
#define GST2(voff, val, base, mod) asm volatile("global_store_dwordx2 %0, %1, %2 " mod :: "v"(voff), "v"(val), "s"(base) : "memory")
#define RLD2(dst, voff, base) asm volatile("global_load_dwordx2 %0, %1, %2 sc1\n\ts_waitcnt vmcnt(0)" : "=&v"(dst) : "v"(voff), "s"(base) : "memory")
#define RLD4(dst, voff, base) asm volatile("global_load_dwordx4 %0, %1, %2 sc1\n\ts_waitcnt vmcnt(0)" : "=&v"(dst) : "v"(voff), "s"(base) : "memory")
#define PIN(x) asm volatile("" : "+v"(x))
#define UNDEF(x) asm volatile("" : "=v"(x))

DI int cidx(int l) {
    int s = (l >= 256) + (l >= 512) + (l >= 1024);
    int r = (l + ((1 << s) >> 1) - (s > 0) + ((l >> s) & (s > 0))) >> s;
    return l < 0 ? 641 : 128 * s + r;
}

template <int TS>
DI void conv_pair(f4 (&acc)[4], int k0, u32 ub, u32 la, u32 q2, u32 p3, const u4& sel2, const u4& sel3) {
    constexpr int T[2][4] = {{0, 3, 4, 7}, {1, 2, 5, 6}};
    constexpr int NT = T[TS][3] + 1;
#pragma unroll 1
    for (int kk = 0; kk < 2; ++kk, ++k0) {
        u4 A[NT], B[NT];
        u32 u = ub - 64 * k0, l = la + 608 - 64 * k0, q = q2 + 64 * k0, p = p3 + 16 * k0;
        PIN(u); PIN(l); PIN(q); PIN(p);
        auto lin = [&](u32 a) { u4 r = {LDS(u32, a), LDS(u32, a + 4), LDS(u32, a + 8), LDS(u32, a + 12)}; return r; };
        auto seg2 = [&](u2 v) { u4 r;
            r.x = __builtin_amdgcn_perm(v.y, v.x, sel2.x); r.y = __builtin_amdgcn_perm(v.y, v.x, sel2.y);
            r.z = __builtin_amdgcn_perm(v.y, v.x, sel2.z); r.w = __builtin_amdgcn_perm(v.y, v.x, sel2.w); return r; };
        auto seg3 = [&](u32 v) { u4 r;
            r.x = __builtin_amdgcn_perm(v, v, sel3.x); r.y = __builtin_amdgcn_perm(v, v, sel3.y);
            r.z = __builtin_amdgcn_perm(v, v, sel3.z); r.w = __builtin_amdgcn_perm(v, v, sel3.w); return r; };
        A[0] = lin(l + 512);
#pragma unroll
        for (int i = 0; i < 4; ++i) B[T[TS][i]] = LDS(u4, u + 512 * T[TS][i]);
        A[1] = lin(l);
#pragma unroll
        for (int s = 0; s < NT; ++s) if (s != T[TS][0] && s != T[TS][1] && s != T[TS][2] && s != T[TS][3]) B[s] = LDS(u4, u + 512 * s);
        const u2 v2 = LDS(u2, q), v3 = LDS(u2, q + 512);
        u32 v4[4];
#pragma unroll
        for (int d = 4; d < NT; ++d) v4[d - 4] = LDS(u32, p + 128 * (d - 4));
        SB();
        A[2] = seg2(v2); A[3] = seg2(v3);
#pragma unroll
        for (int d = 4; d < NT; ++d) A[d] = seg3(v4[d - 4]);
        if (k0 == 0) { A[2] = lin(la + 96); A[4] = lin(la); }
#pragma unroll
        for (int d = 0; d < NT; ++d)
#pragma unroll
            for (int i = 0; i < 4; ++i)
                if (T[TS][i] >= d) acc[i] = mfma(A[d], B[T[TS][i] - d], acc[i]);
    }
}

__global__ __launch_bounds__(512, 2) void s4ffn_kernel(const u16* __restrict__ x, const u16* __restrict__ Are, const u16* __restrict__ Aim,
        const u16* __restrict__ Bre, const u16* __restrict__ Bim, const u16* __restrict__ Cre, const u16* __restrict__ Cim,
        const u16* __restrict__ logdt, const u16* __restrict__ Dsk, const u16* __restrict__ w1, const u16* __restrict__ w2,
        u16* __restrict__ out, char* __restrict__ ws) {
    const int tid = threadIdx.x, lane = tid & 63, wave = __builtin_amdgcn_readfirstlane(tid >> 6);
    const u32 b = blockIdx.x, xcd = b & 7, idx = b >> 3;
    const int fl = lane & 15, g = lane >> 4, l31 = lane & 31, hh = lane >> 5;
    u32 pfd = 0;
    STAMP_DECL;
    {
        u64 pc; asm volatile("s_getpc_b64 %0" : "=s"(pc));
        pc &= ~127ull;
        if (tid < 5) asm volatile("global_load_dword %0, %1, %2" : "+v"(pfd) : "v"((idx * 5 + tid) * 128), "s"(pc) : "memory");
    }
    STAMP(0); STAMPC(22);
    asm volatile("" :: "s"(x), "s"(Are), "s"(Aim), "s"(Bre), "s"(Bim), "s"(Cre), "s"(Cim), "s"(logdt), "s"(Dsk), "s"(w1), "s"(w2), "s"(out), "s"(ws));
    u32 tag;
    if (idx >= 28) {
        asm volatile("s_load_dword %0, %1, 0x0" : "=s"(tag) : "s"(ws));
        const u32 lb = (idx - 28) * 8 + xcd;
        u4 xr[8];
#pragma unroll
        for (int r = 0; r < 8; ++r) xr[r] = u4{0, 0, 0, 0};
        if (lane < 56) {
            const char* xp = (const char*)x + (lb * 64 + wave * 8) * 896;
            GLD4(xr[0], lane * 16, xp, ""); GLD4(xr[1], lane * 16, xp, "offset:896"); GLD4(xr[2], lane * 16, xp, "offset:1792"); GLD4(xr[3], lane * 16, xp, "offset:2688");
            GLD4(xr[4], lane * 16, xp, "offset:3584"); GLD4(xr[5], lane * 16, xp + 4480, ""); GLD4(xr[6], lane * 16, xp + 4480, "offset:896"); GLD4(xr[7], lane * 16, xp + 4480, "offset:1792");
        }
        auto prefetch = [&](u32 k) {
            const u32 n = ((k * 4 + idx - 28) * 8 + wave) * 64 + lane, m = n - 7168;
            const u32 o = n < 7168 ? ((n / 896 + 4) & 7) * 200704 + n % 896 * 128 : m / 672 * 200704 + 114688 + m % 672 * 128;
            asm volatile("global_load_dword %0, %1, %2" : "+v"(pfd) : "v"(min(o, 1605504u)), "s"(w2) : "memory");
        };
        prefetch(0);
        asm volatile("s_waitcnt vmcnt(1) lgkmcnt(0)" : "+v"(xr[0]), "+v"(xr[1]), "+v"(xr[2]), "+v"(xr[3]), "+v"(xr[4]), "+v"(xr[5]), "+v"(xr[6]), "+v"(xr[7]), "+s"(tag) :: "memory");
        tag += 1;
        prefetch(1);
        float sm[8], sq[8];
#pragma unroll
        for (int r = 0; r < 8; ++r) {
            sm[r] = 0; sq[r] = 0;
#pragma unroll
            for (int i = 0; i < 4; ++i) { sm[r] = dot2(xr[r][i], 0x3f803f80u, sm[r]); sq[r] = dot2(xr[r][i], xr[r][i], sq[r]); }
        }
        float a_[2], c_[2]; u32 st_[2];
#pragma unroll
        for (int j = 0; j < 2; ++j) {
            const float mean = rsum(sw16(sw32(sm[j], sm[j + 4]), sw32(sm[j + 2], sm[j + 6]))) * (1.f / 448.f);
            const float var = fmaxf(rsum(sw16(sw32(sq[j], sq[j + 4]), sw32(sq[j + 2], sq[j + 6]))) * (1.f / 448.f) - mean * mean, 0.f);
            const float mb = rbf(mean), sd_ = rbf(__builtin_sqrtf(rbf(rbf(var) + 1e-5f)));
            a_[j] = 1.f / sd_; c_[j] = -mb * a_[j];
            st_[j] = (__float_as_uint(mb) >> 16) | __float_as_uint(sd_);
        }
        if (fl < 2) { const u2 gr = {fl ? st_[1] : st_[0], tag}; GST2((fl + 2 * g) * 8, gr, ws + WS_STAT + (lb * 64 + wave * 8) * 8, "sc1"); }
#pragma unroll
        for (int r = 0; r < 8; ++r) {
            const float av = __builtin_bit_cast(float, __builtin_amdgcn_readlane(__builtin_bit_cast(int, a_[r & 1]), 16 * (r >> 1)));
            const float cv = __builtin_bit_cast(float, __builtin_amdgcn_readlane(__builtin_bit_cast(int, c_[r & 1]), 16 * (r >> 1)));
            u4 o;
#pragma unroll
            for (int i = 0; i < 4; ++i) o[i] = pkbf(fmaf(bflo(xr[r][i]), av, cv), fmaf(bfhi(xr[r][i]), av, cv));
            if (lane < 56) LDS(u4, (wave * 8 + r) * 928 + lane * 16) = o;
        }
        BARRIER();
#pragma unroll 2
        for (u32 i = 0; i < 14; ++i) {
            const u32 n = wave * 14 + i;
            const u2 v = trread((16 * (n & 3) + 4 * g + (fl >> 2)) * 928 + (n >> 2) * 32 + 8 * (lane & 3));
            const u4 gr = {v.x, tag, v.y, tag};
            GST4(lane * 16, gr, ws + WS_XG + lb * 114688 + n * 1024, "sc1");
        }
        if (tid == 0) { const u2 ar = {tag, tag}; GST2(0, ar, ws + WS_AR + b * 8, "sc1"); }
#pragma unroll 1
        for (u32 k = 2; k < 7; ++k) prefetch(k);
        if (lb == 0 && wave == 0) {
            u4 t0, t1;
#pragma unroll 1
            for (int sp = 0; sp < SPIN_MAX; ++sp) {
                RLD4(t0, lane * 16, ws + WS_AR); RLD4(t1, lane * 16, ws + WS_AR + 1024);
                if (!__any(((t0.x ^ tag) | (t0.z ^ tag) | (t1.x ^ tag) | (t1.z ^ tag)) != 0)) break;
            }
            if (lane == 0) { const u2 e = {tag, tag}; GST2(0, e, ws + WS_EPOCH, "sc1"); }
        }
        asm volatile("s_waitcnt vmcnt(0)" :: "v"(pfd) : "memory");
        return;
    }
    const bool isF = wave < 4;
    const u32 q7 = idx / 7, r7 = idx - 7 * q7;
    const u32 rp = 4 * (xcd >> 1) + q7, cc = 7 * (xcd & 1) + r7, P = 4 * xcd + q7, ct = r7, dp = 28 * xcd + idx;
    const u32 w4 = wave & 3, rg = w4 >> 1, ch = wave & 1, dsel = wave >> 2;
    u32 kt = (r7 + 2 * q7) % 7;
    const int r8 = lane >> 3, p8 = lane & 7, rw = (lane >> 2) & 7;
    u32 vo0 = (32 * w4 + r8) * 896 + 16 * (p8 ^ (r8 >> 1)), vo1 = (vo0 + 8 * 896) ^ 64, vo2 = vo0 + 16 * 896, vo3 = vo1 + 16 * 896;
    u32 vo4 = (16 * w4 + rw) * 3584 + 64 * hh + 16 * ((lane & 3) ^ (rw >> 2)), vo5 = vo4 + 128, vo6 = (vo4 + 8 * 3584) ^ 32, vo7 = vo6 + 128;
    const char *xb = (const char*)x + rp * 114688, *wb = (const char*)w1 + cc * 256;
    const char *pa, *pw;
#define RING_SET() do { asm volatile("" : "+s"(kt)); pa = xb + kt * 128; pw = wb + kt * 229376; kt = kt == 6 ? 0 : kt + 1; } while (0)
#define DMA_I(i, l_) do { if ((i) == 0) dma16(vo0, pa, l_); if ((i) == 1) dma16(vo1, pa, l_ + 1024); \
        if ((i) == 2) dma16(vo2, pa, l_ + 2048); if ((i) == 3) dma16(vo3, pa, l_ + 3072); \
        if ((i) == 4) dma16(vo4, pw, l_ + 16384); if ((i) == 5) dma16(vo5, pw, l_ + 17408); \
        if ((i) == 6) dma16(vo6, pw, l_ + 18432); if ((i) == 7) dma16(vo7, pw, l_ + 19456); } while (0)
#define DMA_ALL(l_) do { DMA_I(0, l_); DMA_I(1, l_); DMA_I(2, l_); DMA_I(3, l_); DMA_I(4, l_); DMA_I(5, l_); DMA_I(6, l_); DMA_I(7, l_); } while (0)
    const u32 lw = L_RING + w4 * 4096;
    u4 sel2, sel3; u32 cv_ub, cv_la, cv_q2, cv_p3, dsk2, hm0, hm1, hm2, hm3;
    auto setup = [&]() {
        const u32 tb = L_TAB + dsel * 4096;
        const int x2 = fl & 7, x3 = (fl + 8 * g) & 15;
        u32 w2_ = (u32)(0x001112222233344ull >> (28 - 4 * x2));
        u32 w3_ = (u32)(0x00000055556aaaffull >> (30 - 2 * x3)) & 0xffff;
        const int m2 = w2_ >> 28, m3 = w3_ >> 14;
        w2_ -= m2 * 0x11111111u; w3_ -= m3 * 0x5555u;
#pragma unroll
        for (int q = 0; q < 4; ++q) {
            sel2[q] = ((w2_ >> (8 * q) & 15) | (w2_ >> (8 * q + 4) & 15) << 16) * 0x0202 + 0x01000100;
            sel3[q] = ((w3_ >> (4 * q) & 3) | (w3_ >> (4 * q + 2) & 3) << 16) * 0x0202 + 0x01000100;
        }
        cv_ub = L_U + dsel * 4608 + 2 * (224 + 16 * fl + 8 * g);
        cv_la = tb + 4 * ((fl & 1) * 304 + ((16 - fl + 8 * g) >> 1));
        cv_q2 = tb + 2432 + 8 * (2 * (2 + (fl >> 3) - g) + m2 - 6);
        cv_p3 = tb + 3456 + 4 * (2 * (1 + ((fl - 8 * g) >> 4)) + m3 - 2);
        PIN(sel2); PIN(sel3); PIN(cv_ub); PIN(cv_la); PIN(cv_q2); PIN(cv_p3);
    };
#define SCALARS_ISSUE() do { asm volatile("s_load_dword %0, %1, 0x0" : "=s"(tag) : "s"(ws)); asm volatile("s_load_dword %0, %1, 0x0" : "=s"(dsk2) : "s"((const char*)Dsk + dp * 4)); } while (0)
#define SCALARS_DONE() do { tag += 1; const u32 pat = 1 + tag % 254; hm0 = (pat & 1) << 15 | (pat & 2) << 30; hm1 = (pat & 4) << 13 | (pat & 8) << 28; \
        hm2 = (pat & 16) << 11 | (pat & 32) << 26; hm3 = (pat & 64) << 9 | (pat & 128) << 24; } while (0)
    const char* hb = ws + WS_H + ((2 * rp + rg) * 112 + 8 * cc + 4 * ch) * 2048;
    const u32 vw2 = r8 * 896 + 16 * (p8 ^ (4 * ((r8 >> 1) & 1)));
    const char* w2b = (const char*)w2 + wave * (224 * 896) + ct * 128;
    if (isF) {
        SCALARS_ISSUE();
        __builtin_amdgcn_s_setprio(2); RING_SET(); DMA_ALL(lw); __builtin_amdgcn_s_setprio(1); RING_SET(); DMA_ALL(lw + 32768);
        __builtin_amdgcn_s_setprio(0); RING_SET(); DMA_ALL(lw + 65536);
        setup();
        asm volatile("s_waitcnt lgkmcnt(0)" : "+s"(tag), "+s"(dsk2) :: "memory");
        SCALARS_DONE();
        STAMP(1);
        f16v acc[2][2]; f4 cs = {0, 0, 0, 0};
#pragma unroll
        for (int i = 0; i < 16; ++i) { acc[0][0][i] = 0; acc[0][1][i] = 0; acc[1][0][i] = 0; acc[1][1][i] = 0; }
        const u32 o1 = ((g ^ lane) & 1) ? 0 : 0x3f803f80u; const u4 ones = {o1, o1, o1, o1};
        u32 ax0 = L_RING + (64 * rg + l31) * 128 + 16 * (hh ^ ((l31 >> 1) & 7)), ax1 = ax0 ^ 32, ax2 = ax0 ^ 64, ax3 = ax0 ^ 96;
        u32 aw0 = L_RING + 16384 + 2048 * hh + 1024 * ch + 512 * rg + 64 * (fl >> 2) + 16 * ((2 * (g & 1) + ((lane & 3) >> 1)) ^ (2 * hh)) + 8 * (lane & 1);
        u32 aw1 = aw0 ^ 272, aw2 = aw0 ^ 512, aw3 = aw0 ^ 784;
        u4 X0[4], W0[4], X1[4], W1[4];
#define G1_RD(i, X, W, a0, a1, o) do { if ((i) == 0) { X[0] = LDS(u4, a0); W[0] = trread2(aw0 + (o), aw1 + (o)); } \
        if ((i) == 1) { X[1] = LDS(u4, a0 + 4096); W[1] = trread2(aw2 + (o), aw3 + (o)); } \
        if ((i) == 2) { X[2] = LDS(u4, a1); W[2] = trread2(aw0 + (o) + 4096, aw1 + (o) + 4096); } \
        if ((i) == 3) { X[3] = LDS(u4, a1 + 4096); W[3] = trread2(aw2 + (o) + 4096, aw3 + (o) + 4096); } } while (0)
#define G1_PHASE(X, W, RD, D0) do { \
        acc[0][0] = mfma32(W[0], X[0], acc[0][0]); RD(0); SB(); \
        acc[0][1] = mfma32(W[0], X[1], acc[0][1]); RD(1); SB(); \
        acc[1][0] = mfma32(W[1], X[0], acc[1][0]); DMA_I(D0, ld); SB(); \
        acc[1][1] = mfma32(W[1], X[1], acc[1][1]); RD(2); SB(); \
        cs = mfma(W[0], ones, cs); DMA_I(D0 + 1, ld); SB(); \
        acc[0][0] = mfma32(W[2], X[2], acc[0][0]); RD(3); SB(); \
        acc[0][1] = mfma32(W[2], X[3], acc[0][1]); DMA_I(D0 + 2, ld); SB(); \
        acc[1][0] = mfma32(W[3], X[2], acc[1][0]); SB(); \
        acc[1][1] = mfma32(W[3], X[3], acc[1][1]); DMA_I(D0 + 3, ld); SB(); \
        cs = mfma(W[2], ones, cs); SB(); } while (0)
#define RDA(i) G1_RD(i, X1, W1, ax2, ax3, 8192)
#define RDB(i) G1_RD(i, X0, W0, ax0, ax1, 0)
        asm volatile("s_waitcnt vmcnt(16)" : "+v"(pfd) :: "memory");
        BARRIER();
        RDB(0); RDB(1); RDB(2); RDB(3);
        STAMP(3);
#pragma unroll 1
        for (u32 s = 0; s < 7; ++s) {
            RING_SET();
            if (s >= 4) pa = pw = w2b + (s < 6 ? 4 * 224 * 896 + (s - 4) * 57344 : 0);
            if (s == 4) { vo0 = vw2; vo1 = vw2 + 7168; vo2 = vw2 + 14336; vo3 = vw2 + 21504; vo4 = vw2 + 28672; vo5 = vw2 + 35840; vo6 = vw2 + 43008; vo7 = vw2 + 50176; }
            const u32 ld = lw + ((s + 3) & 3) * 32768;
            SB();
            G1_PHASE(X0, W0, RDA, 0);
            { const u32 m_ = (0x8000u * (((s & 3) + 1) ^ (((s + 1) & 3) + 1)));
              ax0 ^= m_; ax1 ^= m_; ax2 ^= m_; ax3 ^= m_; aw0 ^= m_; aw1 ^= m_; aw2 ^= m_; aw3 ^= m_; }
            if (s < 6) { ACC_BEGIN(); WAITV(12); ACC_END(20); BARRIER(); ACC_END(21); }
            SB();
            G1_PHASE(X1, W1, RDB, 4);
        }
        if (fl < 2) LDS(f4, L_CS + (64 * ch + 32 * rg + 16 * fl + 4 * g) * 4) = cs;
        STAMP(4);
        BARRIER();
        pa = pw = w2b + 57344;
        { const u32 ld = lw + 65536; DMA_ALL(ld); }
#pragma unroll
        for (int tt = 0; tt < 2; ++tt) {
            const u32 sv = LDS(u32, L_ST + (64 * rg + 32 * tt + l31) * 4);
            const float m = -bflo(sv), rs = __builtin_amdgcn_rcpf(bfhi(sv));
            const f2 m2 = {m, m}, rs2 = {rs, rs};
#pragma unroll
            for (int c = 0; c < 2; ++c)
#pragma unroll
                for (int k = 0; k < 2; ++k) {
                    u4 o;
#pragma unroll
                    for (int q = 0; q < 2; ++q) {
                        const f4 cv = LDS(f4, L_CS + ((64 * ch + 32 * rg + 4 * hh) * 4 ^ (128 * c)) + (2 * k + q) * 32), av = quad(acc[c][tt], 2 * k + q);
                        const f2 v0 = __builtin_elementwise_fma(m2, f2{cv.x, cv.y}, f2{av.x, av.y}) * rs2, v1 = __builtin_elementwise_fma(m2, f2{cv.z, cv.w}, f2{av.z, av.w}) * rs2;
                        o[2 * q] = relu2(pkbf(v0.x, v0.y)) | (q ? hm2 : hm0); o[2 * q + 1] = relu2(pkbf(v1.x, v1.y)) | (q ? hm3 : hm1);
                    }
                    const char* hp = hb + (2 * (c ^ rg) + k) * 2048;
                    if (tt) GST4(lane * 16, o, hp, "offset:1024 sc1"); else GST4(lane * 16, o, hp, "sc1");
                }
        }
        STAMP(5);
        WAITV(24);
    } else {
        __builtin_amdgcn_s_setprio(3);
        u32 pq0, pq1, pq2, pq3, pq4, pq5, ldt2;
        UNDEF(pq0); UNDEF(pq1); UNDEF(pq2); UNDEF(pq3); UNDEF(pq4); UNDEF(pq5);
        { const u32 po = (2 * dp + rg) * 512 + ch * 256 + lane * 4;
          GLD1(pq0, po, Are); GLD1(pq1, po, Aim); GLD1(pq2, po, Bre); GLD1(pq3, po, Bim); GLD1(pq4, po, Cre); GLD1(pq5, po, Cim); }
        SCALARS_ISSUE();
        asm volatile("s_load_dword %0, %1, 0x0" : "=s"(ldt2) : "s"((const char*)logdt + dp * 4));
        __builtin_amdgcn_s_setprio(1);
        setup();
        u32 tix[8];
        {
            const int t = tid & 127;
#pragma unroll
            for (int i = 0; i < 5; ++i) {
                const int u = min(t + 128 * i, 607), pi = u >= 304, ip = u - 304 * pi, top = (ip < 24 ? 1056 : 592) - pi - 2 * ip;
                tix[i] = cidx(top) * 2 | cidx(top - 1) << 17;
            }
            tix[5] = (513 + min(t, 126)) * 0x20002 + 0x20000;
            tix[6] = (388 + (t >> 1) + 2 * (t & 1)) * 0x20002 + 0x20000;
            tix[7] = tix[6] + 64 * 0x20002;
#pragma unroll
            for (int i = 0; i < 8; ++i) PIN(tix[i]);
        }
        asm volatile("s_waitcnt lgkmcnt(0)" : "+s"(tag), "+s"(dsk2), "+s"(ldt2) :: "memory");
        SCALARS_DONE();
        STAMP(1);
        const float dt = rbf(expf(rg ? bfhi(ldt2) : bflo(ldt2)));
        u32 sr = L_SCR + w4 * 4352 + fl * 272 + g * 16; PIN(sr);
        asm volatile("s_mov_b32 m0, %0" :: "s"(L_SCR + w4 * 4352));
        asm volatile("s_waitcnt vmcnt(0)" : "+v"(pq0), "+v"(pq1), "+v"(pq2), "+v"(pq3), "+v"(pq4), "+v"(pq5) :: "memory");
        if (tid == 256) { const u2 ar = {tag, tag}; GST2(0, ar, ws + WS_AR + b * 8, "sc1"); }
        STAMP(2);
        f4 dk[4];
#pragma unroll
        for (int t = 0; t < 4; ++t) dk[t] = f4{0, 0, 0, 0};
        u2 stv; u4 xg0, xg1, xg2, xg3;
        const u32 xo = ((tid & 255) >> 5) * 114688 + ((tid >> 1) & 15) * 256 + (tid & 1) * 16;
        const char* xgb = ws + WS_XG + (dp >> 3) * 4096 + (dp & 7) * 32;
#define SBAR() do { SB(); asm volatile("s_barrier" ::: "memory"); } while (0)
#pragma unroll
        for (int ps = 0; ps < 2; ++ps) {
#define PAR(v) (ps ? bfhi(v) : bflo(v))
            const float a2 = fmul1(dt, PAR(pq0)) * 1.44269504089f, rev = fmul1(dt, PAR(pq1)) * 0.15915494309f;
            const f2 bb = {fmul1(dt, PAR(pq2)), fmul1(dt, PAR(pq3))}, cv = {PAR(pq4), -PAR(pq5)};
            const f2 c = cmulc(cv, bb);
            const f2 z1 = zpow(a2, rev, 1.f), z8 = zpow(a2, rev, 8.f), z64 = zpow(a2, rev, 64.f), z256 = zpow(a2, rev, 256.f);
            const f2 z2 = cmul(z1, z1), z4 = cmul(z2, z2);
            const f2 z32 = zpow(a2, rev, 32.f), z128 = cmul(z64, z64);
            f2 e1 = cmulc(c, z256), e2 = cmulc(e1, z256), e3 = cmulc(e2, z256);
#define PUT(e, v) do { const u32 p_ = pkbf((v).x, (v).y); asm volatile("ds_write_addtid_b32 %0 offset:%1" :: "v"(p_), "i"((e) * 272) : "memory"); } while (0)
            f2 t = c, t4 = cmulc(c, z4), t8 = e1; PUT(0, t); PUT(4, t4); PUT(8, t8);
            t = cmulc(t, z1); t4 = cmulc(t4, z1); t8 = cmulc(t8, z2); PUT(1, t); PUT(5, t4); PUT(9, t8);
            if (ps == 1) { STAMP(17); SBAR(); }
            t = cmulc(t, z1); t4 = cmulc(t4, z1); t8 = cmulc(t8, z2); PUT(2, t); PUT(6, t4); PUT(10, t8);
            t = cmulc(t, z1); t4 = cmulc(t4, z1); t8 = cmulc(t8, z2); PUT(3, t); PUT(7, t4); PUT(11, t8);
            t = cmulc(e2, z4); t4 = cmulc(e3, z4); PUT(12, e2); PUT(14, e3); e3 = cmulc(e3, z256); PUT(13, t); PUT(15, t4);
            u4 B0[4], B1[4];
#pragma unroll
            for (int s = 0; s < 4; ++s) B0[s] = LDS(u4, sr + s * 64);
            const f2 z512 = cmul(z256, z256);
            e2 = cmulc(e3, z256); PUT(0, e3); PUT(1, e2); e3 = cmulc(e3, z512); e2 = cmulc(e2, z512); PUT(2, e3); PUT(3, e2); e3 = cmulc(e3, z512); PUT(4, e3);
#pragma unroll
            for (int s = 0; s < 4; ++s) B1[s] = LDS(u4, sr + s * 64);
            if (ps == 0) STAMP(3); else STAMP(18);
            SBAR();
            f2 r0 = {1.f, 0.f}, r1 = z32, r2 = z64, r3 = cmul(z64, z32);
#pragma unroll
            for (int h = 0; h < 2; ++h) {
                f2 t0 = r0, t1 = r1, t2 = r2, t3 = r3;
                PUT(0, t0); PUT(4, t1); PUT(8, t2); PUT(12, t3);
#define STEP(j) t0 = cmul(t0, z8); t1 = cmul(t1, z8); t2 = cmul(t2, z8); t3 = cmul(t3, z8); PUT(j, t0); PUT(4 + j, t1); PUT(8 + j, t2); PUT(12 + j, t3);
                STEP(1) STEP(2) STEP(3)
                u4 A[4];
#pragma unroll
                for (int s = 0; s < 4; ++s) A[s] = LDS(u4, sr + s * 64);
                asm volatile("" ::: "memory");
                if (h == 0) { r0 = z128; r1 = cmul(r1, z128); r2 = cmul(r2, z128); r3 = cmul(r3, z128); }
#pragma unroll
                for (int s = 0; s < 4; ++s) { dk[2 * h] = mfma(A[s], B0[s], dk[2 * h]); dk[2 * h + 1] = mfma(A[s], B1[s], dk[2 * h + 1]); }
                if (ps == 1 && h == 0) {
                    UNDEF(stv); UNDEF(xg0); UNDEF(xg1); UNDEF(xg2); UNDEF(xg3);
                    GLD2(stv, (tid & 127) * 8, ws + WS_STAT + rp * 1024, "sc1");
                    GLD4(xg0, xo, xgb, "sc1"); GLD4(xg1, xo, xgb + 8 * 114688, "sc1"); GLD4(xg2, xo, xgb + 16 * 114688, "sc1"); GLD4(xg3, xo, xgb + 24 * 114688, "sc1");
                }
                if (ps == 0) STAMP(15 + h); else if (h == 0) STAMP(19);
                if (ps == 1 && h == 1) {
                    const u32 sw = L_SCR + w4 * 4352 + lane * 16;
                    LDS(f4, sw) = self4(ch, dk[0], dk[2]); LDS(f4, sw + 1024) = self4(ch, dk[1], dk[3]);
                    SB(); BARRIER();
                } else SBAR();
            }
        }
        STAMP(12);
        {
            const u32 a = L_SCR + (w4 ^ 1) * 4352 + lane * 16;
#pragma unroll
            for (int jt = 0; jt < 2; ++jt) {
                const f4 s = self4(ch, dk[2 + jt], dk[jt]) + LDS(f4, a + jt * 1024);
                const int j = jt * 16 + fl, i0 = ch * 16 + 4 * g;
                const int mul = jt ? 1 : j < 8 ? 8 : j < 12 ? 4 : 2;
                const int base = jt ? 512 + 32 * fl : j < 8 ? j : j < 12 ? 248 + j : 384 + (j & 1) + 64 * ((j - 12) >> 1);
                const u32 o = L_KC + rg * 1536 + (base + mul * i0) * 2;
                const u32 p01 = pkbf(s.x, s.y), p23 = pkbf(s.z, s.w);
                if (jt == 0) { LDS(u16, o) = (u16)p01; LDS(u16, o + mul * 2) = (u16)(p01 >> 16); LDS(u16, o + mul * 4) = (u16)p23; LDS(u16, o + mul * 6) = (u16)(p23 >> 16); }
                else if (fl < 4) LDS(u2, o) = u2{p01, p23};
                else if (fl == 4 && i0 == 0) LDS(u32, o) = p01 & 0xffff;
            }
            asm volatile("s_waitcnt vmcnt(4)" : "+v"(stv) :: "memory");
#pragma unroll 1
            for (int sp = 0; __any(stv.y != tag) && sp < SPIN_MAX; ++sp) RLD2(stv, (tid & 127) * 8, ws + WS_STAT + rp * 1024);
            if (w4 < 2) LDS(u32, L_ST + (tid & 127) * 4) = stv.x;
        }
        STAMP(4);
        BARRIER();
        {
            u32 kc = L_KC + rg * 1536, td = L_TAB + rg * 4096 + (tid & 127) * 4; PIN(td);
            u32 v[8];
#pragma unroll
            for (int i = 0; i < 8; ++i) v[i] = (u32)LDS(u16, kc + (tix[i] & 0xffff)) | ((u32)LDS(u16, kc + (tix[i] >> 16)) << 16);
#pragma unroll
            for (int i = 0; i < 4; ++i) LDS(u32, td + 512 * i) = v[i];
            if ((tid & 127) < 96) LDS(u32, td + 2048) = v[4];
            LDS(u32, td + 3456) = v[5]; LDS(u32, td + 2432) = v[6]; LDS(u32, td + 2944) = v[7];
        }
        {
            asm volatile("s_waitcnt vmcnt(0)" : "+v"(xg0), "+v"(xg1), "+v"(xg2), "+v"(xg3) :: "memory");
#define XG_FIX(v, i) _Pragma("unroll 1") for (int sp = 0; __any(v.y != tag || v.w != tag) && sp < SPIN_MAX; ++sp) RLD4(v, xo, xgb + (i) * 8 * 114688)
            if (__any(((xg0.y ^ tag) | (xg0.w ^ tag) | (xg1.y ^ tag) | (xg1.w ^ tag) | (xg2.y ^ tag) | (xg2.w ^ tag) | (xg3.y ^ tag) | (xg3.w ^ tag)) != 0)) {
                XG_FIX(xg0, 0); XG_FIX(xg1, 1); XG_FIX(xg2, 2); XG_FIX(xg3, 3);
            }
            u32 a = L_U + 480 + (tid & 1) * 4608 + ((tid & 255) >> 1) * 8; PIN(a);
            LDS(u2, a) = u2{xg0.x, xg0.z}; LDS(u2, a + 1024) = u2{xg1.x, xg1.z}; LDS(u2, a + 2048) = u2{xg2.x, xg2.z}; LDS(u2, a + 3072) = u2{xg3.x, xg3.z};
            if ((tid & 255) < 120) LDS(u2, a - 480) = u2{0, 0};
        }
        __builtin_amdgcn_s_setprio(0);
        STAMP(5);
    }
    BARRIER();
    STAMP(6);
    const u32 pb0 = lw + (isF ? 32768 : 98304), pb1 = lw + (isF ? 65536 : 0);
    u4 Bw[14][2];
    u32 aw2a = pb0 + (4 * hh + (fl >> 2)) * 128 + 16 * ((2 * (g & 1) + ((lane & 3) >> 1)) ^ (4 * ((fl >> 3) & 1))) + 8 * (lane & 1);
    u32 aw2b = aw2a ^ 64, aw2c = aw2a - pb0 + pb1, aw2d = aw2c ^ 64;
#define W2_RD(j, p, n) do { const u32 o_ = ((n) >> 1) * 16384 + ((n) & 1) * 2048; \
        Bw[j][0] = trread2(((p) ? aw2c : aw2a) + o_, ((p) ? aw2c : aw2a) + o_ + 1024); Bw[j][1] = trread2(((p) ? aw2d : aw2b) + o_, ((p) ? aw2d : aw2b) + o_ + 1024); } while (0)
    f4 cacc[4];
    u4 hA0, hA1, hA2, hA3, hA4, hA5, hA6, hB0, hB1, hB2, hB3, hB4, hB5, hB6;
    const char* hrd = ws + WS_H + (P * 112 + wave * 14) * 2048;
#define H_LOAD(h, n) do { const char* p_ = hrd + ((n) & 1) * 14336 + ((n) >> 1) * 1024; GLD4(h##0, lane * 16, p_, "sc1"); GLD4(h##1, lane * 16, p_, "offset:2048 sc1"); \
        GLD4(h##2, lane * 16, p_ + 4096, "sc1"); GLD4(h##3, lane * 16, p_ + 4096, "offset:2048 sc1"); GLD4(h##4, lane * 16, p_ + 8192, "sc1"); \
        GLD4(h##5, lane * 16, p_ + 8192, "offset:2048 sc1"); GLD4(h##6, lane * 16, p_ + 12288, "sc1"); } while (0)
#define H_UNDEF(h) do { UNDEF(h##0); UNDEF(h##1); UNDEF(h##2); UNDEF(h##3); UNDEF(h##4); UNDEF(h##5); UNDEF(h##6); } while (0)
    {
        const int kh = (wave >> 1) & 1, ts = wave & 1;
#pragma unroll
        for (int i = 0; i < 4; ++i) cacc[i] = f4{0, 0, 0, 0};
        if (ts) conv_pair<1>(cacc, 4 * kh, cv_ub, cv_la, cv_q2, cv_p3, sel2, sel3); else conv_pair<0>(cacc, 4 * kh, cv_ub, cv_la, cv_q2, cv_p3, sel2, sel3);
        STAMP(13);
        WAITV(8);
        W2_RD(0, 0, 0); W2_RD(1, 0, 1); W2_RD(2, 0, 2); W2_RD(3, 0, 3); W2_RD(4, 1, 0); W2_RD(5, 1, 1); W2_RD(6, 1, 2); W2_RD(7, 1, 3);
        asm volatile("s_waitcnt lgkmcnt(0)" ::: "memory");
#pragma unroll 1
        for (u32 i = 0; i < 12; ++i) dma16(vw2, w2b + (16 + i) * 7168, (i < 8 ? pb0 : pb1) + ((i >> 2) & 1) * 16384 + (i & 3) * 1024);
        STAMP(14);
        if (ts) conv_pair<1>(cacc, 4 * kh + 2, cv_ub, cv_la, cv_q2, cv_p3, sel2, sel3); else conv_pair<0>(cacc, 4 * kh + 2, cv_ub, cv_la, cv_q2, cv_p3, sel2, sel3);
        STAMP(7);
        H_UNDEF(hA); H_UNDEF(hB);
        H_LOAD(hA, 0); H_LOAD(hB, 1);
        BARRIER();
        u32 ra = wave * 2048 + lane * 16; PIN(ra);
        LDS(f4, ra) = self4(kh, cacc[0], cacc[2]);
        LDS(f4, ra + 1024) = self4(kh, cacc[1], cacc[3]);
        ra ^= 4096;
        BARRIER();
        const float dk = dsel ? bfhi(dsk2) : bflo(dsk2);
#pragma unroll
        for (int i = 0; i < 2; ++i) {
            const int tau = kh ? (ts ? 5 + i : 4 + 3 * i) : (ts ? 1 + i : 3 * i);
            const f4 y = self4(kh, cacc[2 + i], cacc[i]) + LDS(f4, ra + i * 1024);
            const u2 uu = LDS(u2, L_U + dsel * 4608 + 480 + (256 * tau + 16 * fl + 4 * g) * 2);
            const u32 y01 = pkbf(y.x, y.y), y23 = pkbf(y.z, y.w);
            const u32 s01 = pkbf(bflo(uu.x) * dk, bfhi(uu.x) * dk), s23 = pkbf(bflo(uu.y) * dk, bfhi(uu.y) * dk);
            const u4 o = {pkbf(bflo(y01) + bflo(s01), bfhi(y01) + bfhi(s01)), tag, pkbf(bflo(y23) + bflo(s23), bfhi(y23) + bfhi(s23)), tag};
            GST4((fl >> 2) * 114688 + ((fl & 3) + 4 * g) * 16, o, ws + WS_YG + (4 * tau * 448 + 2 * dp + dsel) * 256, "sc1");
        }
    }
    STAMP(8);
    WAITV(16);
    W2_RD(8, 0, 0); W2_RD(9, 0, 1); W2_RD(10, 0, 2); W2_RD(11, 0, 3); W2_RD(12, 1, 0); W2_RD(13, 1, 1);
    u2 yv[8], xv[2];
    u32 pw0 = pb0 + lane * 16, pw1 = pb1 + lane * 16; PIN(pw0); PIN(pw1);
#define OR4(v) ((v.x | v.y) | (v.z | v.w))
#define H_WAIT(h, NW) asm volatile("s_waitcnt vmcnt(" #NW ")" : "+v"(h##0), "+v"(h##1), "+v"(h##2), "+v"(h##3), "+v"(h##4), "+v"(h##5), "+v"(h##6) :: "memory")
#define UNM(v) do { v.x ^= hm0; v.y ^= hm1; v.z ^= hm2; v.w ^= hm3; } while (0)
#define H_UNM(h) do { UNM(h##0); UNM(h##1); UNM(h##2); UNM(h##3); UNM(h##4); UNM(h##5); UNM(h##6); } while (0)
#define H_OR(h) (OR4(h##0) | OR4(h##1) | OR4(h##2) | OR4(h##3) | OR4(h##4) | OR4(h##5) | OR4(h##6))
#define H_MM1(h, j, o) do { a0 = mfma32(Bw[(o) + j][0], h##j, a0); a1 = mfma32(Bw[(o) + j][1], h##j, a1); } while (0)
#define H_MMA(h, o) do { H_MM1(h, 0, o); H_MM1(h, 1, o); H_MM1(h, 2, o); H_MM1(h, 3, o); H_MM1(h, 4, o); H_MM1(h, 5, o); H_MM1(h, 6, o); } while (0)
#define H_ZERO() _Pragma("unroll") for (int i = 0; i < 16; ++i) { a0[i] = 0; a1[i] = 0; }
#define H_PUT(pw) _Pragma("unroll") for (int q = 0; q < 4; ++q) { LDS(f4, (pw) + q * 1024) = quad(a0, q); LDS(f4, (pw) + 16384 + q * 1024) = quad(a1, q); }
    u32 hbad = 0;
    f16v a0, a1;
    H_ZERO();
    H_WAIT(hA, 9); H_UNM(hA); hbad |= H_OR(hA); PIN(hbad); H_MMA(hA, 0);
    STAMP(9);
    H_LOAD(hA, 2);
    H_WAIT(hB, 9); H_UNM(hB); hbad |= H_OR(hB); PIN(hbad); H_MMA(hB, 7); H_PUT(pw0);
    H_LOAD(hB, 3);
    H_ZERO();
    H_WAIT(hA, 7); H_UNM(hA); hbad |= H_OR(hA); PIN(hbad); H_MMA(hA, 0);
    const u32 dq = 32 * rg + 16 * ch + 4 * hh;
    const u32 yo = dq * 256 + (2 * dsel + (g & 1) + 4 * ((lane >> 2) & 3)) * 16 + ((lane >> 1) & 1) * 8;
    const char* yb = ws + WS_YG + (P * 448 + 64 * ct) * 256;
#define Y_LOAD() do { GLD2(yv[0], yo, yb, "sc1"); GLD2(yv[1], yo, yb, "offset:256 sc1"); GLD2(yv[2], yo, yb, "offset:512 sc1"); GLD2(yv[3], yo, yb, "offset:768 sc1"); \
        GLD2(yv[4], yo, yb, "offset:2048 sc1"); GLD2(yv[5], yo, yb, "offset:2304 sc1"); GLD2(yv[6], yo, yb, "offset:2560 sc1"); GLD2(yv[7], yo, yb, "offset:2816 sc1"); } while (0)
    const u32 xo = (32 * dsel + l31) * 896 + dq * 2;
#pragma unroll
    for (int i = 0; i < 8; ++i) UNDEF(yv[i]);
    UNDEF(xv[0]); UNDEF(xv[1]);
    Y_LOAD();
    GLD2(xv[0], xo, (const char*)x + P * 57344 + ct * 128, ""); GLD2(xv[1], xo, (const char*)x + P * 57344 + ct * 128, "offset:16");
    H_WAIT(hB, 10); H_UNM(hB); hbad |= H_OR(hB); PIN(hbad); H_MMA(hB, 7); H_PUT(pw1);
    if (__any(hbad & 0x80008000u)) {
#pragma unroll 1
        for (int r = 0; r < 2; ++r) {
            H_ZERO();
#define H_FIX(j) { u4 t; _Pragma("unroll 1") for (int sp = 0; sp < SPIN_MAX; ++sp) { RLD4(t, lane * 16, hrd + r * 1024 + (j) * 2048); UNM(t); if (!__any(OR4(t) & 0x80008000u)) break; } \
                a0 = mfma32(Bw[j][0], t, a0); a1 = mfma32(Bw[j][1], t, a1); }
            H_FIX(0) H_FIX(1) H_FIX(2) H_FIX(3) H_FIX(4) H_FIX(5) H_FIX(6) H_FIX(7) H_FIX(8) H_FIX(9) H_FIX(10) H_FIX(11) H_FIX(12) H_FIX(13)
            H_PUT(r ? pw1 : pw0);
        }
    }
    BARRIER();
    STAMP(10);
    f4 r2[2];
    {
        u32 aF = L_RING + (dsel ? 65536 : 32768) + rg * 16384 + ch * 2048 + lane * 16, aS = L_RING + (dsel ? 0 : 98304) + rg * 16384 + ch * 2048 + lane * 16;
        PIN(aF); PIN(aS);
#pragma unroll
        for (int i = 0; i < 2; ++i)
            r2[i] = ((LDS(f4, aF + i * 1024) + LDS(f4, aF + i * 1024 + 4096)) + (LDS(f4, aF + i * 1024 + 8192) + LDS(f4, aF + i * 1024 + 12288))) +
                    ((LDS(f4, aS + i * 1024) + LDS(f4, aS + i * 1024 + 4096)) + (LDS(f4, aS + i * 1024 + 8192) + LDS(f4, aS + i * 1024 + 12288)));
    }
    asm volatile("s_waitcnt vmcnt(0)" : "+v"(yv[0]), "+v"(yv[1]), "+v"(yv[2]), "+v"(yv[3]), "+v"(yv[4]), "+v"(yv[5]), "+v"(yv[6]), "+v"(yv[7]), "+v"(xv[0]), "+v"(xv[1]) :: "memory");
    if (__any(((yv[0].y ^ tag) | (yv[1].y ^ tag) | (yv[2].y ^ tag) | (yv[3].y ^ tag) | (yv[4].y ^ tag) | (yv[5].y ^ tag) | (yv[6].y ^ tag) | (yv[7].y ^ tag)) != 0)) {
#define Y_FIX(i) _Pragma("unroll 1") for (int sp = 0; __any(yv[i].y != tag) && sp < SPIN_MAX; ++sp) RLD2(yv[i], yo, yb + ((i) & 3) * 256 + ((i) >> 2) * 2048)
        Y_FIX(0); Y_FIX(1); Y_FIX(2); Y_FIX(3); Y_FIX(4); Y_FIX(5); Y_FIX(6); Y_FIX(7);
    }
#pragma unroll
    for (int i = 0; i < 2; ++i) {
        float yy[4];
#pragma unroll
        for (int r = 0; r < 4; ++r) yy[r] = (lane & 1) ? bfhi(yv[4 * i + r].x) : bflo(yv[4 * i + r].x);
        const u32 a01 = pkbf(bflo(xv[i].x) + yy[0], bfhi(xv[i].x) + yy[1]), a23 = pkbf(bflo(xv[i].y) + yy[2], bfhi(xv[i].y) + yy[3]);
        const u32 f01 = pkbf(r2[i].x, r2[i].y), f23 = pkbf(r2[i].z, r2[i].w);
        const u2 o = {pkbf(bflo(a01) + bflo(f01), bfhi(a01) + bfhi(f01)), pkbf(bflo(a23) + bflo(f23), bfhi(a23) + bfhi(f23))};
        *(u2*)((char*)out + P * 57344 + ct * 128 + xo + i * 16) = o;
    }
    STAMP(11); STAMPC(23);
    STAMP_FLUSH(24);
}

__global__ void kA(char* ws) { if (threadIdx.x == 0) *(u32*)(ws + WS_AR + 8192) = (u32)__builtin_amdgcn_s_memrealtime(); }
__global__ __launch_bounds__(512, 2) void kX(char* ws) { const u32 t = (u32)__builtin_amdgcn_s_memrealtime(); if (0) asm volatile("v_mov_b32 v215, 0" ::: "v215"); if (threadIdx.x == 0) ((u32*)(ws + WS_AR + 12288))[blockIdx.x] = t; }
__global__ void kB(char* ws) {
    const u32 ta = *(u32*)(ws + WS_AR + 8192);
    int t = threadIdx.x;
    int st = (int)(((u32*)(ws + WS_AR + 12288))[t < 256 ? t : 0] - ta);
    __shared__ int smn; if (t == 0) smn = 1 << 30; __syncthreads();
    atomicMin(&smn, st); __syncthreads();
    u64 e_ = __builtin_amdgcn_s_memrealtime() + (u64)smn * 10; while (__builtin_amdgcn_s_memrealtime() < e_) __builtin_amdgcn_s_sleep(8);
}

extern "C" void kernel_launch(void* const* d_in, const int* in_sizes, int n_in, void* d_out, int out_size, void* d_ws, size_t ws_size,
                              hipStream_t stream) {
    (void)hipFuncSetAttribute((const void*)s4ffn_kernel, hipFuncAttributeMaxDynamicSharedMemorySize, LDS_BYTES);
    (void)hipFuncSetAttribute((const void*)kX, hipFuncAttributeMaxDynamicSharedMemorySize, LDS_BYTES);
    kA<<<1, 64, 0, stream>>>((char*)d_ws);
    kX<<<256, 512, 65536, stream>>>((char*)d_ws);
    s4ffn_kernel<<<256, 512, LDS_BYTES, stream>>>((const u16*)d_in[0], (const u16*)d_in[1], (const u16*)d_in[2], (const u16*)d_in[3],
        (const u16*)d_in[4], (const u16*)d_in[5], (const u16*)d_in[6], (const u16*)d_in[7], (const u16*)d_in[8],
        (const u16*)d_in[10], (const u16*)d_in[11], (u16*)d_out, (char*)d_ws);
    kB<<<1, 256, 0, stream>>>((char*)d_ws);
}
```

```cpp
#include <hip/hip_runtime.h>
#include <stdint.h>

typedef unsigned short u16;
typedef unsigned int u32;
typedef unsigned long long u64;
typedef __attribute__((ext_vector_type(8))) __bf16 bf8;
typedef __attribute__((ext_vector_type(2))) __bf16 bf2;
typedef __attribute__((ext_vector_type(4))) short s4;
typedef __attribute__((ext_vector_type(4))) float f4;
typedef __attribute__((ext_vector_type(16))) float f16v;
typedef __attribute__((ext_vector_type(2))) float f2;
typedef __attribute__((ext_vector_type(4))) u32 u4;
typedef __attribute__((ext_vector_type(2))) u32 u2;
#define LAS __attribute__((address_space(3)))
#define DI __device__ __forceinline__
#define LDS(T, off) (*(LAS T*)(uintptr_t)(u32)(off))

constexpr u32 WS_EPOCH = 0;
constexpr u32 WS_STAT = 4096;
constexpr u32 WS_AR = WS_STAT + 16384;
constexpr u32 WS_XG = 65536;
constexpr u32 WS_YG = WS_XG + 3670016;
constexpr u32 WS_H = WS_YG + 3670016;
constexpr u32 WS_STAMP = 32u << 20;
constexpr u32 L_TAB = 0;
constexpr u32 L_XT = 4096;
constexpr u32 L_KC = 8192;
constexpr u32 L_CS = 11264;
constexpr u32 L_ST = 11776;
constexpr u32 L_SEL = 12288;
constexpr u32 L_SCR = 15360;
constexpr u32 L_U = 23552;
constexpr u32 L_RING = 32768;
constexpr u32 LDS_BYTES = 163840;
constexpr int SPIN_MAX = 1 << 16;
constexpr int PF_LINES = 6;

#define STAMP(i)
#define STAMPC(i)
#define STAMPF(i)
#define ACC_BEGIN()
#define ACC_END(i)
#define STAMP_DECL
#define STAMP_FLUSH(n)

DI float bflo(u32 v) { return __uint_as_float(v << 16); }
DI float bfhi(u32 v) { return __uint_as_float(v & 0xffff0000u); }
DI u32 pkbf(float a, float b) { f2 v = {a, b}; return __builtin_bit_cast(u32, __builtin_convertvector(v, bf2)); }
DI float rbf(float a) { return bflo(pkbf(a, a)); }
DI f4 mfma(u4 a, u4 b, f4 c) { return __builtin_amdgcn_mfma_f32_16x16x32_bf16(__builtin_bit_cast(bf8, a), __builtin_bit_cast(bf8, b), c, 0, 0, 0); }
DI f16v mfma32(u4 a, u4 b, f16v c) { return __builtin_amdgcn_mfma_f32_32x32x16_bf16(__builtin_bit_cast(bf8, a), __builtin_bit_cast(bf8, b), c, 0, 0, 0); }
DI u2 trread(u32 addr) { return __builtin_bit_cast(u2, __builtin_amdgcn_ds_read_tr16_b64_v4i16((LAS s4*)(uintptr_t)addr)); }
DI u4 trread2(u32 a, u32 b) { const u2 l = trread(a), h = trread(b); return u4{l.x, l.y, h.x, h.y}; }
template <int C> DI float dpp(float x) { return __builtin_bit_cast(float, __builtin_amdgcn_mov_dpp(__builtin_bit_cast(int, x), C, 0xf, 0xf, true)); }
DI float rsum(float x) { x += dpp<0xB1>(x); x += dpp<0x4E>(x); x += dpp<0x141>(x); x += dpp<0x140>(x); return x; }
DI float rmax(float x) { x = fmaxf(x, dpp<0xB1>(x)); x = fmaxf(x, dpp<0x4E>(x)); x = fmaxf(x, dpp<0x141>(x)); return fmaxf(x, dpp<0x140>(x)); }
DI float sw32(float a, float b) { auto s = __builtin_amdgcn_permlane32_swap(__float_as_uint(a), __float_as_uint(b), false, false); return __uint_as_float(s[0]) + __uint_as_float(s[1]); }
DI float sw16(float a, float b) { auto s = __builtin_amdgcn_permlane16_swap(__float_as_uint(a), __float_as_uint(b), false, false); return __uint_as_float(s[0]) + __uint_as_float(s[1]); }
DI float dot2(u32 a, u32 b, float c) { return __builtin_amdgcn_fdot2_f32_bf16(__builtin_bit_cast(bf2, a), __builtin_bit_cast(bf2, b), c, false); }
DI f4 self4(int c, f4 a, f4 b) { return c ? a : b; }
DI f2 cmul(f2 a, f2 b) { float x, y;
    asm("v_mul_f32 %0, %2, %4\n\tv_mul_f32 %1, %3, %4\n\tv_fma_f32 %0, -%3, %5, %0\n\tv_fmac_f32 %1, %2, %5" : "=&v"(x), "=&v"(y) : "v"(a.x), "v"(a.y), "v"(b.x), "v"(b.y));
    return f2{x, y}; }
DI f2 cmulc(f2 a, f2 b) { float x, y;
    asm("v_mul_f32 %0, %2, %4\n\tv_mul_f32 %1, %3, %4\n\tv_fmac_f32 %0, %3, %5\n\tv_fma_f32 %1, -%2, %5, %1" : "=&v"(x), "=&v"(y) : "v"(a.x), "v"(a.y), "v"(b.x), "v"(b.y));
    return f2{x, y}; }
DI float fmul1(float a, float b) { float r; asm("v_mul_f32 %0, %1, %2" : "=v"(r) : "v"(a), "v"(b)); return r; }
typedef __attribute__((ext_vector_type(2))) _Float16 h2;
typedef __attribute__((ext_vector_type(8))) _Float16 h8;
DI h2 cvth(f2 a) { return __builtin_convertvector(a, h2); }
DI h2 hmul(h2 a, h2 b) { const h2 bx = {b.x, b.x}, by = {-b.y, b.y}; return __builtin_elementwise_fma(__builtin_shufflevector(a, a, 1, 0), by, a * bx); }
DI h2 hmulc(h2 a, h2 b) { const h2 bx = {b.x, b.x}, by = {b.y, -b.y}; return __builtin_elementwise_fma(__builtin_shufflevector(a, a, 1, 0), by, a * bx); }
DI f4 mfmah(u4 a, u4 b, f4 c) { return __builtin_amdgcn_mfma_f32_16x16x32_f16(__builtin_bit_cast(h8, a), __builtin_bit_cast(h8, b), c, 0, 0, 0); }
DI u32 relu2(u32 v) { typedef __attribute__((ext_vector_type(2))) short s2; const s2 z = {0, 0}; return __builtin_bit_cast(u32, __builtin_elementwise_max(__builtin_bit_cast(s2, v), z)); }
DI f2 zpow(float a2, float rev, float p) {
    float e = __builtin_amdgcn_exp2f(a2 * p), r = __builtin_amdgcn_fractf(rev * p);
    float x, y;
    asm("s_nop 0\n\tv_mul_f32 %0, %2, %3\n\tv_mul_f32 %1, %2, %4" : "=&v"(x), "=&v"(y) : "v"(e), "v"(__builtin_amdgcn_cosf(r)), "v"(__builtin_amdgcn_sinf(r)));
    return f2{x, y};
}
DI f4 quad(const f16v& v, int q) { return f4{v[4 * q], v[4 * q + 1], v[4 * q + 2], v[4 * q + 3]}; }

#define SB() __builtin_amdgcn_sched_barrier(0)
#define BARRIER() asm volatile("s_waitcnt lgkmcnt(0)\n\ts_barrier" ::: "memory")
#define WAITV(n) asm volatile("s_waitcnt vmcnt(" #n ")" ::: "memory")
DI void dma16(u32 voff, const void* base, u32 lds) {
    asm volatile("s_mov_b32 m0, %2\n\ts_nop 0\n\tglobal_load_lds_dwordx4 %0, %1" :: "v"(voff), "s"(base), "s"(lds) : "memory");
}
#define GLD4(dst, voff, base, mod) asm volatile("global_load_dwordx4 %0, %1, %2 " mod : "+v"(dst) : "v"(voff), "s"(base) : "memory")
#define GLD2(dst, voff, base, mod) asm volatile("global_load_dwordx2 %0, %1, %2 " mod : "+v"(dst) : "v"(voff), "s"(base) : "memory")
#define GLD1(dst, voff, base) asm volatile("global_load_dword %0, %1, %2" : "+v"(dst) : "v"(voff), "s"(base) : "memory")
#define GST4(voff, val, base, mod) asm volatile("global_store_dwordx4 %0, %1, %2 " mod "\n\ts_nop 1" :: "v"(voff), "v"(val), "s"(base) : "memory")
#define GST2(voff, val, base, mod) asm volatile("global_store_dwordx2 %0, %1, %2 " mod :: "v"(voff), "v"(val), "s"(base) : "memory")
#define RLD2(dst, voff, base) asm volatile("global_load_dwordx2 %0, %1, %2 sc1\n\ts_waitcnt vmcnt(0)" : "=&v"(dst) : "v"(voff), "s"(base) : "memory")
#define RLD4(dst, voff, base) asm volatile("global_load_dwordx4 %0, %1, %2 sc1\n\ts_waitcnt vmcnt(0)" : "=&v"(dst) : "v"(voff), "s"(base) : "memory")
#define PREFETCH(voff, base) asm volatile("global_load_dword v255, %0, %1" :: "v"(voff), "s"(base) : "v255", "memory")
#define PIN(x) asm volatile("" : "+v"(x))
#define UNDEF(x) asm volatile("" : "=v"(x))

struct ConstTab { u32 tix[2][128][4]; u32 sel[3][64][4]; u32 red[64][4]; };
constexpr int cidx(int l) {
    if (l < 0) return 16 + l;
    const int s = (l >= 256) + (l >= 512) + (l >= 1024);
    return 16 + 128 * s + (s ? (l + (1 << (s - 1)) - 1 + ((l >> s) & 1)) >> s : l);
}
constexpr ConstTab make_tab() {
    ConstTab c{};
    for (int t = 0; t < 128; ++t) {
        u32 v[8] = {};
        for (int i = 0; i < 5; ++i) {
            const int u = t + 128 * i < 607 ? t + 128 * i : 607, pi = u >= 304, ip = u - 304 * pi, top = (ip < 24 ? 1056 : 592) - pi - 2 * ip;
            v[i] = (u32)cidx(top) * 2 | (u32)cidx(top - 1) << 17;
        }
        v[5] = (529 + (t < 126 ? t : 126)) * 0x20002u + 0x20000u;
        v[6] = (404 + (t >> 1) + 2 * (t & 1)) * 0x20002u + 0x20000u;
        v[7] = v[6] + 64 * 0x20002u;
        for (int i = 0; i < 8; ++i) c.tix[i >> 2][t][i & 3] = v[i];
    }
    for (int l = 0; l < 64; ++l) {
        const int fl = l & 15, g = l >> 4, x2 = fl & 7, x3 = (fl + 8 * g) & 15;
        const int m2 = x2 < 2 ? -2 : x2 < 5 ? -1 : 0, m3 = x3 < 3 ? -1 : x3 < 12 ? 0 : 1;
        for (int q = 0; q < 4; ++q)
            for (int h = 0; h < 2; ++h) {
                const int e2 = x2 - 2 * q - h, e3 = x3 - 2 * q - h;
                c.sel[0][l][q] |= (u32)(((e2 >= -5) + (e2 >= -2) + (e2 >= 3) + (e2 >= 6) - 2 - m2) * 0x0202 + 0x0100) << (16 * h);
                c.sel[1][l][q] |= (u32)(((e3 >= -4) + (e3 >= 5) + (e3 >= 12) - 1 - m3) * 0x0202 + 0x0100) << (16 * h);
            }
        const int mul = fl < 8 ? 8 : fl < 12 ? 4 : 2, base = fl < 8 ? fl : fl < 12 ? 248 + fl : 384 + (fl & 1) + 64 * ((fl - 12) >> 1);
        c.red[l][0] = L_KC + (16 + base + mul * 4 * g) * 2; c.red[l][1] = 2 * mul;
        c.red[l][2] = L_KC + (fl < 4 ? (528 + 32 * fl + 4 * g) * 2 : fl == 4 && g == 0 ? 1312 : 1528);
        c.red[l][3] = L_KC + (fl < 4 ? (544 + 32 * fl + 4 * g) * 2 : 1528);
        c.sel[2][l][0] = L_U + 2 * (224 + 16 * fl + 8 * g);
        c.sel[2][l][1] = L_TAB + 4 * ((fl & 1) * 304 + ((16 - fl + 8 * g) >> 1));
        c.sel[2][l][2] = L_TAB + 2432 + 8 * (2 * (2 + (fl >> 3) - g) + m2 - 4);
        c.sel[2][l][3] = L_TAB + 3456 + 4 * (2 * (1 + ((fl - 8 * g + 32) >> 4) - 2) + m3 - 1);
    }
    return c;
}
static __device__ const ConstTab CTAB __attribute__((aligned(128))) = make_tab();

template <int TS, int DMA>
DI void conv_steps(f4 (&acc)[4], int k0, int n, u32 ub, u32 la, u32 q2, u32 p3, const u4& sel2, const u4& sel3, u32 vo, const char* src, u32 d0, u32 d1) {
    constexpr int T[2][4] = {{0, 3, 4, 7}, {1, 2, 5, 6}};
    constexpr int NT = T[TS][3] + 1;
#pragma unroll 1
    for (int kk = 0; kk < n; ++kk, ++k0) {
        u4 A[NT], B[NT];
        u32 u = ub - 64 * k0, l = la + 608 - 64 * k0, q = q2 + 64 * k0, p = p3 + 16 * k0;
        PIN(u); PIN(l); PIN(q); PIN(p);
        auto lin = [&](u32 a) { u4 r = {LDS(u32, a), LDS(u32, a + 4), LDS(u32, a + 8), LDS(u32, a + 12)}; return r; };
        auto seg2 = [&](u2 v) { u4 r;
            r.x = __builtin_amdgcn_perm(v.y, v.x, sel2.x); r.y = __builtin_amdgcn_perm(v.y, v.x, sel2.y);
            r.z = __builtin_amdgcn_perm(v.y, v.x, sel2.z); r.w = __builtin_amdgcn_perm(v.y, v.x, sel2.w); return r; };
        auto seg3 = [&](u32 v) { u4 r;
            r.x = __builtin_amdgcn_perm(v, v, sel3.x); r.y = __builtin_amdgcn_perm(v, v, sel3.y);
            r.z = __builtin_amdgcn_perm(v, v, sel3.z); r.w = __builtin_amdgcn_perm(v, v, sel3.w); return r; };
        A[0] = lin(l + 512);
#pragma unroll
        for (int i = 0; i < 4; ++i) B[T[TS][i]] = LDS(u4, u + 512 * T[TS][i]);
        A[1] = lin(l);
#pragma unroll
        for (int s = 0; s < NT; ++s) if (s != T[TS][0] && s != T[TS][1] && s != T[TS][2] && s != T[TS][3]) B[s] = LDS(u4, u + 512 * s);
        const u2 v2 = LDS(u2, q), v3 = LDS(u2, q + 512);
        u32 v4[4];
#pragma unroll
        for (int d = 4; d < NT; ++d) v4[d - 4] = LDS(u32, p + 128 * (d - 4));
        SB();
        A[2] = seg2(v2); A[3] = seg2(v3);
#pragma unroll
        for (int d = 4; d < NT; ++d) A[d] = seg3(v4[d - 4]);
        if (k0 == 0) { A[2] = lin(la + 96); A[4] = lin(la); }
        const u32 dst = (kk == 2 ? d1 : d0) + (kk & 1) * 16384;
#pragma unroll
        for (int d = 0; d < NT; ++d) {
#pragma unroll
            for (int i = 0; i < 4; ++i)
                if (T[TS][i] >= d) acc[i] = mfma(A[d], B[T[TS][i] - d], acc[i]);
            if (DMA && d < 4) { SB(); dma16(vo, src + d * 7168, dst + d * 1024); SB(); }
        }
        src += 4 * 7168;
    }
}

__global__ __launch_bounds__(512, 2) void s4ffn_kernel(const u16* __restrict__ x, const u16* __restrict__ Are, const u16* __restrict__ Aim,
        const u16* __restrict__ Bre, const u16* __restrict__ Bim, const u16* __restrict__ Cre, const u16* __restrict__ Cim,
        const u16* __restrict__ logdt, const u16* __restrict__ Dsk, const u16* __restrict__ w1, const u16* __restrict__ w2,
        u16* __restrict__ out, char* __restrict__ ws) {
    const int tid = threadIdx.x, lane = tid & 63, wave = __builtin_amdgcn_readfirstlane(tid >> 6);
    const u32 b = blockIdx.x, xcd = b & 7, idx = b >> 3;
    const int fl = lane & 15, g = lane >> 4, l31 = lane & 31, hh = lane >> 5;
    STAMP_DECL;
    {
        u64 pc; asm volatile("s_getpc_b64 %0" : "=s"(pc));
        pc &= ~127ull;
        if (tid < PF_LINES) PREFETCH((idx + tid * 32) * 128, pc);
    }
    STAMP(0); STAMPC(22);
    asm volatile("" :: "s"(x), "s"(Are), "s"(Aim), "s"(Bre), "s"(Bim), "s"(Cre), "s"(Cim), "s"(logdt), "s"(Dsk), "s"(w1), "s"(w2), "s"(out), "s"(ws));
    u32 tag;
    const u32 w4 = wave & 3;
#define LN_LOAD() u4 xr0 = {0, 0, 0, 0}, xr1 = {0, 0, 0, 0}; \
        if (lane < 56) { const char* xp = (const char*)x + (b * 8 + 2 * w4) * 896; GLD4(xr0, lane * 16, xp, ""); GLD4(xr1, lane * 16, xp, "offset:896"); }
#define LN_ROWS() do { \
        float s0 = 0, s1 = 0, q0 = 0, q1 = 0; \
        _Pragma("unroll") for (int i = 0; i < 4; ++i) { s0 = dot2(xr0[i], 0x3f803f80u, s0); s1 = dot2(xr1[i], 0x3f803f80u, s1); q0 = dot2(xr0[i], xr0[i], q0); q1 = dot2(xr1[i], xr1[i], q1); } \
        const u32 v_ = __float_as_uint(rsum(sw16(sw32(s0, s1), sw32(q0, q1))) * (1.f / 448.f)); \
        auto e_ = __builtin_amdgcn_permlane16_swap(v_, v_, false, false);     \
        const float mean = __uint_as_float(e_[0]), var = fmaxf(__uint_as_float(e_[1]) - mean * mean, 0.f); \
        const float mb = rbf(mean), sd_ = rbf(__builtin_amdgcn_sqrtf(rbf(rbf(var) + 1e-5f))), a_ = __builtin_amdgcn_rcpf(sd_), c_ = -mb * a_; \
        const u32 st_ = (__float_as_uint(mb) >> 16) | __float_as_uint(sd_); \
        auto a2_ = __builtin_amdgcn_permlane32_swap(__float_as_uint(a_), __float_as_uint(a_), false, false); \
        auto c2_ = __builtin_amdgcn_permlane32_swap(__float_as_uint(c_), __float_as_uint(c_), false, false); \
        u4 o0, o1; \
        _Pragma("unroll") for (int i = 0; i < 4; ++i) { \
            o0[i] = pkbf(fmaf(bflo(xr0[i]), __uint_as_float(a2_[0]), __uint_as_float(c2_[0])), fmaf(bfhi(xr0[i]), __uint_as_float(a2_[0]), __uint_as_float(c2_[0]))); \
            o1[i] = pkbf(fmaf(bflo(xr1[i]), __uint_as_float(a2_[1]), __uint_as_float(c2_[1])), fmaf(bfhi(xr1[i]), __uint_as_float(a2_[1]), __uint_as_float(c2_[1]))); } \
        if (lane < 56) { LDS(u4, L_XT + w4 * 1920 + lane * 16) = o0; LDS(u4, L_XT + w4 * 1920 + 960 + lane * 16) = o1; } \
        if (l31 == 0) LDS(u32, L_XT + w4 * 1920 + hh * 960 + 896) = st_; } while (0)
#define LN_PUBLISH() do { \
        const u32 a_ = L_XT + (fl >> 2) * 960 + w4 * 128 + g * 32 + (lane & 3) * 8; \
        const u4 v0 = trread2(a_, a_ + 3840), v1 = trread2(a_ + 512, a_ + 3840 + 512); \
        GST4(lane * 16, v0, ws + WS_XG + b * 7168 + w4 * 1024, "sc1"); \
        if (w4 < 3) GST4(lane * 16, v1, ws + WS_XG + b * 7168 + w4 * 1024 + 4096, "sc1"); \
        else if (lane < 2) { const u32 s_ = L_XT + lane * 3840 + 896; const u4 st = {LDS(u32, s_), LDS(u32, s_ + 960), LDS(u32, s_ + 1920), LDS(u32, s_ + 2880)}; \
            GST4(lane * 16, st, ws + WS_STAT + b * 32, "sc1"); } } while (0)
    if (idx >= 28) {
        asm volatile("s_load_dword %0, %1, 0x0" : "=s"(tag) : "s"(ws));
        auto prefetch = [&](u32 k, u32 j) {
            const u32 n = ((k * 4 + idx - 28) * 8 + j) * 64 + lane, m = n - 7168;
            const u32 o = n < 7168 ? (n / 896) * 200704 + n % 896 * 128 : m / 672 * 200704 + 114688 + m % 672 * 128;
            PREFETCH(min(o, 1605504u), w2);
        };
        if (wave < 4) {
            LN_LOAD();
            asm volatile("s_waitcnt vmcnt(0)" : "+v"(xr0), "+v"(xr1) :: "memory");
            STAMP(1);
            LN_ROWS();
            BARRIER();
            __builtin_amdgcn_s_sleep(8);
            prefetch(1, wave); prefetch(1, wave + 4);
        } else {
            __builtin_amdgcn_s_sleep(4);
            prefetch(0, wave - 4); prefetch(0, wave);
            BARRIER();
            LN_PUBLISH();
            WAITV(0);
        }
        asm volatile("s_waitcnt lgkmcnt(0)\n\ts_barrier" : "+s"(tag) :: "memory");
        tag += 1;
        STAMP(3);
        if (tid == 256) { const u2 ar = {tag, tag}; GST2(0, ar, ws + WS_AR + b * 8, "sc1"); }
#pragma unroll 1
        for (u32 k = 2; k < 7; ++k) prefetch(k, wave);
        if (b == 224 && wave == 0) {
            u4 t0, t1;
#pragma unroll 1
            for (int sp = 0; sp < SPIN_MAX; ++sp) {
                RLD4(t0, lane * 16, ws + WS_AR); RLD4(t1, lane * 16, ws + WS_AR + 1024);
                if (!__any(((t0.x ^ tag) | (t0.z ^ tag) | (t1.x ^ tag) | (t1.z ^ tag)) != 0)) break;
            }
            if (lane == 0) { const u2 e = {tag, tag}; GST2(0, e, ws + WS_EPOCH, "sc1"); }
        }
        WAITV(0);
        STAMP(5); STAMP_FLUSH(24);
        return;
    }
    const bool isF = wave < 4;
    const u32 q7 = idx / 7, r7 = idx - 7 * q7;
    const u32 rp = 4 * (xcd >> 1) + q7, cc = 7 * (xcd & 1) + r7, P = 4 * xcd + q7, ct = r7, dp = 28 * xcd + idx;
    const u32 rg = w4 >> 1, ch = wave & 1, hi4 = wave >> 2;
    u32 kt = (r7 + 2 * q7) % 7;
    const int r8 = lane >> 3, p8 = lane & 7, rw = (lane >> 2) & 7;
    u32 vo0 = (32 * w4 + r8) * 896 + 16 * (p8 ^ (r8 >> 1)), vo1 = (vo0 + 8 * 896) ^ 64, vo2 = vo0 + 16 * 896, vo3 = vo1 + 16 * 896;
    u32 vo4 = (16 * w4 + rw) * 3584 + 64 * hh + 16 * ((lane & 3) ^ (rw >> 2)), vo5 = vo4 + 128, vo6 = (vo4 + 8 * 3584) ^ 32, vo7 = vo6 + 128;
    const char *xb = (const char*)x + rp * 114688, *wb = (const char*)w1 + cc * 256;
    const char *pa, *pw;
#define RING_SET() do { asm volatile("" : "+s"(kt)); pa = xb + kt * 128; pw = wb + kt * 229376; kt = kt == 6 ? 0 : kt + 1; } while (0)
#define DMA_I(i, l_) do { if ((i) == 0) dma16(vo0, pa, l_); if ((i) == 1) dma16(vo1, pa, l_ + 1024); \
        if ((i) == 2) dma16(vo2, pa, l_ + 2048); if ((i) == 3) dma16(vo3, pa, l_ + 3072); \
        if ((i) == 4) dma16(vo4, pw, l_ + 16384); if ((i) == 5) dma16(vo5, pw, l_ + 17408); \
        if ((i) == 6) dma16(vo6, pw, l_ + 18432); if ((i) == 7) dma16(vo7, pw, l_ + 19456); } while (0)
#define DMA_ALL(l_) do { DMA_I(0, l_); DMA_I(1, l_); DMA_I(2, l_); DMA_I(3, l_); DMA_I(4, l_); DMA_I(5, l_); DMA_I(6, l_); DMA_I(7, l_); } while (0)
    const u32 lw = L_RING + w4 * 4096;
    u4 sel2, sel3; u32 cv_ub, cv_la, cv_q2, cv_p3, dsk2, hm0, hm1, hm2, hm3;
#define SEL_READ() do { sel2 = LDS(u4, L_SEL + lane * 16); sel3 = LDS(u4, L_SEL + 1024 + lane * 16); const u4 c_ = LDS(u4, L_SEL + 2048 + lane * 16); \
        cv_ub = c_.x + rg * 4608; cv_la = c_.y + rg * 4096; cv_q2 = c_.z + rg * 4096; cv_p3 = c_.w + rg * 4096; } while (0)
#define SCALARS_ISSUE() do { asm volatile("s_load_dword %0, %1, 0x0" : "=s"(tag) : "s"(ws)); asm volatile("s_load_dword %0, %1, 0x0" : "=s"(dsk2) : "s"((const char*)Dsk + dp * 4)); } while (0)
#define SCALARS_DONE() do { tag += 1; const u32 pat = 1 + tag % 254; hm0 = (pat & 1) << 15 | (pat & 2) << 30; hm1 = (pat & 4) << 13 | (pat & 8) << 28; \
        hm2 = (pat & 16) << 11 | (pat & 32) << 26; hm3 = (pat & 64) << 9 | (pat & 128) << 24; } while (0)
    const char* hb = ws + WS_H + ((2 * rp + rg) * 112 + 8 * cc + 4 * ch) * 2048;
    const u32 vw2 = r8 * 896 + 16 * (p8 ^ (4 * ((r8 >> 1) & 1)));
    const char* w2b = (const char*)w2 + wave * (224 * 896) + ct * 128;
    const u32 pb0 = lw + (isF ? 98304 : 32768), pb1 = lw + (isF ? 0 : 65536);
    u4 Bw[14][2];
    const u32 aw2a = pb0 + (4 * hh + (fl >> 2)) * 128 + 16 * ((2 * (g & 1) + ((lane & 3) >> 1)) ^ (4 * ((fl >> 3) & 1))) + 8 * (lane & 1);
    const u32 aw2b = aw2a ^ 64, aw2c = aw2a - pb0 + pb1, aw2d = aw2c ^ 64;
#define W2_RD(j, p, n) do { const u32 o_ = ((n) >> 1) * 16384 + ((n) & 1) * 2048; \
        Bw[j][0] = trread2(((p) ? aw2c : aw2a) + o_, ((p) ? aw2c : aw2a) + o_ + 1024); Bw[j][1] = trread2(((p) ? aw2d : aw2b) + o_, ((p) ? aw2d : aw2b) + o_ + 1024); } while (0)
#define W2_PIECE(p) do { _Pragma("unroll 1") for (u32 i = 0; i < 8; ++i) dma16(vw2, w2b + ((p) * 8 + i) * 7168, ((p) ? pb1 : pb0) + (i >> 2) * 16384 + (i & 3) * 1024); asm volatile("; AUDIT_VM 7"); } while (0)
#define W2_DRAIN() do { W2_RD(0, 0, 0); W2_RD(1, 0, 1); W2_RD(2, 0, 2); W2_RD(3, 0, 3); W2_RD(4, 1, 0); W2_RD(5, 1, 1); W2_RD(6, 1, 2); W2_RD(7, 1, 3); \
        asm volatile("s_waitcnt lgkmcnt(0)" ::: "memory"); } while (0)
    f4 cacc[4];
#pragma unroll
    for (int i = 0; i < 4; ++i) cacc[i] = f4{0, 0, 0, 0};
#define CONV(k0, n, DMA) do { if (ch) conv_steps<1, DMA>(cacc, k0, n, cv_ub, cv_la, cv_q2, cv_p3, sel2, sel3, vw2, w2b + 16 * 7168, pb0, pb1); \
        else conv_steps<0, DMA>(cacc, k0, n, cv_ub, cv_la, cv_q2, cv_p3, sel2, sel3, vw2, w2b + 16 * 7168, pb0, pb1); } while (0)
    if (isF) {
        SCALARS_ISSUE();
        __builtin_amdgcn_s_setprio(3);
        LN_LOAD();
        __builtin_amdgcn_s_setprio(2); RING_SET(); DMA_ALL(lw); __builtin_amdgcn_s_setprio(1); RING_SET();
        DMA_I(0, lw + 32768); DMA_I(1, lw + 32768); DMA_I(2, lw + 32768); DMA_I(3, lw + 32768);
        __builtin_amdgcn_s_setprio(0);
        asm volatile("s_waitcnt lgkmcnt(0)" : "+s"(tag), "+s"(dsk2) :: "memory");
        SCALARS_DONE();
        f16v acc[2][2]; f4 cs = {0, 0, 0, 0};
#pragma unroll
        for (int i = 0; i < 16; ++i) { acc[0][0][i] = 0; acc[0][1][i] = 0; acc[1][0][i] = 0; acc[1][1][i] = 0; }
        const u32 o1 = ((g ^ lane) & 1) ? 0 : 0x3f803f80u; u4 ones = {o1, o1, o1, o1};
        u32 ax0 = L_RING + (64 * rg + l31) * 128 + 16 * (hh ^ ((l31 >> 1) & 7)), ax1 = ax0 ^ 32, ax2 = ax0 ^ 64, ax3 = ax0 ^ 96;
        u32 aw0 = L_RING + 16384 + 2048 * hh + 1024 * ch + 512 * rg + 64 * (fl >> 2) + 16 * ((2 * (g & 1) + ((lane & 3) >> 1)) ^ (2 * hh)) + 8 * (lane & 1);
        u32 aw1 = aw0 ^ 272, aw2 = aw0 ^ 512, aw3 = aw0 ^ 784;
        u4 X0[4], W0[4], X1[4], W1[4];
#define G1_RD(i, X, W, a0, a1, o) do { if ((i) == 0) { X[0] = LDS(u4, a0); W[0] = trread2(aw0 + (o), aw1 + (o)); } \
        if ((i) == 1) { X[1] = LDS(u4, a0 + 4096); W[1] = trread2(aw2 + (o), aw3 + (o)); } \
        if ((i) == 2) { X[2] = LDS(u4, a1); W[2] = trread2(aw0 + (o) + 4096, aw1 + (o) + 4096); } \
        if ((i) == 3) { X[3] = LDS(u4, a1 + 4096); W[3] = trread2(aw2 + (o) + 4096, aw3 + (o) + 4096); } } while (0)
#define G1_PHASE(X, W, RD, DM) do { \
        acc[0][0] = mfma32(W[0], X[0], acc[0][0]); RD(0); SB(); \
        acc[0][1] = mfma32(W[0], X[1], acc[0][1]); RD(1); SB(); \
        acc[1][0] = mfma32(W[1], X[0], acc[1][0]); DM(0); SB(); \
        acc[1][1] = mfma32(W[1], X[1], acc[1][1]); RD(2); SB(); \
        cs = mfma(W[0], ones, cs); DM(1); SB(); \
        acc[0][0] = mfma32(W[2], X[2], acc[0][0]); RD(3); SB(); \
        acc[0][1] = mfma32(W[2], X[3], acc[0][1]); DM(2); SB(); \
        acc[1][0] = mfma32(W[3], X[2], acc[1][0]); SB(); \
        acc[1][1] = mfma32(W[3], X[3], acc[1][1]); DM(3); SB(); \
        cs = mfma(W[2], ones, cs); SB(); } while (0)
#define RDA(i) G1_RD(i, X1, W1, ax2, ax3, 8192)
#define RDB(i) G1_RD(i, X0, W0, ax0, ax1, 0)
#define DMA_A(i) DMA_I(4 + i, ld)
#define DMA_B(i) DMA_I(i, ld)
#define NONE(i)
        PIN(acc[0][0]); PIN(acc[0][1]); PIN(acc[1][0]); PIN(acc[1][1]);
        PIN(ax0); PIN(ax1); PIN(ax2); PIN(ax3); PIN(aw0); PIN(aw1); PIN(aw2); PIN(aw3); PIN(ones);
        __builtin_amdgcn_s_setprio(2);
        asm volatile("s_waitcnt vmcnt(12)" : "+v"(xr0), "+v"(xr1) :: "memory");
        STAMP(1);
        DMA_I(4, lw + 32768); DMA_I(5, lw + 32768); DMA_I(6, lw + 32768); DMA_I(7, lw + 32768);
        RING_SET();
        LN_ROWS();
        __builtin_amdgcn_s_setprio(0);
        STAMP(2);
        WAITV(8);
        BARRIER();
        RDB(0); RDB(1); RDB(2); RDB(3);
        DMA_I(0, lw + 65536); DMA_I(1, lw + 65536); DMA_I(2, lw + 65536); DMA_I(3, lw + 65536);
        STAMP(3);
        u32 ld = lw + 65536;
#pragma unroll 1
        for (u32 s = 0; s < 6; ++s) {
            G1_PHASE(X0, W0, RDA, DMA_A);
            { const u32 m_ = (0x8000u * (((s & 3) + 1) ^ (((s + 1) & 3) + 1)));
              ax0 ^= m_; ax1 ^= m_; ax2 ^= m_; ax3 ^= m_; aw0 ^= m_; aw1 ^= m_; aw2 ^= m_; aw3 ^= m_; }
            RING_SET();
            if (s >= 4) pa = pw = w2b + (s - 4) * 57344;
            if (s == 4) { vo0 = vw2; vo1 = vw2 + 7168; vo2 = vw2 + 14336; vo3 = vw2 + 21504; vo4 = vw2 + 28672; vo5 = vw2 + 35840; vo6 = vw2 + 43008; vo7 = vw2 + 50176; }
            ld = lw + ((s + 3) & 3) * 32768;
            SB();
            { ACC_BEGIN(); WAITV(8); ACC_END(20); BARRIER(); ACC_END(21); }
            SB();
            G1_PHASE(X1, W1, RDB, DMA_B);
        }
        G1_PHASE(X0, W0, RDA, DMA_A);
        G1_PHASE(X1, W1, NONE, NONE);
        if (fl < 2) LDS(f4, L_CS + (64 * ch + 32 * rg + 16 * fl + 4 * g) * 4) = cs;
        STAMP(4);
        BARRIER();
        u4 ho[8];
#pragma unroll
        for (int tt = 0; tt < 2; ++tt) {
            const u32 sv = LDS(u32, L_ST + (64 * rg + 32 * tt + l31) * 4);
            const float m = -bflo(sv), rs = __builtin_amdgcn_rcpf(bfhi(sv));
            const f2 m2 = {m, m}, rs2 = {rs, rs};
#pragma unroll
            for (int c = 0; c < 2; ++c)
#pragma unroll
                for (int k = 0; k < 2; ++k)
#pragma unroll
                    for (int q = 0; q < 2; ++q) {
                        const f4 cv = LDS(f4, L_CS + ((64 * ch + 32 * rg + 4 * hh) * 4 ^ (128 * c)) + (2 * k + q) * 32), av = quad(acc[c][tt], 2 * k + q);
                        const f2 v0 = __builtin_elementwise_fma(m2, f2{cv.x, cv.y}, f2{av.x, av.y}) * rs2, v1 = __builtin_elementwise_fma(m2, f2{cv.z, cv.w}, f2{av.z, av.w}) * rs2;
                        ho[4 * tt + 2 * c + k][2 * q] = relu2(pkbf(v0.x, v0.y)) | (q ? hm2 : hm0); ho[4 * tt + 2 * c + k][2 * q + 1] = relu2(pkbf(v1.x, v1.y)) | (q ? hm3 : hm1);
                    }
        }
#pragma unroll
        for (int i = 0; i < 8; ++i) PIN(ho[i]);
        SEL_READ();
        STAMP(5);
        BARRIER();
        STAMP(6);
#pragma unroll
        for (int i = 0; i < 4; ++i) { const char* hp = hb + (2 * ((i >> 1) ^ rg) + (i & 1)) * 2048; GST4(lane * 16, ho[i], hp, "sc1"); GST4(lane * 16, ho[4 + i], hp, "offset:1024 sc1"); }
        STAMP(12);
    } else {
        __builtin_amdgcn_s_setprio(3);
        u32 pq0, pq1, pq2, pq3, pq4, pq5, ldt2;
        UNDEF(pq0); UNDEF(pq1); UNDEF(pq2); UNDEF(pq3); UNDEF(pq4); UNDEF(pq5);
        { const u32 po = (2 * dp + rg) * 512 + ch * 256 + lane * 4;
          GLD1(pq0, po, Are); GLD1(pq1, po, Aim); GLD1(pq2, po, Bre); GLD1(pq3, po, Bim); GLD1(pq4, po, Cre); GLD1(pq5, po, Cim); }
        __builtin_amdgcn_s_setprio(1);
        { const u32 i1 = min(w4 + 4, 6u); dma16(lane * 16, (const char*)&CTAB + w4 * 1024, L_TAB + w4 * 1024); dma16(lane * 16, (const char*)&CTAB + i1 * 1024, L_SEL + (i1 - 4) * 1024); }
        u4 red; UNDEF(red); GLD4(red, lane * 16, (const char*)&CTAB + 7168, "");
        SCALARS_ISSUE();
        asm volatile("s_load_dword %0, %1, 0x0" : "=s"(ldt2) : "s"((const char*)logdt + dp * 4));
        asm volatile("s_waitcnt lgkmcnt(0)" : "+s"(tag), "+s"(dsk2), "+s"(ldt2) :: "memory");
        SCALARS_DONE();
        STAMP(1);
        const float dt = rbf(expf(rg ? bfhi(ldt2) : bflo(ldt2)));
        u32 sr = L_SCR + w4 * 4352 + fl * 272 + g * 16; PIN(sr);
        asm volatile("s_mov_b32 m0, %0" :: "s"(L_SCR + w4 * 4352));
        asm volatile("s_waitcnt vmcnt(3)" : "+v"(pq0), "+v"(pq1), "+v"(pq2), "+v"(pq3), "+v"(pq4), "+v"(pq5) :: "memory");
        STAMP(2);
        f2 c0 = cmulc(f2{bflo(pq4), -bflo(pq5)}, f2{dt * bflo(pq2), dt * bflo(pq3)}), c1 = cmulc(f2{bfhi(pq4), -bfhi(pq5)}, f2{dt * bfhi(pq2), dt * bfhi(pq3)});
        float isc;
        {
            float m = rmax(fmaxf(fmaxf(fabsf(c0.x), fabsf(c0.y)), fmaxf(fabsf(c1.x), fabsf(c1.y))));
            auto s1 = __builtin_amdgcn_permlane16_swap(__float_as_uint(m), __float_as_uint(m), false, false); u32 e = max(s1[0], s1[1]);
            auto s2 = __builtin_amdgcn_permlane32_swap(e, e, false, false); e = max(s2[0], s2[1]) & 0x7f800000u;
            isc = __uint_as_float(e);
            const float sc = __uint_as_float(0x7f000000u - e);
            c0.x *= sc; c0.y *= sc; c1.x *= sc; c1.y *= sc;
        }
        f4 dk[4];
#pragma unroll
        for (int t = 0; t < 4; ++t) dk[t] = f4{0, 0, 0, 0};
        u2 arf; u32 stv; u4 xg0, xg1;
        const u32 xo = ((tid & 255) >> 1) * 7168 + (tid & 1) * 16;
#define SBAR() do { SB(); ACC_BEGIN(); asm volatile("s_barrier" ::: "memory"); ACC_END(21); SB(); } while (0)
#define PUT(e, v) asm volatile("ds_write_addtid_b32 %0 offset:%1" :: "v"(v), "i"((e) * 272) : "memory")
#define RD4(A) _Pragma("unroll") for (int s = 0; s < 4; ++s) A[s] = LDS(u4, sr + s * 64)
#define MM2(h, s) do { dk[2 * (h)] = mfmah(A[s], B0[s], dk[2 * (h)]); dk[2 * (h) + 1] = mfmah(A[s], B1[s], dk[2 * (h) + 1]); } while (0)
        u4 A[4], B0[4], B1[4];
        f2 z1, z8, z32, z256;
        h2 w8, w16, w24, a32, a64, a96, a128;
#define KG_POW(hi) do { const float a2 = fmul1(dt, (hi) ? bfhi(pq0) : bflo(pq0)) * 1.44269504089f, rev = fmul1(dt, (hi) ? bfhi(pq1) : bflo(pq1)) * 0.15915494309f; \
        z1 = zpow(a2, rev, 1.f); z8 = zpow(a2, rev, 8.f); z32 = zpow(a2, rev, 32.f); z256 = zpow(a2, rev, 256.f); } while (0)
        f2 z512, e4, e5; h2 f1, f2_, f4_, g0, g1, g2, g3;
#define KG_COLS_A(c, PEND) do { \
        const f2 z2 = cmul(z1, z1), e1 = cmulc(c, z256), z16 = cmul(z8, z8), z64 = cmul(z32, z32); z512 = cmul(z256, z256); \
        if (PEND) MM2(1, 0); \
        const f2 z4 = cmul(z2, z2), e2 = cmulc(c, z512), e3 = cmulc(e1, z512), z128 = cmul(z64, z64); \
        if (PEND) MM2(1, 1); \
        f1 = cvth(z1); f2_ = cvth(z2); f4_ = cvth(z4); g0 = cvth(c); g1 = cvth(e1); g2 = cvth(e2); g3 = cvth(e3); \
        if (PEND) MM2(1, 2); \
        e4 = cmulc(e2, z512); e5 = cmulc(e3, z512); \
        if (PEND) MM2(1, 3); \
        w8 = cvth(z8); w16 = cvth(z16); w24 = hmul(w8, w16); a32 = cvth(z32); a64 = cvth(z64); a96 = hmul(a32, a64); a128 = cvth(z128); } while (0)
#define KG_COLS_B() do { \
        const h2 f3 = hmul(f1, f2_), f6 = hmul(f2_, f4_), g04 = hmulc(g0, f4_); \
        PUT(0, g0); PUT(1, hmulc(g0, f1)); PUT(2, hmulc(g0, f2_)); PUT(3, hmulc(g0, f3)); \
        PUT(4, g04); PUT(5, hmulc(g04, f1)); PUT(6, hmulc(g0, f6)); PUT(7, hmulc(g04, f3)); \
        const f2 e6 = cmulc(e4, z512), e7 = cmulc(e5, z512); \
        PUT(8, g1); PUT(9, hmulc(g1, f2_)); PUT(10, hmulc(g1, f4_)); PUT(11, hmulc(g1, f6)); \
        const f2 e8 = cmulc(e6, z512); \
        PUT(12, g2); PUT(13, hmulc(g2, f4_)); PUT(14, g3); PUT(15, hmulc(g3, f4_)); \
        RD4(B0); \
        PUT(0, cvth(e4)); PUT(1, cvth(e5)); PUT(2, cvth(e6)); PUT(3, cvth(e7)); PUT(4, cvth(e8)); \
        RD4(B1); } while (0)
#define KG_QUAD(e, a) do { PUT(e, a); PUT((e) + 1, hmul(a, w8)); PUT((e) + 2, hmul(a, w16)); PUT((e) + 3, hmul(a, w24)); } while (0)
#define KG_ROWS_A() do { const h2 one = {1, 0}; PUT(0, one); PUT(1, w8); PUT(2, w16); PUT(3, w24); KG_QUAD(4, a32); KG_QUAD(8, a64); KG_QUAD(12, a96); RD4(A); } while (0)
#define KG_ROWS_B() do { const h2 a160 = hmul(a128, a32), a192 = hmul(a128, a64), a224 = hmul(a128, a96); \
        KG_QUAD(0, a128); MM2(0, 0); KG_QUAD(4, a160); MM2(0, 1); KG_QUAD(8, a192); MM2(0, 2); KG_QUAD(12, a224); MM2(0, 3); RD4(A); } while (0)
        KG_POW(0); KG_COLS_A(c0, 0); KG_COLS_B();
        STAMP(3);
        SBAR();
        LN_PUBLISH();
        KG_ROWS_A();
        STAMPF(15); SBAR();
        KG_ROWS_B();
        STAMPF(16); SBAR();
        KG_POW(1); KG_COLS_A(c1, 1);
        asm volatile("s_waitcnt vmcnt(0)" : "+v"(red) :: "memory");
        STAMPF(17); SBAR();
        if (tid == 256) { const u2 ar = {tag, tag}; GST2(0, ar, ws + WS_AR + b * 8, "sc1"); }
        KG_COLS_B();
        STAMPF(18); SBAR();
        KG_ROWS_A();
        STAMPF(19); SBAR();
        UNDEF(arf); GLD2(arf, (tid & 255) * 8, ws + WS_AR, "sc1");
        KG_ROWS_B();
        MM2(1, 0); MM2(1, 1); MM2(1, 2); MM2(1, 3);
        { const u32 sw = L_SCR + w4 * 4352 + lane * 16;
          LDS(f4, sw) = self4(ch, dk[0], dk[2]) * isc; LDS(f4, sw + 1024) = self4(ch, dk[1], dk[3]) * isc; }
        asm volatile("s_waitcnt vmcnt(0)" : "+v"(arf) :: "memory");
#pragma unroll 1
        for (int sp = 0; __any(arf.x != tag) && sp < SPIN_MAX; ++sp) RLD2(arf, (tid & 255) * 8, ws + WS_AR);
        STAMP(12);
        SB(); BARRIER();
        UNDEF(stv); UNDEF(xg0); UNDEF(xg1);
        asm volatile("global_load_dword %0, %1, %2 sc1" : "+v"(stv) : "v"((tid & 127) * 4), "s"(ws + WS_STAT + rp * 512) : "memory");
        GLD4(xg0, xo, ws + WS_XG + dp * 32, "sc1"); GLD4(xg1, xo, ws + WS_XG + dp * 32 + 128 * 7168, "sc1");
        {
            const u32 a = L_SCR + (w4 ^ 1) * 4352 + lane * 16;
            const f4 s0 = self4(ch, dk[2], dk[0]) * isc + LDS(f4, a), s1 = self4(ch, dk[3], dk[1]) * isc + LDS(f4, a + 1024);
            const u32 o = red.x + ch * 16 * red.y + rg * 1536, p01 = pkbf(s0.x, s0.y), p23 = pkbf(s0.z, s0.w);
            LDS(u16, o) = (u16)p01; LDS(u16, o + red.y) = (u16)(p01 >> 16); LDS(u16, o + 2 * red.y) = (u16)p23; LDS(u16, o + 3 * red.y) = (u16)(p23 >> 16);
            LDS(u2, (ch ? red.w : red.z) + rg * 1536) = u2{pkbf(s1.x, s1.y), pkbf(s1.z, s1.w)};
            if (lane == 0) LDS(u4, L_KC + rg * 1536 + ch * 16) = u4{0, 0, 0, 0};
        }
        const u4 tixa = LDS(u4, L_TAB + (tid & 127) * 16), tixb = LDS(u4, L_TAB + 2048 + (tid & 127) * 16);
        SEL_READ();
        asm volatile("s_waitcnt vmcnt(2)" : "+v"(stv) :: "memory");
        if (w4 < 2) LDS(u32, L_ST + (tid & 127) * 4) = stv;
        __builtin_amdgcn_s_setprio(0);
        W2_PIECE(0);
        STAMP(4);
        BARRIER();
        {
            u32 kc = L_KC + rg * 1536, td = L_TAB + rg * 4096 + (tid & 127) * 4; PIN(td);
            u32 v[8];
#pragma unroll
            for (int i = 0; i < 8; ++i) { const u32 tx = i < 4 ? tixa[i & 3] : tixb[i & 3]; v[i] = (u32)LDS(u16, kc + (tx & 0xffff)) | ((u32)LDS(u16, kc + (tx >> 16)) << 16); }
#pragma unroll
            for (int i = 0; i < 4; ++i) LDS(u32, td + 512 * i) = v[i];
            if ((tid & 127) < 96) LDS(u32, td + 2048) = v[4];
            LDS(u32, td + 3456) = v[5]; LDS(u32, td + 2432) = v[6]; LDS(u32, td + 2944) = v[7];
        }
        {
            asm volatile("s_waitcnt vmcnt(8)" : "+v"(xg0), "+v"(xg1) :: "memory");
            u32 a = L_U + 480 + (tid & 1) * 4608 + ((tid & 255) >> 1) * 16; PIN(a);
            LDS(u4, a) = xg0; LDS(u4, a + 2048) = xg1;
            if ((tid & 255) < 60) LDS(u4, a - 480) = u4{0, 0, 0, 0};
        }
        STAMP(5);
        BARRIER();
        W2_PIECE(1);
        STAMP(6);
    }
    CONV(3, isF ? 0 : 2, 0);
    STAMP(13);
    if (isF) WAITV(8); else WAITV(0);
    W2_DRAIN();
    STAMP(14);
    CONV(isF ? 0 : 5, 3, 1);
    asm volatile("; AUDIT_VM 8");
    u4 hA0, hA1, hA2, hA3, hA4, hA5, hA6, hB0, hB1, hB2, hB3, hB4, hB5, hB6;
    const char* hrd = ws + WS_H + (P * 112 + wave * 14) * 2048;
#define H_LOAD(h, n) do { const char* p_ = hrd + ((n) & 1) * 14336 + ((n) >> 1) * 1024; GLD4(h##0, lane * 16, p_, "sc1"); GLD4(h##1, lane * 16, p_, "offset:2048 sc1"); \
        GLD4(h##2, lane * 16, p_ + 4096, "sc1"); GLD4(h##3, lane * 16, p_ + 4096, "offset:2048 sc1"); GLD4(h##4, lane * 16, p_ + 8192, "sc1"); \
        GLD4(h##5, lane * 16, p_ + 8192, "offset:2048 sc1"); GLD4(h##6, lane * 16, p_ + 12288, "sc1"); } while (0)
#define H_UNDEF(h) do { UNDEF(h##0); UNDEF(h##1); UNDEF(h##2); UNDEF(h##3); UNDEF(h##4); UNDEF(h##5); UNDEF(h##6); } while (0)
    {
        const int kh = hi4, ts = ch;
        STAMP(7);
        H_UNDEF(hA); H_UNDEF(hB);
        H_LOAD(hA, 0); H_LOAD(hB, 1);
        BARRIER();
        u32 ra = wave * 2048 + lane * 16; PIN(ra);
        LDS(f4, ra) = self4(kh, cacc[0], cacc[2]);
        LDS(f4, ra + 1024) = self4(kh, cacc[1], cacc[3]);
        ra ^= 8192;
        BARRIER();
        const float dk = rg ? bfhi(dsk2) : bflo(dsk2);
#pragma unroll
        for (int i = 0; i < 2; ++i) {
            const int tau = kh ? (ts ? 5 + i : 4 + 3 * i) : (ts ? 1 + i : 3 * i);
            const f4 y = self4(kh, cacc[2 + i], cacc[i]) + LDS(f4, ra + i * 1024);
            const u2 uu = LDS(u2, L_U + rg * 4608 + 480 + (256 * tau + 16 * fl + 4 * g) * 2);
            const u32 y01 = pkbf(y.x, y.y), y23 = pkbf(y.z, y.w);
            const u32 s01 = pkbf(bflo(uu.x) * dk, bfhi(uu.x) * dk), s23 = pkbf(bflo(uu.y) * dk, bfhi(uu.y) * dk);
            const u4 o = {pkbf(bflo(y01) + bflo(s01), bfhi(y01) + bfhi(s01)), tag, pkbf(bflo(y23) + bflo(s23), bfhi(y23) + bfhi(s23)), tag};
            GST4((fl >> 2) * 114688 + ((fl & 3) + 4 * g) * 16, o, ws + WS_YG + (4 * tau * 448 + 2 * dp + rg) * 256, "sc1");
        }
    }
    STAMP(8);
    WAITV(16);
    W2_RD(8, 0, 0); W2_RD(9, 0, 1); W2_RD(10, 0, 2); W2_RD(11, 0, 3); W2_RD(12, 1, 0); W2_RD(13, 1, 1);
    u2 yv[8], xv[2];
    u32 pw0 = pb0 + lane * 16, pw1 = pb1 + lane * 16; PIN(pw0); PIN(pw1);
#define OR4(v) ((v.x | v.y) | (v.z | v.w))
#define H_WAIT(h, NW) asm volatile("s_waitcnt vmcnt(" #NW ")" : "+v"(h##0), "+v"(h##1), "+v"(h##2), "+v"(h##3), "+v"(h##4), "+v"(h##5), "+v"(h##6) :: "memory")
#define UNM(v) do { v.x ^= hm0; v.y ^= hm1; v.z ^= hm2; v.w ^= hm3; } while (0)
#define H_UNM(h) do { UNM(h##0); UNM(h##1); UNM(h##2); UNM(h##3); UNM(h##4); UNM(h##5); UNM(h##6); } while (0)
#define H_OR(h) (OR4(h##0) | OR4(h##1) | OR4(h##2) | OR4(h##3) | OR4(h##4) | OR4(h##5) | OR4(h##6))
#define H_MM1(h, j, o) do { a0 = mfma32(Bw[(o) + j][0], h##j, a0); a1 = mfma32(Bw[(o) + j][1], h##j, a1); } while (0)
#define H_MMA(h, o) do { H_MM1(h, 0, o); H_MM1(h, 1, o); H_MM1(h, 2, o); H_MM1(h, 3, o); H_MM1(h, 4, o); H_MM1(h, 5, o); H_MM1(h, 6, o); } while (0)
#define H_ZERO() _Pragma("unroll") for (int i = 0; i < 16; ++i) { a0[i] = 0; a1[i] = 0; }
#define H_PUT(pw) _Pragma("unroll") for (int q = 0; q < 4; ++q) { LDS(f4, (pw) + q * 1024) = quad(a0, q); LDS(f4, (pw) + 16384 + q * 1024) = quad(a1, q); }
    u32 hbad = 0;
    f16v a0, a1;
    H_ZERO();
    H_WAIT(hA, 9); H_UNM(hA); hbad |= H_OR(hA); PIN(hbad); H_MMA(hA, 0);
    STAMP(9);
    H_LOAD(hA, 2);
    H_WAIT(hB, 9); H_UNM(hB); hbad |= H_OR(hB); PIN(hbad); H_MMA(hB, 7); H_PUT(pw0);
    H_LOAD(hB, 3);
    H_ZERO();
    H_WAIT(hA, 7); H_UNM(hA); hbad |= H_OR(hA); PIN(hbad); H_MMA(hA, 0);
    const u32 dq = 32 * rg + 16 * ch + 4 * hh;
    const u32 yo = dq * 256 + (2 * hi4 + (g & 1) + 4 * ((lane >> 2) & 3)) * 16 + ((lane >> 1) & 1) * 8;
    const char* yb = ws + WS_YG + (P * 448 + 64 * ct) * 256;
#define Y_LOAD() do { GLD2(yv[0], yo, yb, "sc1"); GLD2(yv[1], yo, yb, "offset:256 sc1"); GLD2(yv[2], yo, yb, "offset:512 sc1"); GLD2(yv[3], yo, yb, "offset:768 sc1"); \
        GLD2(yv[4], yo, yb, "offset:2048 sc1"); GLD2(yv[5], yo, yb, "offset:2304 sc1"); GLD2(yv[6], yo, yb, "offset:2560 sc1"); GLD2(yv[7], yo, yb, "offset:2816 sc1"); } while (0)
    const u32 xo = (32 * hi4 + l31) * 896 + dq * 2;
#pragma unroll
    for (int i = 0; i < 8; ++i) UNDEF(yv[i]);
    UNDEF(xv[0]); UNDEF(xv[1]);
    Y_LOAD();
    GLD2(xv[0], xo, (const char*)x + P * 57344 + ct * 128, ""); GLD2(xv[1], xo, (const char*)x + P * 57344 + ct * 128, "offset:16");
    H_WAIT(hB, 10); H_UNM(hB); hbad |= H_OR(hB); PIN(hbad); H_MMA(hB, 7); H_PUT(pw1);
    if (__any(hbad & 0x80008000u)) {
#pragma unroll 1
        for (int r = 0; r < 2; ++r) {
            H_ZERO();
#define H_FIX(j) { u4 t; _Pragma("unroll 1") for (int sp = 0; sp < SPIN_MAX; ++sp) { RLD4(t, lane * 16, hrd + r * 1024 + (j) * 2048); UNM(t); if (!__any(OR4(t) & 0x80008000u)) break; } \
                a0 = mfma32(Bw[j][0], t, a0); a1 = mfma32(Bw[j][1], t, a1); }
            H_FIX(0) H_FIX(1) H_FIX(2) H_FIX(3) H_FIX(4) H_FIX(5) H_FIX(6) H_FIX(7) H_FIX(8) H_FIX(9) H_FIX(10) H_FIX(11) H_FIX(12) H_FIX(13)
            H_PUT(r ? pw1 : pw0);
        }
    }
    BARRIER();
    STAMP(10);
    f4 r2[2];
    {
        u32 aF = L_RING + (hi4 ? 0 : 98304) + rg * 16384 + ch * 2048 + lane * 16, aS = L_RING + (hi4 ? 65536 : 32768) + rg * 16384 + ch * 2048 + lane * 16;
        PIN(aF); PIN(aS);
#pragma unroll
        for (int i = 0; i < 2; ++i)
            r2[i] = ((LDS(f4, aF + i * 1024) + LDS(f4, aF + i * 1024 + 4096)) + (LDS(f4, aF + i * 1024 + 8192) + LDS(f4, aF + i * 1024 + 12288))) +
                    ((LDS(f4, aS + i * 1024) + LDS(f4, aS + i * 1024 + 4096)) + (LDS(f4, aS + i * 1024 + 8192) + LDS(f4, aS + i * 1024 + 12288)));
    }
    asm volatile("s_waitcnt vmcnt(0)" : "+v"(yv[0]), "+v"(yv[1]), "+v"(yv[2]), "+v"(yv[3]), "+v"(yv[4]), "+v"(yv[5]), "+v"(yv[6]), "+v"(yv[7]), "+v"(xv[0]), "+v"(xv[1]) :: "memory");
    if (__any(((yv[0].y ^ tag) | (yv[1].y ^ tag) | (yv[2].y ^ tag) | (yv[3].y ^ tag) | (yv[4].y ^ tag) | (yv[5].y ^ tag) | (yv[6].y ^ tag) | (yv[7].y ^ tag)) != 0)) {
#define Y_FIX(i) _Pragma("unroll 1") for (int sp = 0; __any(yv[i].y != tag) && sp < SPIN_MAX; ++sp) RLD2(yv[i], yo, yb + ((i) & 3) * 256 + ((i) >> 2) * 2048)
        Y_FIX(0); Y_FIX(1); Y_FIX(2); Y_FIX(3); Y_FIX(4); Y_FIX(5); Y_FIX(6); Y_FIX(7);
    }
#pragma unroll
    for (int i = 0; i < 2; ++i) {
        float yy[4];
#pragma unroll
        for (int r = 0; r < 4; ++r) yy[r] = (lane & 1) ? bfhi(yv[4 * i + r].x) : bflo(yv[4 * i + r].x);
        const u32 a01 = pkbf(bflo(xv[i].x) + yy[0], bfhi(xv[i].x) + yy[1]), a23 = pkbf(bflo(xv[i].y) + yy[2], bfhi(xv[i].y) + yy[3]);
        const u32 f01 = pkbf(r2[i].x, r2[i].y), f23 = pkbf(r2[i].z, r2[i].w);
        const u2 o = {pkbf(bflo(a01) + bflo(f01), bfhi(a01) + bfhi(f01)), pkbf(bflo(a23) + bflo(f23), bfhi(a23) + bfhi(f23))};
        *(u2*)((char*)out + P * 57344 + ct * 128 + xo + i * 16) = o;
    }
    STAMP(11); STAMPC(23);
    STAMP_FLUSH(24);
}

extern "C" void kernel_launch(void* const* d_in, const int* in_sizes, int n_in, void* d_out, int out_size, void* d_ws, size_t ws_size,
                              hipStream_t stream) {
    (void)hipFuncSetAttribute((const void*)s4ffn_kernel, hipFuncAttributeMaxDynamicSharedMemorySize, LDS_BYTES);
    s4ffn_kernel<<<256, 512, LDS_BYTES, stream>>>((const u16*)d_in[0], (const u16*)d_in[1], (const u16*)d_in[2], (const u16*)d_in[3],
        (const u16*)d_in[4], (const u16*)d_in[5], (const u16*)d_in[6], (const u16*)d_in[7], (const u16*)d_in[8],
        (const u16*)d_in[10], (const u16*)d_in[11], (u16*)d_out, (char*)d_ws);
}
```

```cpp
#include <hip/hip_runtime.h>
#include <stdint.h>

typedef unsigned short u16;
typedef unsigned int u32;
typedef unsigned long long u64;
typedef __attribute__((ext_vector_type(8))) __bf16 bf8;
typedef __attribute__((ext_vector_type(2))) __bf16 bf2;
typedef __attribute__((ext_vector_type(4))) short s4;
typedef __attribute__((ext_vector_type(4))) float f4;
typedef __attribute__((ext_vector_type(16))) float f16v;
typedef __attribute__((ext_vector_type(2))) float f2;
typedef __attribute__((ext_vector_type(4))) u32 u4;
typedef __attribute__((ext_vector_type(2))) u32 u2;
#define LAS __attribute__((address_space(3)))
#define DI __device__ __forceinline__
#define LDS(T, off) (*(LAS T*)(uintptr_t)(u32)(off))

constexpr u32 WS_EPOCH = 0;
constexpr u32 WS_STAT = 4096;
constexpr u32 WS_AR = WS_STAT + 16384;
constexpr u32 WS_XG = 65536;
constexpr u32 WS_YG = WS_XG + 3670016;
constexpr u32 WS_H = WS_YG + 3670016;
constexpr u32 WS_STAMP = 32u << 20;
constexpr u32 L_TAB = 0;
constexpr u32 L_CS = 10240;
constexpr u32 L_ST = 10752;
constexpr u32 L_SEL = 12288;
constexpr u32 L_SCR = 15360;
constexpr u32 L_KC = L_SCR + 2048;
constexpr u32 L_U = 23552;
constexpr u32 L_RING = 32768;
constexpr u32 L_XT = L_RING + 98304;
constexpr u32 LDS_BYTES = 163840;
constexpr int SPIN_MAX = 1 << 16;
constexpr u32 PF_LINES = 176;

#define STAMP(i)
#define STAMPC(i)
#define STAMPF(i)
#define FSTAMP(s)
#define ACC_BEGIN()
#define ACC_END(i)
#define STAMP_DECL
#define STAMP_FLUSH(n)

DI float bflo(u32 v) { return __uint_as_float(v << 16); }
DI float bfhi(u32 v) { return __uint_as_float(v & 0xffff0000u); }
DI u32 pkbf(float a, float b) { f2 v = {a, b}; return __builtin_bit_cast(u32, __builtin_convertvector(v, bf2)); }
DI float rbf(float a) { return bflo(pkbf(a, a)); }
DI f4 mfma(u4 a, u4 b, f4 c) { return __builtin_amdgcn_mfma_f32_16x16x32_bf16(__builtin_bit_cast(bf8, a), __builtin_bit_cast(bf8, b), c, 0, 0, 0); }
DI f16v mfma32(u4 a, u4 b, f16v c) { return __builtin_amdgcn_mfma_f32_32x32x16_bf16(__builtin_bit_cast(bf8, a), __builtin_bit_cast(bf8, b), c, 0, 0, 0); }
DI u2 trread(u32 addr) { return __builtin_bit_cast(u2, __builtin_amdgcn_ds_read_tr16_b64_v4i16((LAS s4*)(uintptr_t)addr)); }
DI u4 trread2(u32 a, u32 b) { const u2 l = trread(a), h = trread(b); return u4{l.x, l.y, h.x, h.y}; }
template <int C> DI float dpp(float x) { return __builtin_bit_cast(float, __builtin_amdgcn_mov_dpp(__builtin_bit_cast(int, x), C, 0xf, 0xf, true)); }
DI float rsum(float x) { x += dpp<0xB1>(x); x += dpp<0x4E>(x); x += dpp<0x141>(x); x += dpp<0x140>(x); return x; }
DI float rmax(float x) { x = fmaxf(x, dpp<0xB1>(x)); x = fmaxf(x, dpp<0x4E>(x)); x = fmaxf(x, dpp<0x141>(x)); return fmaxf(x, dpp<0x140>(x)); }
DI float sw32(float a, float b) { auto s = __builtin_amdgcn_permlane32_swap(__float_as_uint(a), __float_as_uint(b), false, false); return __uint_as_float(s[0]) + __uint_as_float(s[1]); }
DI float sw16(float a, float b) { auto s = __builtin_amdgcn_permlane16_swap(__float_as_uint(a), __float_as_uint(b), false, false); return __uint_as_float(s[0]) + __uint_as_float(s[1]); }
DI float dot2(u32 a, u32 b, float c) { return __builtin_amdgcn_fdot2_f32_bf16(__builtin_bit_cast(bf2, a), __builtin_bit_cast(bf2, b), c, false); }
DI f4 self4(int c, f4 a, f4 b) { return c ? a : b; }
DI f2 cmul(f2 a, f2 b) { float x, y;
    asm("v_mul_f32 %0, %2, %4\n\tv_mul_f32 %1, %3, %4\n\tv_fma_f32 %0, -%3, %5, %0\n\tv_fmac_f32 %1, %2, %5" : "=&v"(x), "=&v"(y) : "v"(a.x), "v"(a.y), "v"(b.x), "v"(b.y));
    return f2{x, y}; }
DI f2 cmulc(f2 a, f2 b) { float x, y;
    asm("v_mul_f32 %0, %2, %4\n\tv_mul_f32 %1, %3, %4\n\tv_fmac_f32 %0, %3, %5\n\tv_fma_f32 %1, -%2, %5, %1" : "=&v"(x), "=&v"(y) : "v"(a.x), "v"(a.y), "v"(b.x), "v"(b.y));
    return f2{x, y}; }
DI float fmul1(float a, float b) { float r; asm("v_mul_f32 %0, %1, %2" : "=v"(r) : "v"(a), "v"(b)); return r; }
typedef __attribute__((ext_vector_type(2))) _Float16 h2;
typedef __attribute__((ext_vector_type(8))) _Float16 h8;
DI h2 cvth(f2 a) { return __builtin_convertvector(a, h2); }
DI h2 hmul(h2 a, h2 b) { const h2 bx = {b.x, b.x}, by = {-b.y, b.y}; return __builtin_elementwise_fma(__builtin_shufflevector(a, a, 1, 0), by, a * bx); }
DI h2 hmulc(h2 a, h2 b) { const h2 bx = {b.x, b.x}, by = {b.y, -b.y}; return __builtin_elementwise_fma(__builtin_shufflevector(a, a, 1, 0), by, a * bx); }
DI f4 mfmah(u4 a, u4 b, f4 c) { return __builtin_amdgcn_mfma_f32_16x16x32_f16(__builtin_bit_cast(h8, a), __builtin_bit_cast(h8, b), c, 0, 0, 0); }
DI u32 relu2(u32 v) { typedef __attribute__((ext_vector_type(2))) short s2; const s2 z = {0, 0}; return __builtin_bit_cast(u32, __builtin_elementwise_max(__builtin_bit_cast(s2, v), z)); }
DI f2 zpow(float a2, float rev, float p) {
    float e = __builtin_amdgcn_exp2f(a2 * p), r = __builtin_amdgcn_fractf(rev * p);
    float x, y;
    asm("s_nop 0\n\tv_mul_f32 %0, %2, %3\n\tv_mul_f32 %1, %2, %4" : "=&v"(x), "=&v"(y) : "v"(e), "v"(__builtin_amdgcn_cosf(r)), "v"(__builtin_amdgcn_sinf(r)));
    return f2{x, y};
}
DI f4 quad(const f16v& v, int q) { return f4{v[4 * q], v[4 * q + 1], v[4 * q + 2], v[4 * q + 3]}; }

#define SB() __builtin_amdgcn_sched_barrier(0)
#define BARRIER() asm volatile("s_waitcnt lgkmcnt(0)\n\ts_barrier" ::: "memory")
#define WAITV(n) asm volatile("s_waitcnt vmcnt(" #n ")" ::: "memory")
DI void dma16(u32 voff, const void* base, u32 lds) {
    asm volatile("s_mov_b32 m0, %2\n\ts_nop 0\n\tglobal_load_lds_dwordx4 %0, %1" :: "v"(voff), "s"(base), "s"(lds) : "memory");
}
#define SETM0(lds) asm volatile("s_mov_b32 m0, %0\n\ts_nop 0" :: "s"(lds) : "memory")
#define DMAO(voff, base, off) asm volatile("global_load_lds_dwordx4 %0, %1 offset:" #off :: "v"(voff), "s"(base) : "memory")
#define GLD4(dst, voff, base, mod) asm volatile("global_load_dwordx4 %0, %1, %2 " mod : "+v"(dst) : "v"(voff), "s"(base) : "memory")
#define GLD2(dst, voff, base, mod) asm volatile("global_load_dwordx2 %0, %1, %2 " mod : "+v"(dst) : "v"(voff), "s"(base) : "memory")
#define GLD1(dst, voff, base) asm volatile("global_load_dword %0, %1, %2" : "+v"(dst) : "v"(voff), "s"(base) : "memory")
#define GST4(voff, val, base, mod) asm volatile("global_store_dwordx4 %0, %1, %2 " mod "\n\ts_nop 1" :: "v"(voff), "v"(val), "s"(base) : "memory")
#define GST2(voff, val, base, mod) asm volatile("global_store_dwordx2 %0, %1, %2 " mod :: "v"(voff), "v"(val), "s"(base) : "memory")
#define RLD2(dst, voff, base) asm volatile("global_load_dwordx2 %0, %1, %2 sc1\n\ts_waitcnt vmcnt(0)" : "=&v"(dst) : "v"(voff), "s"(base) : "memory")
#define RLD4(dst, voff, base) asm volatile("global_load_dwordx4 %0, %1, %2 sc1\n\ts_waitcnt vmcnt(0)" : "=&v"(dst) : "v"(voff), "s"(base) : "memory")
#define PREFETCH(voff, base) asm volatile("global_load_dword v255, %0, %1" :: "v"(voff), "s"(base) : "v255", "memory")
#define PIN(x) asm volatile("" : "+v"(x))
#define UNDEF(x) asm volatile("" : "=v"(x))

struct ConstTab { u32 tix[3][128][4]; u32 sel[3][64][4]; u32 red[64][4]; };
constexpr int cidx(int l) {
    if (l < 0) return 16 + l;
    const int s = (l >= 256) + (l >= 512) + (l >= 1024);
    return 16 + 128 * s + (s ? (l + (1 << (s - 1)) - 1 + ((l >> s) & 1)) >> s : l);
}
constexpr ConstTab make_tab() {
    ConstTab c{};
    for (int t = 0; t < 128; ++t) {
        u32 v[12] = {};
        for (int i = 0; i < 5; ++i) {
            const int u = t + 128 * i < 607 ? t + 128 * i : 607, pi = u >= 304, ip = u - 304 * pi, top = (ip < 24 ? 1056 : 592) - pi - 2 * ip;
            v[i] = (u32)cidx(top > 1028 ? -1 : top) * 2 | (u32)cidx(top - 1 > 1028 ? -1 : top - 1) << 17;
        }
        v[5] = (404 + (t >> 1) + 2 * (t & 1)) * 0x20002u + 0x20000u;
        v[6] = v[5] + 64 * 0x20002u;
        for (int i = 0; i < 3; ++i) {
            const int u = t + 128 * i < 303 ? t + 128 * i : 303, q = u & 3, m = 2 * (52 + (u >> 2)) + (q < 2 ? 2 - 4 * q : 9 - 4 * q);
            v[7 + i] = (u32)cidx(m >= 129 ? 8 * m - (m == 256) : -1) * 2 | (u32)cidx(m - 2 >= 129 ? 8 * (m - 2) : -1) << 17;
        }
        for (int i = 0; i < 12; ++i) c.tix[i >> 2][t][i & 3] = v[i];
    }
    for (int l = 0; l < 64; ++l) {
        const int fl = l & 15, g = l >> 4, x2 = fl & 7;
        const int m2 = x2 < 2 ? -2 : x2 < 5 ? -1 : 0;
        for (int q = 0; q < 4; ++q)
            for (int h = 0; h < 2; ++h) {
                const int e2 = x2 - 2 * q - h, e = 8 * g + 2 * q + h - 8 - fl;
                c.sel[0][l][q] |= (u32)(((e2 >= -5) + (e2 >= -2) + (e2 >= 3) + (e2 >= 6) - 2 - m2) * 0x0202 + 0x0100) << (16 * h);
                c.sel[1][l][q] |= (u32)(e >= -3 && e <= 3 ? 0x3f80 : e >= -4 && e <= 4 ? 0x3f81 : 0) << (16 * h);
            }
        const int mul = fl < 8 ? 8 : fl < 12 ? 4 : 2, base = fl < 8 ? fl : fl < 12 ? 248 + fl : 384 + (fl & 1) + 64 * ((fl - 12) >> 1);
        c.red[l][0] = L_KC + (16 + base + mul * 4 * g) * 2; c.red[l][1] = 2 * mul;
        c.red[l][2] = L_KC + (fl < 4 ? (528 + 32 * fl + 4 * g) * 2 : fl == 4 && g == 0 ? 1312 : 1528);
        c.red[l][3] = L_KC + (fl < 4 ? (544 + 32 * fl + 4 * g) * 2 : 1528);
        c.sel[2][l][0] = L_U + 2 * (224 + 16 * fl + 8 * g);
        c.sel[2][l][1] = L_TAB + 4 * ((fl & 1) * 304 + ((16 - fl + 8 * g) >> 1));
        c.sel[2][l][2] = L_TAB + 2432 + 8 * (2 * (2 + (fl >> 3) - g) + m2 - 4);
        c.sel[2][l][3] = L_TAB + 3456 + 16 * (12 + fl - 4 * g);
    }
    return c;
}
static __device__ const ConstTab CTAB __attribute__((aligned(128))) = make_tab();

template <int TS>
DI void conv_steps(f4 (&acc)[4], int k0, int n, bool dma, u32 ub, u32 la, u32 q2, const u4& sel2, u32 vq0, u32 vq1, u32 vq2, u32 vq3, const char* src, u32 dst) {
    constexpr int T[2][4] = {{0, 3, 4, 7}, {1, 2, 5, 6}};
    constexpr int NT = T[TS][3] + 1;
#pragma unroll 1
    for (int kk = 0; kk < n; ++kk, ++k0) {
        u4 A[4], B[NT];
        u32 u = ub - 64 * k0, l = la + 608 - 64 * k0, q = q2 + 64 * k0;
        PIN(u); PIN(l); PIN(q);
        auto lin = [&](u32 a) { u4 r = {LDS(u32, a), LDS(u32, a + 4), LDS(u32, a + 8), LDS(u32, a + 12)}; return r; };
        auto seg2 = [&](u2 v) { u4 r;
            r.x = __builtin_amdgcn_perm(v.y, v.x, sel2.x); r.y = __builtin_amdgcn_perm(v.y, v.x, sel2.y);
            r.z = __builtin_amdgcn_perm(v.y, v.x, sel2.z); r.w = __builtin_amdgcn_perm(v.y, v.x, sel2.w); return r; };
        A[0] = lin(l + 512);
#pragma unroll
        for (int i = 0; i < 4; ++i) B[T[TS][i]] = LDS(u4, u + 512 * T[TS][i]);
        A[1] = lin(l);
#pragma unroll
        for (int s = 0; s < NT; ++s) if (s != T[TS][0] && s != T[TS][1] && s != T[TS][2] && s != T[TS][3]) B[s] = LDS(u4, u + 512 * s);
        const u2 v2 = LDS(u2, q), v3 = LDS(u2, q + 512);
        SETM0(dst + kk * 16384);
        SB();
        A[2] = seg2(v2); A[3] = seg2(v3);
        if (k0 == 0) {
            A[2] = lin(la + 96);
            const u4 a4 = lin(la);
            acc[2] = mfma(a4, B[T[TS][2] - 4], acc[2]); acc[3] = mfma(a4, B[T[TS][3] - 4], acc[3]);
        }
#pragma unroll
        for (int d = 0; d < 4; ++d) {
#pragma unroll
            for (int i = 0; i < 4; ++i)
                if (T[TS][i] >= d) acc[i] = mfma(A[d], B[T[TS][i] - d], acc[i]);
            if (dma) { SB(); if (d == 0) DMAO(vq0, src, 0); if (d == 1) DMAO(vq1, src, 1024); if (d == 2) DMAO(vq2, src, 2048); if (d == 3) DMAO(vq3, src, 3072); SB(); }
        }
        src += 4 * 7168;
    }
}

template <int TS>
DI void conv_far(f4 (&acc)[4], u32 ub, u32 te, const u4& box) {
    constexpr int T2 = TS ? 5 : 4, T3 = TS ? 6 : 7, NS = T3 - 3;
    const u4 b9 = box & 0xfffefffeu, b7 = box & ~((box & 0x00010001u) * 0xffffu);
    u4 Ue[NS], Uo[NS], Tt[4];
#pragma unroll
    for (int s = 0; s < NS; ++s) { Ue[s] = LDS(u4, ub - 16 + 512 * s); Uo[s] = LDS(u4, ub + 512 * s); Tt[s] = LDS(u4, te + 256 * s); }
#pragma unroll
    for (int s = 0; s < NS; ++s) {
        const f4 e = mfma(Ue[s], b9, f4{0, 0, 0, 0}), o = mfma(Uo[s], b7, f4{0, 0, 0, 0});
        const u4 z = {pkbf(e.x, e.y), pkbf(e.z, e.w), pkbf(o.x, o.y), pkbf(o.z, o.w)};
        acc[3] = mfma(z, Tt[T3 - 4 - s], acc[3]);
        if (s <= T2 - 4) acc[2] = mfma(z, Tt[T2 - 4 - s], acc[2]);
    }
}

__global__ __launch_bounds__(512, 2) void s4ffn_kernel(const u16* __restrict__ x, const u16* __restrict__ Are, const u16* __restrict__ Aim,
        const u16* __restrict__ Bre, const u16* __restrict__ Bim, const u16* __restrict__ Cre, const u16* __restrict__ Cim,
        const u16* __restrict__ logdt, const u16* __restrict__ Dsk, const u16* __restrict__ w1, const u16* __restrict__ w2,
        u16* __restrict__ out, char* __restrict__ ws) {
    const int tid = threadIdx.x, lane = tid & 63, wave = __builtin_amdgcn_readfirstlane(tid >> 6);
    const u32 b = blockIdx.x, xcd = b & 7, idx = b >> 3;
    const int fl = lane & 15, g = lane >> 4, l31 = lane & 31, hh = lane >> 5;
    STAMP_DECL;
    {
        u64 pc; asm volatile("s_getpc_b64 %0" : "=s"(pc));
        pc &= ~127ull;
        if (tid < 8 && idx + tid * 32 < PF_LINES) PREFETCH((idx + tid * 32) * 128, pc);
    }
    STAMP(0); STAMPC(22);
    asm volatile("" :: "s"(x), "s"(Are), "s"(Aim), "s"(Bre), "s"(Bim), "s"(Cre), "s"(Cim), "s"(logdt), "s"(Dsk), "s"(w1), "s"(w2), "s"(out), "s"(ws));
    u32 tag;
    const u32 w4 = wave & 3;
    u4 xr0, xr1; u32 pq0, pq1, pq2, pq3, pq4, pq5;
    if (wave < 4)
        asm volatile("v_mov_b64 v[232:233], 0\n\tv_mov_b64 v[234:235], 0\n\tv_mov_b64 v[236:237], 0\n\tv_mov_b64 v[238:239], 0\n\ts_mov_b32 exec_hi, 0xffffff\n\t"
                     "global_load_dwordx4 v[232:235], %0, %1\n\tglobal_load_dwordx4 v[236:239], %0, %1 offset:896\n\ts_mov_b32 exec_hi, -1"
                     :: "v"(lane * 16), "s"((const char*)x + (b * 8 + 2 * w4) * 896) : "v232", "v233", "v234", "v235", "v236", "v237", "v238", "v239", "memory");
    else
        asm volatile("global_load_dword v240, %0, %1\n\tglobal_load_dword v241, %0, %2\n\tglobal_load_dword v242, %0, %3\n\t"
                     "global_load_dword v243, %0, %4\n\tglobal_load_dword v244, %0, %5\n\tglobal_load_dword v245, %0, %6"
                     :: "v"(min(28 * xcd + idx, 223u) * 1024 + w4 * 256 + lane * 4), "s"(Are), "s"(Aim), "s"(Bre), "s"(Bim), "s"(Cre), "s"(Cim)
                     : "v240", "v241", "v242", "v243", "v244", "v245", "memory");
    asm volatile("s_load_dword s84, %0, 0x0\n\ts_load_dword s85, %1, %3\n\ts_load_dword s86, %2, %3" :: "s"(ws), "s"(logdt), "s"(Dsk), "s"(min(28 * xcd + idx, 223u) * 4) : "s84", "s85", "s86");
    u32 ldt2, dsk2;
#define SCAL_WAIT() asm volatile("s_waitcnt lgkmcnt(0)" : "={s84}"(tag), "={s85}"(ldt2), "={s86}"(dsk2) :: "memory")
#define ROWS_WAIT(n) asm volatile("s_waitcnt vmcnt(" #n ")" : "={v[232:235]}"(xr0), "={v[236:239]}"(xr1) :: "memory")
#define PARS_WAIT(n) asm volatile("s_waitcnt vmcnt(" #n ")" : "={v240}"(pq0), "={v241}"(pq1), "={v242}"(pq2), "={v243}"(pq3), "={v244}"(pq4), "={v245}"(pq5) :: "memory")
#define LN_ROWS() do { \
        float s0 = 0, s1 = 0, q0 = 0, q1 = 0; \
        _Pragma("unroll") for (int i = 0; i < 4; ++i) { s0 = dot2(xr0[i], 0x3f803f80u, s0); s1 = dot2(xr1[i], 0x3f803f80u, s1); q0 = dot2(xr0[i], xr0[i], q0); q1 = dot2(xr1[i], xr1[i], q1); } \
        const u32 v_ = __float_as_uint(rsum(sw16(sw32(s0, s1), sw32(q0, q1))) * (1.f / 448.f)); \
        auto e_ = __builtin_amdgcn_permlane16_swap(v_, v_, false, false);     \
        const float mean = __uint_as_float(e_[0]), var = fmaxf(__uint_as_float(e_[1]) - mean * mean, 0.f); \
        const float mb = rbf(mean), sd_ = rbf(__builtin_amdgcn_sqrtf(rbf(rbf(var) + 1e-5f))), a_ = __builtin_amdgcn_rcpf(sd_), c_ = -mb * a_; \
        const u32 st_ = (__float_as_uint(mb) >> 16) | __float_as_uint(sd_); \
        auto a2_ = __builtin_amdgcn_permlane32_swap(__float_as_uint(a_), __float_as_uint(a_), false, false); \
        auto c2_ = __builtin_amdgcn_permlane32_swap(__float_as_uint(c_), __float_as_uint(c_), false, false); \
        u4 o0, o1; \
        _Pragma("unroll") for (int i = 0; i < 4; ++i) { \
            o0[i] = pkbf(fmaf(bflo(xr0[i]), __uint_as_float(a2_[0]), __uint_as_float(c2_[0])), fmaf(bfhi(xr0[i]), __uint_as_float(a2_[0]), __uint_as_float(c2_[0]))); \
            o1[i] = pkbf(fmaf(bflo(xr1[i]), __uint_as_float(a2_[1]), __uint_as_float(c2_[1])), fmaf(bfhi(xr1[i]), __uint_as_float(a2_[1]), __uint_as_float(c2_[1]))); } \
        if (lane < 56) { LDS(u4, L_XT + w4 * 1920 + lane * 16) = o0; LDS(u4, L_XT + w4 * 1920 + 960 + lane * 16) = o1; } \
        if (l31 == 0) LDS(u32, L_XT + w4 * 1920 + hh * 960 + 896) = st_; } while (0)
#define LN_PUBLISH() do { \
        const u32 a_ = L_XT + (fl >> 2) * 960 + w4 * 128 + g * 32 + (lane & 3) * 8; \
        const u4 v0 = trread2(a_, a_ + 3840), v1 = trread2(a_ + 512, a_ + 3840 + 512); \
        GST4(lane * 16, v0, ws + WS_XG + b * 7168 + w4 * 1024, "sc1"); \
        if (w4 < 3) GST4(lane * 16, v1, ws + WS_XG + b * 7168 + w4 * 1024 + 4096, "sc1"); \
        else if (lane < 2) { const u32 s_ = L_XT + lane * 3840 + 896; const u4 st = {LDS(u32, s_), LDS(u32, s_ + 960), LDS(u32, s_ + 1920), LDS(u32, s_ + 2880)}; \
            GST4(lane * 16, st, ws + WS_STAT + b * 32, "sc1"); } } while (0)
    if (idx >= 28) {
        auto prefetch = [&](u32 k, u32 j) {
            const u32 n = ((k * 4 + idx - 28) * 8 + j) * 64 + lane, m = n - 7168;
            const u32 o = n < 7168 ? (n / 896) * 200704 + n % 896 * 128 : m / 672 * 200704 + 114688 + m % 672 * 128;
            PREFETCH(min(o, 1605504u), w2);
        };
        if (wave < 4) {
            ROWS_WAIT(0);
            STAMP(1);
            LN_ROWS();
            BARRIER();
            __builtin_amdgcn_s_sleep(8);
            prefetch(1, wave); prefetch(1, wave + 4);
        } else {
            prefetch(0, wave - 4); prefetch(0, wave);
            BARRIER();
            LN_PUBLISH();
            WAITV(0);
        }
        SCAL_WAIT();
        BARRIER();
        tag += 1;
        STAMP(3);
        if (tid == 256) { const u2 ar = {tag, tag}; GST2(0, ar, ws + WS_AR + b * 8, "sc1"); }
#pragma unroll 1
        for (u32 k = 2; k < 7; ++k) prefetch(k, wave);
        if (b == 224 && wave == 0) {
            u4 t0, t1;
#pragma unroll 1
            for (int sp = 0; sp < SPIN_MAX; ++sp) {
                RLD4(t0, lane * 16, ws + WS_AR); RLD4(t1, lane * 16, ws + WS_AR + 1024);
                if (!__any(((t0.x ^ tag) | (t0.z ^ tag) | (t1.x ^ tag) | (t1.z ^ tag)) != 0)) break;
            }
            if (lane == 0) { const u2 e = {tag, tag}; GST2(0, e, ws + WS_EPOCH, "sc1"); }
        }
        WAITV(0);
        STAMP(5); STAMP_FLUSH(24);
        return;
    }
    const bool isF = wave < 4;
    const u32 q7 = idx / 7, r7 = idx - 7 * q7;
    const u32 rp = 4 * (xcd >> 1) + q7, cc = 7 * (xcd & 1) + r7, P = 4 * xcd + q7, ct = r7, dp = 28 * xcd + idx;
    const u32 rg = w4 >> 1, ch = wave & 1, hi4 = wave >> 2;
    u32 kt = (r7 + 2 * q7) % 7;
    const int r8 = lane >> 3, p8 = lane & 7, rw = (lane >> 2) & 7;
    u32 vo0 = (32 * w4 + r8) * 896 + 16 * (p8 ^ (r8 >> 1)), vo1 = ((vo0 + 8 * 896) ^ 64) + 2048, vo2 = vo0 + 16 * 896 + 1024, vo3 = vo1 + 16 * 896 - 2048;
    u32 vo4 = (16 * w4 + rw) * 3584 + 64 * hh + 16 * ((lane & 3) ^ (rw >> 2)), vo5 = vo4 + 128 + 2048, vo6 = ((vo4 + 8 * 3584) ^ 32) + 1024, vo7 = vo6 + 128 - 1024;
    vo0 += 3072; vo4 += 3072;
    const char *xb = (const char*)x + rp * 114688 - 3072, *wb = (const char*)w1 + cc * 256 - 3072;
    const char *pa, *pw;
#define RING_SET() do { asm volatile("" : "+s"(kt)); pa = xb + kt * 128; pw = wb + kt * 229376; kt = kt == 6 ? 0 : kt + 1; } while (0)
#define DMA_X(i) do { if ((i) == 0) DMAO(vo0, pa, 0); if ((i) == 1) DMAO(vo1, pa, 1024); if ((i) == 2) DMAO(vo2, pa, 2048); if ((i) == 3) DMAO(vo3, pa, 3072); } while (0)
#define DMA_W(i) do { if ((i) == 0) DMAO(vo4, pw, 0); if ((i) == 1) DMAO(vo5, pw, 1024); if ((i) == 2) DMAO(vo6, pw, 2048); if ((i) == 3) DMAO(vo7, pw, 3072); } while (0)
#define DMA_X4(l_) do { SETM0(l_); DMA_X(0); DMA_X(1); DMA_X(2); DMA_X(3); } while (0)
#define DMA_W4(l_) do { SETM0((l_) + 16384); DMA_W(0); DMA_W(1); DMA_W(2); DMA_W(3); } while (0)
    const u32 lw = L_RING + w4 * 4096;
    u4 sel2, sel3; u32 cv_ub, cv_la, cv_q2, cv_p3, hm0, hm1, hm2, hm3;
#define SEL_READ() do { sel2 = LDS(u4, L_SEL + lane * 16); sel3 = LDS(u4, L_SEL + 1024 + lane * 16); const u4 c_ = LDS(u4, L_SEL + 2048 + lane * 16); \
        cv_ub = c_.x + rg * 4608; cv_la = c_.y + rg * 5120; cv_q2 = c_.z + rg * 5120; cv_p3 = c_.w + rg * 5120; } while (0)
#define SCALARS_DONE() do { tag += 1; const u32 pat = 1 + tag % 254; hm0 = (pat & 1) << 15 | (pat & 2) << 30; hm1 = (pat & 4) << 13 | (pat & 8) << 28; \
        hm2 = (pat & 16) << 11 | (pat & 32) << 26; hm3 = (pat & 64) << 9 | (pat & 128) << 24; } while (0)
    const char* hb = ws + WS_H + ((2 * rp + rg) * 112 + 8 * cc + 4 * ch) * 2048;
    const u32 vw2 = r8 * 896 + 16 * (p8 ^ (4 * ((r8 >> 1) & 1)));
    const char* w2b = (const char*)w2 + wave * (224 * 896) + ct * 128;
    const u32 pb0 = lw + (isF ? 32768 : 98304), pb1 = lw + (isF ? 65536 : 0);
    u4 Bw[14][2];
    const u32 aw2a = pb0 + (4 * hh + (fl >> 2)) * 128 + 16 * ((2 * (g & 1) + ((lane & 3) >> 1)) ^ (4 * ((fl >> 3) & 1))) + 8 * (lane & 1);
    const u32 aw2b = aw2a ^ 64, aw2c = aw2a - pb0 + pb1, aw2d = aw2c ^ 64;
#define W2_RD(j, p, n) do { const u32 o_ = ((n) >> 1) * 16384 + ((n) & 1) * 2048; \
        Bw[j][0] = trread2(((p) ? aw2c : aw2a) + o_, ((p) ? aw2c : aw2a) + o_ + 1024); Bw[j][1] = trread2(((p) ? aw2d : aw2b) + o_, ((p) ? aw2d : aw2b) + o_ + 1024); } while (0)
    const u32 vq0 = vw2 + 3072, vq1 = vw2 + 7168 + 2048, vq2 = vw2 + 14336 + 1024, vq3 = vw2 + 21504;
#define W2_4(src, dst) do { SETM0(dst); DMAO(vq0, src, 0); DMAO(vq1, src, 1024); DMAO(vq2, src, 2048); DMAO(vq3, src, 3072); } while (0)
#define W2_PIECE(n, dst) do { W2_4(w2b - 3072 + (n) * 57344, dst); W2_4(w2b - 3072 + (n) * 57344 + 28672, (dst) + 16384); } while (0)
#define W2_DRAIN(n, p) do { W2_RD(4 * (n), p, 0); W2_RD(4 * (n) + 1, p, 1); W2_RD(4 * (n) + 2, p, 2); W2_RD(4 * (n) + 3, p, 3); \
        _Pragma("unroll") for (int j = 4 * (n); j < 4 * (n) + 4; ++j) { PIN(Bw[j][0]); PIN(Bw[j][1]); } } while (0)
    f4 cacc[4];
#pragma unroll
    for (int i = 0; i < 4; ++i) cacc[i] = f4{0, 0, 0, 0};
#define CONV(k0, n, dma, src, dst) do { if (ch) conv_steps<1>(cacc, k0, n, dma, cv_ub, cv_la, cv_q2, sel2, vq0, vq1, vq2, vq3, src, dst); \
        else conv_steps<0>(cacc, k0, n, dma, cv_ub, cv_la, cv_q2, sel2, vq0, vq1, vq2, vq3, src, dst); } while (0)
    if (isF) {
        __builtin_amdgcn_s_setprio(2); RING_SET(); DMA_X4(lw); DMA_W4(lw); __builtin_amdgcn_s_setprio(1); RING_SET();
        DMA_X4(lw + 32768);
        __builtin_amdgcn_s_setprio(0);
        f16v acc[2][2]; f4 cs = {0, 0, 0, 0};
#pragma unroll
        for (int i = 0; i < 16; ++i) { acc[0][0][i] = 0; acc[0][1][i] = 0; acc[1][0][i] = 0; acc[1][1][i] = 0; }
        const u32 o1 = ((g ^ lane) & 1) ? 0 : 0x3f803f80u; u4 ones = {o1, o1, o1, o1};
        u32 ax0 = L_RING + (64 * rg + l31) * 128 + 16 * (hh ^ ((l31 >> 1) & 7)), ax1 = ax0 ^ 32, ax2 = ax0 ^ 64, ax3 = ax0 ^ 96;
        u32 aw0 = L_RING + 16384 + 2048 * hh + 1024 * ch + 512 * rg + 64 * (fl >> 2) + 16 * ((2 * (g & 1) + ((lane & 3) >> 1)) ^ (2 * hh)) + 8 * (lane & 1);
        u32 aw1 = aw0 ^ 272, aw2 = aw0 ^ 512, aw3 = aw0 ^ 784;
        u4 X0[4], W0[4], X1[4], W1[4];
#define G1_RD(i, X, W, a0, a1, o) do { if ((i) == 0) { X[0] = LDS(u4, a0); W[0] = trread2(aw0 + (o), aw1 + (o)); } \
        if ((i) == 1) { X[1] = LDS(u4, a0 + 4096); W[1] = trread2(aw2 + (o), aw3 + (o)); } \
        if ((i) == 2) { X[2] = LDS(u4, a1); W[2] = trread2(aw0 + (o) + 4096, aw1 + (o) + 4096); } \
        if ((i) == 3) { X[3] = LDS(u4, a1 + 4096); W[3] = trread2(aw2 + (o) + 4096, aw3 + (o) + 4096); } } while (0)
#define G1_PHASE(X, W, RD, DM, EX) do { \
        acc[0][0] = mfma32(W[0], X[0], acc[0][0]); RD(0); SB(); \
        acc[0][1] = mfma32(W[0], X[1], acc[0][1]); RD(1); SB(); \
        cs = mfma(W[0], ones, cs); SB(); \
        acc[1][0] = mfma32(W[1], X[0], acc[1][0]); DM(0); EX(0); SB(); \
        acc[1][1] = mfma32(W[1], X[1], acc[1][1]); RD(2); SB(); \
        acc[0][0] = mfma32(W[2], X[2], acc[0][0]); DM(1); RD(3); SB(); \
        cs = mfma(W[2], ones, cs); SB(); \
        acc[0][1] = mfma32(W[2], X[3], acc[0][1]); DM(2); EX(1); SB(); \
        acc[1][0] = mfma32(W[3], X[2], acc[1][0]); EX(2); SB(); \
        acc[1][1] = mfma32(W[3], X[3], acc[1][1]); DM(3); EX(3); SB(); } while (0)
#define RDA(i) G1_RD(i, X1, W1, ax2, ax3, 8192)
#define RDB(i) G1_RD(i, X0, W0, ax0, ax1, 0)
#define NONE(i)
#define EXA(i) do { if ((i) == 0) { ax0 ^= m_; ax1 ^= m_; asm volatile("" : "+s"(kt)); pa = s < 4 ? xb + kt * 128 : w2b - 3072 + (s - 4) * 57344; } \
        if ((i) == 1) { aw0 ^= m_; aw1 ^= m_; ax2 ^= m_; } if ((i) == 2) { aw2 ^= m_; aw3 ^= m_; ax3 ^= m_; \
            if (s == 4) { vo0 = vw2 + 3072; vo1 = vw2 + 7168 + 2048; vo2 = vw2 + 14336 + 1024; vo3 = vw2 + 21504; } } \
        if ((i) == 3) { ld = lw + sl * 32768; SETM0(ld); } } while (0)
#define EXB(i) do { if ((i) == 0) { pw = s < 4 ? wb + kt * 229376 : pa; kt = kt == 6 ? 0 : kt + 1; } \
        if ((i) == 1) { sl = sl == 2 ? 0 : sl + 1; m_ = 0x8000u * (sl == 0 ? 3 : sl); } \
        if ((i) == 2) { if (s == 4) { vo4 = vw2 + 28672 + 3072; vo5 = vw2 + 35840 + 2048; vo6 = vw2 + 43008 + 1024; vo7 = vw2 + 50176; } } \
        if ((i) == 3) SETM0(ld + 16384); } while (0)
        PIN(acc[0][0]); PIN(acc[0][1]); PIN(acc[1][0]); PIN(acc[1][1]);
        PIN(ax0); PIN(ax1); PIN(ax2); PIN(ax3); PIN(aw0); PIN(aw1); PIN(aw2); PIN(aw3); PIN(ones);
        __builtin_amdgcn_s_setprio(2);
        ROWS_WAIT(12);
        STAMP(1);
        DMA_W4(lw + 32768);
        RING_SET();
        LN_ROWS();
        SCAL_WAIT();
        SCALARS_DONE();
        __builtin_amdgcn_s_setprio(0);
        STAMP(2);
        WAITV(8);
        BARRIER();
        RDB(0); RDB(1); RDB(2); RDB(3);
        DMA_X4(lw + 65536);
        SETM0(lw + 65536 + 16384);
        STAMP(3);
        u32 ld, sl = 0, m_ = 0x18000;
#pragma unroll 1
        for (u32 s = 0; s < 6; ++s) {
            G1_PHASE(X0, W0, RDA, DMA_W, EXA);
            FSTAMP(s);
            WAITV(8); BARRIER();
            SB();
            G1_PHASE(X1, W1, RDB, DMA_X, EXB);
        }
        G1_PHASE(X0, W0, RDA, DMA_W, NONE);
        G1_PHASE(X1, W1, NONE, NONE, NONE);
        if (fl < 2) LDS(f4, L_CS + (64 * ch + 32 * rg + 16 * fl + 4 * g) * 4) = cs;
        STAMP(4);
        BARRIER();
        u4 ho[8];
#pragma unroll
        for (int tt = 0; tt < 2; ++tt) {
            const u32 sv = LDS(u32, L_ST + (64 * rg + 32 * tt + l31) * 4);
            const float m = -bflo(sv), rs = __builtin_amdgcn_rcpf(bfhi(sv));
            const f2 m2 = {m, m}, rs2 = {rs, rs};
#pragma unroll
            for (int c = 0; c < 2; ++c)
#pragma unroll
                for (int k = 0; k < 2; ++k)
#pragma unroll
                    for (int q = 0; q < 2; ++q) {
                        const f4 cv = LDS(f4, L_CS + ((64 * ch + 32 * rg + 4 * hh) * 4 ^ (128 * c)) + (2 * k + q) * 32), av = quad(acc[c][tt], 2 * k + q);
                        const f2 v0 = __builtin_elementwise_fma(m2, f2{cv.x, cv.y}, f2{av.x, av.y}) * rs2, v1 = __builtin_elementwise_fma(m2, f2{cv.z, cv.w}, f2{av.z, av.w}) * rs2;
                        ho[4 * tt + 2 * c + k][2 * q] = relu2(pkbf(v0.x, v0.y)) | (q ? hm2 : hm0); ho[4 * tt + 2 * c + k][2 * q + 1] = relu2(pkbf(v1.x, v1.y)) | (q ? hm3 : hm1);
                    }
        }
#pragma unroll
        for (int i = 0; i < 8; ++i) PIN(ho[i]);
        SEL_READ();
        STAMP(5);
        BARRIER();
        STAMP(6);
#pragma unroll
        for (int i = 0; i < 4; ++i) { const char* hp = hb + (2 * ((i >> 1) ^ rg) + (i & 1)) * 2048; GST4(lane * 16, ho[i], hp, "sc1"); GST4(lane * 16, ho[4 + i], hp, "offset:1024 sc1"); }
        STAMP(12);
        WAITV(8);
        W2_DRAIN(0, 0); W2_DRAIN(1, 1);
    } else {
        __builtin_amdgcn_s_setprio(1);
        SCAL_WAIT();
        SCALARS_DONE();
        STAMP(1);
        const float dt = rbf(expf(rg ? bfhi(ldt2) : bflo(ldt2)));
        u32 sr = L_SCR + w4 * 4352 + fl * 272 + g * 16; PIN(sr);
        asm volatile("s_mov_b32 m0, %0" :: "s"(L_SCR + w4 * 4352));
        PARS_WAIT(0);
        STAMP(2);
        f2 c0 = cmulc(f2{bflo(pq4), -bflo(pq5)}, f2{dt * bflo(pq2), dt * bflo(pq3)}), c1 = cmulc(f2{bfhi(pq4), -bfhi(pq5)}, f2{dt * bfhi(pq2), dt * bfhi(pq3)});
        float isc;
        {
            float m = rmax(fmaxf(fmaxf(fabsf(c0.x), fabsf(c0.y)), fmaxf(fabsf(c1.x), fabsf(c1.y))));
            auto s1 = __builtin_amdgcn_permlane16_swap(__float_as_uint(m), __float_as_uint(m), false, false); u32 e = max(s1[0], s1[1]);
            auto s2 = __builtin_amdgcn_permlane32_swap(e, e, false, false); e = max(s2[0], s2[1]) & 0x7f800000u;
            isc = __uint_as_float(e);
            const float sc = __uint_as_float(0x7f000000u - e);
            c0.x *= sc; c0.y *= sc; c1.x *= sc; c1.y *= sc;
        }
        f4 dk[4];
#pragma unroll
        for (int t = 0; t < 4; ++t) dk[t] = f4{0, 0, 0, 0};
        u2 arf; u32 stv; u4 xg0, xg1;
        const u32 xo = ((tid & 255) >> 1) * 7168 + (tid & 1) * 16;
#define SBAR() do { SB(); ACC_BEGIN(); asm volatile("s_barrier" ::: "memory"); ACC_END(21); SB(); } while (0)
#define PUT(e, v) asm volatile("ds_write_addtid_b32 %0 offset:%1" :: "v"(v), "i"((e) * 272) : "memory")
#define RD4(A) _Pragma("unroll") for (int s = 0; s < 4; ++s) A[s] = LDS(u4, sr + s * 64)
#define MM2(h, s) do { dk[2 * (h)] = mfmah(A[s], B0[s], dk[2 * (h)]); dk[2 * (h) + 1] = mfmah(A[s], B1[s], dk[2 * (h) + 1]); } while (0)
        u4 A[4], B0[4], B1[4];
        f2 z1, z8, z32, z256;
        h2 w8, w16, w24, a32, a64, a96, a128;
#define KG_POW(hi) do { const float a2 = fmul1(dt, (hi) ? bfhi(pq0) : bflo(pq0)) * 1.44269504089f, rev = fmul1(dt, (hi) ? bfhi(pq1) : bflo(pq1)) * 0.15915494309f; \
        z1 = zpow(a2, rev, 1.f); z8 = zpow(a2, rev, 8.f); z32 = zpow(a2, rev, 32.f); z256 = zpow(a2, rev, 256.f); } while (0)
        f2 z512, e4, e5; h2 f1, f2_, f4_, g0, g1, g2, g3;
#define KG_COLS_A(c, PEND) do { \
        const f2 z2 = cmul(z1, z1), e1 = cmulc(c, z256), z16 = cmul(z8, z8), z64 = cmul(z32, z32); z512 = cmul(z256, z256); \
        if (PEND) MM2(1, 0); \
        const f2 z4 = cmul(z2, z2), e2 = cmulc(c, z512), e3 = cmulc(e1, z512), z128 = cmul(z64, z64); \
        if (PEND) MM2(1, 1); \
        f1 = cvth(z1); f2_ = cvth(z2); f4_ = cvth(z4); g0 = cvth(c); g1 = cvth(e1); g2 = cvth(e2); g3 = cvth(e3); \
        if (PEND) MM2(1, 2); \
        e4 = cmulc(e2, z512); e5 = cmulc(e3, z512); \
        if (PEND) MM2(1, 3); \
        w8 = cvth(z8); w16 = cvth(z16); w24 = hmul(w8, w16); a32 = cvth(z32); a64 = cvth(z64); a96 = hmul(a32, a64); a128 = cvth(z128); } while (0)
#define KG_COLS_B() do { \
        const h2 f3 = hmul(f1, f2_), f6 = hmul(f2_, f4_), g04 = hmulc(g0, f4_); \
        PUT(0, g0); PUT(1, hmulc(g0, f1)); PUT(2, hmulc(g0, f2_)); PUT(3, hmulc(g0, f3)); \
        PUT(4, g04); PUT(5, hmulc(g04, f1)); PUT(6, hmulc(g0, f6)); PUT(7, hmulc(g04, f3)); \
        const f2 e6 = cmulc(e4, z512), e7 = cmulc(e5, z512); \
        PUT(8, g1); PUT(9, hmulc(g1, f2_)); PUT(10, hmulc(g1, f4_)); PUT(11, hmulc(g1, f6)); \
        const f2 e8 = cmulc(e6, z512); \
        PUT(12, g2); PUT(13, hmulc(g2, f4_)); PUT(14, g3); PUT(15, hmulc(g3, f4_)); \
        RD4(B0); \
        PUT(0, cvth(e4)); PUT(1, cvth(e5)); PUT(2, cvth(e6)); PUT(3, cvth(e7)); PUT(4, cvth(e8)); \
        RD4(B1); } while (0)
#define KG_QUAD(e, a) do { PUT(e, a); PUT((e) + 1, hmul(a, w8)); PUT((e) + 2, hmul(a, w16)); PUT((e) + 3, hmul(a, w24)); } while (0)
#define KG_ROWS_A() do { const h2 one = {1, 0}; PUT(0, one); PUT(1, w8); PUT(2, w16); PUT(3, w24); KG_QUAD(4, a32); KG_QUAD(8, a64); KG_QUAD(12, a96); RD4(A); } while (0)
#define KG_ROWS_B() do { const h2 a160 = hmul(a128, a32), a192 = hmul(a128, a64), a224 = hmul(a128, a96); \
        KG_QUAD(0, a128); MM2(0, 0); KG_QUAD(4, a160); MM2(0, 1); KG_QUAD(8, a192); MM2(0, 2); KG_QUAD(12, a224); MM2(0, 3); RD4(A); } while (0)
        KG_POW(0); KG_COLS_A(c0, 0); KG_COLS_B(); KG_ROWS_A();
        STAMP(3);
        SBAR();
        LN_PUBLISH();
        KG_ROWS_B();
        STAMPF(15); SBAR();
        { const u32 i0 = min(w4, 2u); SETM0(L_TAB + i0 * 2048); DMAO(lane * 16, (const char*)&CTAB + i0 * 2048, 0); DMAO(lane * 16, (const char*)&CTAB + i0 * 2048, 1024);
          dma16(lane * 16, (const char*)&CTAB + 6144 + i0 * 1024, L_SEL + i0 * 1024); }
        u4 red; UNDEF(red); GLD4(red, lane * 16, (const char*)&CTAB + 9216, "");
        W2_PIECE(0, pb0); SETM0(L_SCR + w4 * 4352);
        KG_POW(1);
        STAMPF(16); SBAR();
        KG_COLS_A(c1, 1);
        asm volatile("s_waitcnt vmcnt(0)" : "+v"(red) :: "memory");
        STAMPF(17); SBAR();
        if (tid == 256) { const u2 ar = {tag, tag}; GST2(0, ar, ws + WS_AR + b * 8, "sc1"); }
        W2_DRAIN(0, 0);
        W2_PIECE(1, pb0); SETM0(L_SCR + w4 * 4352);
        KG_COLS_B();
        STAMPF(18); SBAR();
        KG_ROWS_A();
        STAMPF(19); SBAR();
        UNDEF(arf); GLD2(arf, (tid & 255) * 8, ws + WS_AR, "sc1");
        KG_ROWS_B();
        MM2(1, 0); MM2(1, 1); MM2(1, 2); MM2(1, 3);
        { const u32 sw = L_SCR + w4 * 4352 + lane * 16;
          LDS(f4, sw) = self4(ch, dk[0], dk[2]) * isc; LDS(f4, sw + 1024) = self4(ch, dk[1], dk[3]) * isc; }
        asm volatile("s_waitcnt vmcnt(0)" : "+v"(arf) :: "memory");
#pragma unroll 1
        for (int sp = 0; __any(arf.x != tag) && sp < SPIN_MAX; ++sp) RLD2(arf, (tid & 255) * 8, ws + WS_AR);
        STAMP(12);
        SB(); BARRIER();
        UNDEF(stv); UNDEF(xg0); UNDEF(xg1);
        asm volatile("global_load_dword %0, %1, %2 sc1" : "+v"(stv) : "v"((tid & 127) * 4), "s"(ws + WS_STAT + rp * 512) : "memory");
        GLD4(xg0, xo, ws + WS_XG + dp * 32, "sc1"); GLD4(xg1, xo, ws + WS_XG + dp * 32 + 128 * 7168, "sc1");
        {
            const u32 a = L_SCR + (w4 ^ 1) * 4352 + lane * 16;
            const f4 s0 = self4(ch, dk[2], dk[0]) * isc + LDS(f4, a), s1 = self4(ch, dk[3], dk[1]) * isc + LDS(f4, a + 1024);
            const u32 o = red.x + ch * 16 * red.y + rg * 4352, p01 = pkbf(s0.x, s0.y), p23 = pkbf(s0.z, s0.w);
            LDS(u16, o) = (u16)p01; LDS(u16, o + red.y) = (u16)(p01 >> 16); LDS(u16, o + 2 * red.y) = (u16)p23; LDS(u16, o + 3 * red.y) = (u16)(p23 >> 16);
            LDS(u2, (ch ? red.w : red.z) + rg * 4352) = u2{pkbf(s1.x, s1.y), pkbf(s1.z, s1.w)};
            if (lane == 0) LDS(u4, L_KC + rg * 4352 + ch * 16) = u4{0, 0, 0, 0};
        }
        const u4 tixa = LDS(u4, L_TAB + (tid & 127) * 16), tixb = LDS(u4, L_TAB + 2048 + (tid & 127) * 16), tixc = LDS(u4, L_TAB + 4096 + (tid & 127) * 16);
        SEL_READ();
        asm volatile("s_waitcnt vmcnt(2)" : "+v"(stv) :: "memory");
        if (w4 < 2) LDS(u32, L_ST + (tid & 127) * 4) = stv;
        __builtin_amdgcn_s_setprio(0);
        W2_DRAIN(1, 0);
        W2_PIECE(2, pb0);
        STAMP(4);
        BARRIER();
        {
            u32 kc = L_KC + rg * 4352, td = L_TAB + rg * 5120 + (tid & 127) * 4; PIN(td);
            u32 v[10];
#pragma unroll
            for (int i = 0; i < 10; ++i) { const u32 tx = i < 4 ? tixa[i & 3] : i < 8 ? tixb[i & 3] : tixc[i & 3]; v[i] = (u32)LDS(u16, kc + (tx & 0xffff)) | ((u32)LDS(u16, kc + (tx >> 16)) << 16); }
#pragma unroll
            for (int i = 0; i < 4; ++i) LDS(u32, td + 512 * i) = v[i];
            if ((tid & 127) < 96) LDS(u32, td + 2048) = v[4];
            LDS(u32, td + 2432) = v[5]; LDS(u32, td + 2944) = v[6]; LDS(u32, td + 3456) = v[7]; LDS(u32, td + 3968) = v[8]; LDS(u32, td + 4480) = v[9];
        }
        {
            asm volatile("s_waitcnt vmcnt(8)" : "+v"(xg0), "+v"(xg1) :: "memory");
            u32 a = L_U + 480 + (tid & 1) * 4608 + ((tid & 255) >> 1) * 16; PIN(a);
            LDS(u4, a) = xg0; LDS(u4, a + 2048) = xg1;
            if ((tid & 255) < 60) LDS(u4, a - 480) = u4{0, 0, 0, 0};
        }
        STAMP(5);
        BARRIER();
        STAMP(6);
    }
    u4 hA0, hA1, hA2, hA3, hA4, hA5, hA6, hB0, hB1, hB2, hB3, hB4, hB5, hB6, hC0, hC1, hC2, hC3, hC4, hC5, hC6;
    const char* hrd = ws + WS_H + (P * 112 + wave * 14) * 2048;
#define H_LOAD_hA(n) do { const char* p_ = hrd + ((n) & 1) * 14336 + ((n) >> 1) * 1024; asm volatile( \
        "global_load_dwordx4 v[196:199], %7, %8 sc1\n\t" \
        "global_load_dwordx4 v[200:203], %7, %8 offset:2048 sc1\n\t" \
        "global_load_dwordx4 v[204:207], %7, %9 sc1\n\t" \
        "global_load_dwordx4 v[208:211], %7, %9 offset:2048 sc1\n\t" \
        "global_load_dwordx4 v[212:215], %7, %10 sc1\n\t" \
        "global_load_dwordx4 v[216:219], %7, %10 offset:2048 sc1\n\t" \
        "global_load_dwordx4 v[220:223], %7, %11 sc1\n\t" \
        "" : "={v[196:199]}"(hA0), "={v[200:203]}"(hA1), "={v[204:207]}"(hA2), "={v[208:211]}"(hA3), "={v[212:215]}"(hA4), "={v[216:219]}"(hA5), "={v[220:223]}"(hA6) : "v"(lane * 16), "s"(p_), "s"(p_ + 4096), "s"(p_ + 8192), "s"(p_ + 12288) : "memory"); } while (0)
#define H_TAKE_hA(n, NW) do { const char* p_ = hrd + ((n) & 1) * 14336 + ((n) >> 1) * 1024; u32 t0_, t1_, t2_, sp_; asm volatile( \
        "s_waitcnt vmcnt(" #NW ")\n\ts_mov_b32 %10, 0x10000\n" "1:\n\t" \
        "v_bitop3_b32 %8, v196, v200, %16 bitop3:0x7e\n\tv_bitop3_b32 %9, v204, v208, %16 bitop3:0x7e\n\t" \
        "v_or_b32 %7, %8, %9\n\t" \
        "v_bitop3_b32 %8, v212, v216, %16 bitop3:0x7e\n\tv_xor_b32 %9, %16, v220\n\tv_or3_b32 %7, %7, %8, %9\n\t" \
        "v_bitop3_b32 %8, v197, v201, %17 bitop3:0x7e\n\tv_bitop3_b32 %9, v205, v209, %17 bitop3:0x7e\n\t" \
        "v_or3_b32 %7, %7, %8, %9\n\t" \
        "v_bitop3_b32 %8, v213, v217, %17 bitop3:0x7e\n\tv_xor_b32 %9, %17, v221\n\tv_or3_b32 %7, %7, %8, %9\n\t" \
        "v_bitop3_b32 %8, v198, v202, %18 bitop3:0x7e\n\tv_bitop3_b32 %9, v206, v210, %18 bitop3:0x7e\n\t" \
        "v_or3_b32 %7, %7, %8, %9\n\t" \
        "v_bitop3_b32 %8, v214, v218, %18 bitop3:0x7e\n\tv_xor_b32 %9, %18, v222\n\tv_or3_b32 %7, %7, %8, %9\n\t" \
        "v_bitop3_b32 %8, v199, v203, %19 bitop3:0x7e\n\tv_bitop3_b32 %9, v207, v211, %19 bitop3:0x7e\n\t" \
        "v_or3_b32 %7, %7, %8, %9\n\t" \
        "v_bitop3_b32 %8, v215, v219, %19 bitop3:0x7e\n\tv_xor_b32 %9, %19, v223\n\tv_or3_b32 %7, %7, %8, %9\n\t" \
        "v_and_b32 %7, 0x80008000, %7\n\tv_cmp_ne_u32 vcc, 0, %7\n\ts_cbranch_vccz 2f\n\t" \
        "global_load_dwordx4 v[196:199], %11, %12 sc1\n\t" \
        "global_load_dwordx4 v[200:203], %11, %12 offset:2048 sc1\n\t" \
        "global_load_dwordx4 v[204:207], %11, %13 sc1\n\t" \
        "global_load_dwordx4 v[208:211], %11, %13 offset:2048 sc1\n\t" \
        "global_load_dwordx4 v[212:215], %11, %14 sc1\n\t" \
        "global_load_dwordx4 v[216:219], %11, %14 offset:2048 sc1\n\t" \
        "global_load_dwordx4 v[220:223], %11, %15 sc1\n\t" \
        "s_sub_u32 %10, %10, 1\n\ts_waitcnt vmcnt(0)\n\ts_cbranch_scc0 1b\n" "2:" \
        : "+{v[196:199]}"(hA0), "+{v[200:203]}"(hA1), "+{v[204:207]}"(hA2), "+{v[208:211]}"(hA3), "+{v[212:215]}"(hA4), "+{v[216:219]}"(hA5), "+{v[220:223]}"(hA6), "=&v"(t0_), "=&v"(t1_), "=&v"(t2_), "=&s"(sp_) \
        : "v"(lane * 16), "s"(p_), "s"(p_ + 4096), "s"(p_ + 8192), "s"(p_ + 12288), "s"(hm0), "s"(hm1), "s"(hm2), "s"(hm3) : "vcc", "scc", "memory"); } while (0)
#define H_LOAD_hB(n) do { const char* p_ = hrd + ((n) & 1) * 14336 + ((n) >> 1) * 1024; asm volatile( \
        "global_load_dwordx4 v[224:227], %7, %8 sc1\n\t" \
        "global_load_dwordx4 v[228:231], %7, %8 offset:2048 sc1\n\t" \
        "global_load_dwordx4 v[232:235], %7, %9 sc1\n\t" \
        "global_load_dwordx4 v[236:239], %7, %9 offset:2048 sc1\n\t" \
        "global_load_dwordx4 v[240:243], %7, %10 sc1\n\t" \
        "global_load_dwordx4 v[244:247], %7, %10 offset:2048 sc1\n\t" \
        "global_load_dwordx4 v[248:251], %7, %11 sc1\n\t" \
        "" : "={v[224:227]}"(hB0), "={v[228:231]}"(hB1), "={v[232:235]}"(hB2), "={v[236:239]}"(hB3), "={v[240:243]}"(hB4), "={v[244:247]}"(hB5), "={v[248:251]}"(hB6) : "v"(lane * 16), "s"(p_), "s"(p_ + 4096), "s"(p_ + 8192), "s"(p_ + 12288) : "memory"); } while (0)
#define H_TAKE_hB(n, NW) do { const char* p_ = hrd + ((n) & 1) * 14336 + ((n) >> 1) * 1024; u32 t0_, t1_, t2_, sp_; asm volatile( \
        "s_waitcnt vmcnt(" #NW ")\n\ts_mov_b32 %10, 0x10000\n" "1:\n\t" \
        "v_bitop3_b32 %8, v224, v228, %16 bitop3:0x7e\n\tv_bitop3_b32 %9, v232, v236, %16 bitop3:0x7e\n\t" \
        "v_or_b32 %7, %8, %9\n\t" \
        "v_bitop3_b32 %8, v240, v244, %16 bitop3:0x7e\n\tv_xor_b32 %9, %16, v248\n\tv_or3_b32 %7, %7, %8, %9\n\t" \
        "v_bitop3_b32 %8, v225, v229, %17 bitop3:0x7e\n\tv_bitop3_b32 %9, v233, v237, %17 bitop3:0x7e\n\t" \
        "v_or3_b32 %7, %7, %8, %9\n\t" \
        "v_bitop3_b32 %8, v241, v245, %17 bitop3:0x7e\n\tv_xor_b32 %9, %17, v249\n\tv_or3_b32 %7, %7, %8, %9\n\t" \
        "v_bitop3_b32 %8, v226, v230, %18 bitop3:0x7e\n\tv_bitop3_b32 %9, v234, v238, %18 bitop3:0x7e\n\t" \
        "v_or3_b32 %7, %7, %8, %9\n\t" \
        "v_bitop3_b32 %8, v242, v246, %18 bitop3:0x7e\n\tv_xor_b32 %9, %18, v250\n\tv_or3_b32 %7, %7, %8, %9\n\t" \
        "v_bitop3_b32 %8, v227, v231, %19 bitop3:0x7e\n\tv_bitop3_b32 %9, v235, v239, %19 bitop3:0x7e\n\t" \
        "v_or3_b32 %7, %7, %8, %9\n\t" \
        "v_bitop3_b32 %8, v243, v247, %19 bitop3:0x7e\n\tv_xor_b32 %9, %19, v251\n\tv_or3_b32 %7, %7, %8, %9\n\t" \
        "v_and_b32 %7, 0x80008000, %7\n\tv_cmp_ne_u32 vcc, 0, %7\n\ts_cbranch_vccz 2f\n\t" \
        "global_load_dwordx4 v[224:227], %11, %12 sc1\n\t" \
        "global_load_dwordx4 v[228:231], %11, %12 offset:2048 sc1\n\t" \
        "global_load_dwordx4 v[232:235], %11, %13 sc1\n\t" \
        "global_load_dwordx4 v[236:239], %11, %13 offset:2048 sc1\n\t" \
        "global_load_dwordx4 v[240:243], %11, %14 sc1\n\t" \
        "global_load_dwordx4 v[244:247], %11, %14 offset:2048 sc1\n\t" \
        "global_load_dwordx4 v[248:251], %11, %15 sc1\n\t" \
        "s_sub_u32 %10, %10, 1\n\ts_waitcnt vmcnt(0)\n\ts_cbranch_scc0 1b\n" "2:" \
        : "+{v[224:227]}"(hB0), "+{v[228:231]}"(hB1), "+{v[232:235]}"(hB2), "+{v[236:239]}"(hB3), "+{v[240:243]}"(hB4), "+{v[244:247]}"(hB5), "+{v[248:251]}"(hB6), "=&v"(t0_), "=&v"(t1_), "=&v"(t2_), "=&s"(sp_) \
        : "v"(lane * 16), "s"(p_), "s"(p_ + 4096), "s"(p_ + 8192), "s"(p_ + 12288), "s"(hm0), "s"(hm1), "s"(hm2), "s"(hm3) : "vcc", "scc", "memory"); } while (0)
#define H_LOAD_hC(n) do { const char* p_ = hrd + ((n) & 1) * 14336 + ((n) >> 1) * 1024; asm volatile( \
        "global_load_dwordx4 v[168:171], %7, %8 sc1\n\t" \
        "global_load_dwordx4 v[172:175], %7, %8 offset:2048 sc1\n\t" \
        "global_load_dwordx4 v[176:179], %7, %9 sc1\n\t" \
        "global_load_dwordx4 v[180:183], %7, %9 offset:2048 sc1\n\t" \
        "global_load_dwordx4 v[184:187], %7, %10 sc1\n\t" \
        "global_load_dwordx4 v[188:191], %7, %10 offset:2048 sc1\n\t" \
        "global_load_dwordx4 v[192:195], %7, %11 sc1\n\t" \
        "" : "={v[168:171]}"(hC0), "={v[172:175]}"(hC1), "={v[176:179]}"(hC2), "={v[180:183]}"(hC3), "={v[184:187]}"(hC4), "={v[188:191]}"(hC5), "={v[192:195]}"(hC6) : "v"(lane * 16), "s"(p_), "s"(p_ + 4096), "s"(p_ + 8192), "s"(p_ + 12288) : "memory"); } while (0)
#define H_TAKE_hC(n, NW) do { const char* p_ = hrd + ((n) & 1) * 14336 + ((n) >> 1) * 1024; u32 t0_, t1_, t2_, sp_; asm volatile( \
        "s_waitcnt vmcnt(" #NW ")\n\ts_mov_b32 %10, 0x10000\n" "1:\n\t" \
        "v_bitop3_b32 %8, v168, v172, %16 bitop3:0x7e\n\tv_bitop3_b32 %9, v176, v180, %16 bitop3:0x7e\n\t" \
        "v_or_b32 %7, %8, %9\n\t" \
        "v_bitop3_b32 %8, v184, v188, %16 bitop3:0x7e\n\tv_xor_b32 %9, %16, v192\n\tv_or3_b32 %7, %7, %8, %9\n\t" \
        "v_bitop3_b32 %8, v169, v173, %17 bitop3:0x7e\n\tv_bitop3_b32 %9, v177, v181, %17 bitop3:0x7e\n\t" \
        "v_or3_b32 %7, %7, %8, %9\n\t" \
        "v_bitop3_b32 %8, v185, v189, %17 bitop3:0x7e\n\tv_xor_b32 %9, %17, v193\n\tv_or3_b32 %7, %7, %8, %9\n\t" \
        "v_bitop3_b32 %8, v170, v174, %18 bitop3:0x7e\n\tv_bitop3_b32 %9, v178, v182, %18 bitop3:0x7e\n\t" \
        "v_or3_b32 %7, %7, %8, %9\n\t" \
        "v_bitop3_b32 %8, v186, v190, %18 bitop3:0x7e\n\tv_xor_b32 %9, %18, v194\n\tv_or3_b32 %7, %7, %8, %9\n\t" \
        "v_bitop3_b32 %8, v171, v175, %19 bitop3:0x7e\n\tv_bitop3_b32 %9, v179, v183, %19 bitop3:0x7e\n\t" \
        "v_or3_b32 %7, %7, %8, %9\n\t" \
        "v_bitop3_b32 %8, v187, v191, %19 bitop3:0x7e\n\tv_xor_b32 %9, %19, v195\n\tv_or3_b32 %7, %7, %8, %9\n\t" \
        "v_and_b32 %7, 0x80008000, %7\n\tv_cmp_ne_u32 vcc, 0, %7\n\ts_cbranch_vccz 2f\n\t" \
        "global_load_dwordx4 v[168:171], %11, %12 sc1\n\t" \
        "global_load_dwordx4 v[172:175], %11, %12 offset:2048 sc1\n\t" \
        "global_load_dwordx4 v[176:179], %11, %13 sc1\n\t" \
        "global_load_dwordx4 v[180:183], %11, %13 offset:2048 sc1\n\t" \
        "global_load_dwordx4 v[184:187], %11, %14 sc1\n\t" \
        "global_load_dwordx4 v[188:191], %11, %14 offset:2048 sc1\n\t" \
        "global_load_dwordx4 v[192:195], %11, %15 sc1\n\t" \
        "s_sub_u32 %10, %10, 1\n\ts_waitcnt vmcnt(0)\n\ts_cbranch_scc0 1b\n" "2:" \
        : "+{v[168:171]}"(hC0), "+{v[172:175]}"(hC1), "+{v[176:179]}"(hC2), "+{v[180:183]}"(hC3), "+{v[184:187]}"(hC4), "+{v[188:191]}"(hC5), "+{v[192:195]}"(hC6), "=&v"(t0_), "=&v"(t1_), "=&v"(t2_), "=&s"(sp_) \
        : "v"(lane * 16), "s"(p_), "s"(p_ + 4096), "s"(p_ + 8192), "s"(p_ + 12288), "s"(hm0), "s"(hm1), "s"(hm2), "s"(hm3) : "vcc", "scc", "memory"); } while (0)
#define H_LD1_0(p) asm volatile("global_load_dwordx4 v[196:199], %1, %2 offset:0 sc1" : "={v[196:199]}"(hA0) : "v"(lane * 16), "s"((p) + 0) : "memory")
#define H_LD1_1(p) asm volatile("global_load_dwordx4 v[200:203], %1, %2 offset:2048 sc1" : "={v[200:203]}"(hA1) : "v"(lane * 16), "s"((p) + 0) : "memory")
#define H_LD1_2(p) asm volatile("global_load_dwordx4 v[204:207], %1, %2 offset:0 sc1" : "={v[204:207]}"(hA2) : "v"(lane * 16), "s"((p) + 4096) : "memory")
#define H_LD1_3(p) asm volatile("global_load_dwordx4 v[208:211], %1, %2 offset:2048 sc1" : "={v[208:211]}"(hA3) : "v"(lane * 16), "s"((p) + 4096) : "memory")
#define H_LD1_4(p) asm volatile("global_load_dwordx4 v[212:215], %1, %2 offset:0 sc1" : "={v[212:215]}"(hA4) : "v"(lane * 16), "s"((p) + 8192) : "memory")
#define H_LD1_5(p) asm volatile("global_load_dwordx4 v[216:219], %1, %2 offset:2048 sc1" : "={v[216:219]}"(hA5) : "v"(lane * 16), "s"((p) + 8192) : "memory")
#define H_LD1_6(p) asm volatile("global_load_dwordx4 v[220:223], %1, %2 offset:0 sc1" : "={v[220:223]}"(hA6) : "v"(lane * 16), "s"((p) + 12288) : "memory")
    {
        const int kh = hi4, ts = ch;
        STAMP(13);
        CONV(isF ? 0 : 3, isF ? 2 : 4, isF, w2b + 16 * 7168 - 3072, pb0);
        asm volatile("; AUDIT_VM 4");
        if (isF) { if (ch) conv_far<1>(cacc, cv_ub, cv_p3, sel3); else conv_far<0>(cacc, cv_ub, cv_p3, sel3); }
        STAMP(14);
        H_LOAD_hA(0);
        CONV(isF ? 2 : 7, 1, true, w2b + 24 * 7168 - 3072, pb1);
        H_LOAD_hB(1); H_LOAD_hC(2);
        STAMP(7);
        BARRIER();
        u32 ra = lw + (isF ? 0 : 65536) + 16384 + lane * 16; PIN(ra);
        LDS(f4, ra) = self4(kh, cacc[0], cacc[2]);
        LDS(f4, ra + 1024) = self4(kh, cacc[1], cacc[3]);
        dma16(r8 * 896 + 16 * (p8 ^ (wave & 6)), (const char*)x + (P * 64 + wave * 8) * 896 + ct * 128, wave * 1024);
        ra = pb1 + 16384 + lane * 16; PIN(ra);
        BARRIER();
        const float dk = rg ? bfhi(dsk2) : bflo(dsk2);
#pragma unroll
        for (int i = 0; i < 2; ++i) {
            const int tau = kh ? (ts ? 5 + i : 4 + 3 * i) : (ts ? 1 + i : 3 * i);
            const f4 y = self4(kh, cacc[2 + i], cacc[i]) + LDS(f4, ra + i * 1024);
            const u2 uu = LDS(u2, L_U + rg * 4608 + 480 + (256 * tau + 16 * fl + 4 * g) * 2);
            const u32 y01 = pkbf(y.x, y.y), y23 = pkbf(y.z, y.w);
            const u32 s01 = pkbf(bflo(uu.x) * dk, bfhi(uu.x) * dk), s23 = pkbf(bflo(uu.y) * dk, bfhi(uu.y) * dk);
            const u4 o = {pkbf(bflo(y01) + bflo(s01), bfhi(y01) + bfhi(s01)), tag, pkbf(bflo(y23) + bflo(s23), bfhi(y23) + bfhi(s23)), tag};
            GST4((fl >> 2) * 114688 + ((fl & 3) + 4 * g) * 16, o, ws + WS_YG + (4 * tau * 448 + 2 * dp + rg) * 256, "sc1");
        }
    }
    STAMP(8);
    WAITV(28);
    W2_RD(8, 0, 0); W2_RD(9, 0, 1); W2_RD(10, 0, 2); W2_RD(11, 0, 3);
    u32 pw0 = pb0 + lane * 16, pw1 = (pb1 + lane * 16) ^ 128; PIN(pw0); PIN(pw1);
#define H_MM1(h, j, o) do { a0 = mfma32(h##j, Bw[(o) + j][0], a0); a1 = mfma32(h##j, Bw[(o) + j][1], a1); } while (0)
#define H_MMA(h, o) do { H_MM1(h, 0, o); H_MM1(h, 1, o); H_MM1(h, 2, o); H_MM1(h, 3, o); H_MM1(h, 4, o); H_MM1(h, 5, o); H_MM1(h, 6, o); } while (0)
#define W2_SGN(j) do { Bw[j][0] ^= u4{hm0, hm1, hm2, hm3}; Bw[j][1] ^= u4{hm0, hm1, hm2, hm3}; PIN(Bw[j][0]); PIN(Bw[j][1]); } while (0)
#define H_MMS(h, o) do { H_MM1(h, 0, o); W2_SGN((o) + 1); SB(); H_MM1(h, 1, o); W2_SGN((o) + 2); SB(); H_MM1(h, 2, o); W2_SGN((o) + 3); SB(); H_MM1(h, 3, o); W2_SGN((o) + 4); SB(); \
        H_MM1(h, 4, o); W2_SGN((o) + 5); SB(); H_MM1(h, 5, o); W2_SGN((o) + 6); SB(); H_MM1(h, 6, o); } while (0)
#define H_ZERO() _Pragma("unroll") for (int i = 0; i < 16; ++i) { a0[i] = 0; a1[i] = 0; }
#define H_PUT(pw) _Pragma("unroll") for (int q = 0; q < 4; ++q) { const u32 a_ = ((pw) ^ (q >> 1) * 64) + q * 1024; LDS(f4, a_) = quad(a0, q); LDS(f4, a_ + 16384) = quad(a1, q); }
    f16v a0, a1;
    H_ZERO();
    W2_SGN(0); W2_SGN(7);
    H_TAKE_hA(0, 21);
    {
        const char* p3 = hrd + 14336 + 1024;
        H_MM1(hA, 0, 0); W2_SGN(1); SB(); H_LD1_0(p3); H_MM1(hA, 1, 0); W2_SGN(2); SB(); H_LD1_1(p3); H_MM1(hA, 2, 0); W2_SGN(3); SB(); H_LD1_2(p3);
        H_MM1(hA, 3, 0); W2_SGN(4); SB(); H_LD1_3(p3); H_MM1(hA, 4, 0); W2_SGN(5); SB(); H_LD1_4(p3); H_MM1(hA, 5, 0); W2_SGN(6); SB(); H_LD1_5(p3);
        H_MM1(hA, 6, 0); SB(); H_LD1_6(p3);
    }
    STAMP(9);
    H_TAKE_hB(1, 17); W2_RD(12, 1, 0); W2_RD(13, 1, 1); H_MMS(hB, 7); H_PUT(pw0);
    H_ZERO();
    H_TAKE_hC(2, 10); H_MMA(hC, 0);
    const u32 al = lane & 3, dc = 16 * rg + (lane >> 2);
    const u32 yo = (32 * hi4 + dc) * 256 + 16 * al + 128 * ch;
    const char* yb = ws + WS_YG + (P * 448 + 64 * ct) * 256;
    u4 yv0, yv1; UNDEF(yv0); UNDEF(yv1);
    GLD4(yv0, yo, yb, "sc1"); GLD4(yv1, yo, yb, "offset:64 sc1");
    H_TAKE_hA(3, 2); H_MMA(hA, 7); H_PUT(pw1);
    BARRIER();
    STAMP(10);
    f4 r2[2];
    float xe[8];
    u32 ax = (16 * al + 8 * ch) * 128 + ((64 * hi4 + 2 * dc) ^ (32 * al)); PIN(ax);
    {
        const u32 o = L_RING + hi4 * 16384 + (2 * (al & 1) + ch) * 1024 + (dc ^ (4 * al)) * 16;
        u32 aF = o + (al & 2 ? 65536 : 32768), aS = o + (al & 2 ? 0 : 98304);
        PIN(aF); PIN(aS);
#pragma unroll
        for (int i = 0; i < 2; ++i)
            r2[i] = ((LDS(f4, aF + i * 512) + LDS(f4, aF + i * 512 + 4096)) + (LDS(f4, aF + i * 512 + 8192) + LDS(f4, aF + i * 512 + 12288))) +
                    ((LDS(f4, aS + i * 512) + LDS(f4, aS + i * 512 + 4096)) + (LDS(f4, aS + i * 512 + 8192) + LDS(f4, aS + i * 512 + 12288)));
#pragma unroll
        for (int i = 0; i < 8; ++i) xe[i] = bflo(LDS(u16, ax + i * 128));
    }
    asm volatile("s_waitcnt vmcnt(0)" : "+v"(yv0), "+v"(yv1) :: "memory");
#pragma unroll 1
    for (int sp = 0; __builtin_expect(__any(((yv0.y ^ tag) | (yv0.w ^ tag) | (yv1.y ^ tag) | (yv1.w ^ tag)) != 0), 0) && sp < SPIN_MAX; ++sp)
        asm volatile("global_load_dwordx4 %0, %2, %3 sc1\n\tglobal_load_dwordx4 %1, %2, %3 offset:64 sc1\n\ts_waitcnt vmcnt(0)" : "=&v"(yv0), "=&v"(yv1) : "v"(yo), "s"(yb) : "memory");
#pragma unroll
    for (int i = 0; i < 2; ++i) {
        const u32 y01 = i ? yv1.x : yv0.x, y23 = i ? yv1.z : yv0.z;
        const u32 a01 = pkbf(xe[4 * i] + bflo(y01), xe[4 * i + 1] + bfhi(y01)), a23 = pkbf(xe[4 * i + 2] + bflo(y23), xe[4 * i + 3] + bfhi(y23));
        const u32 f01 = pkbf(r2[i].x, r2[i].y), f23 = pkbf(r2[i].z, r2[i].w);
        const u32 o01 = pkbf(bflo(a01) + bflo(f01), bfhi(a01) + bfhi(f01)), o23 = pkbf(bflo(a23) + bflo(f23), bfhi(a23) + bfhi(f23));
        LDS(u16, ax + i * 512) = (u16)o01; LDS(u16, ax + i * 512 + 128) = (u16)(o01 >> 16); LDS(u16, ax + i * 512 + 256) = (u16)o23; LDS(u16, ax + i * 512 + 384) = (u16)(o23 >> 16);
    }
    BARRIER();
    *(u4*)((char*)out + (P * 64 + wave * 8 + r8) * 896 + ct * 128 + 16 * (p8 ^ (wave & 6))) = LDS(u4, tid * 16);
    STAMP(11); STAMPC(23);
    STAMP_FLUSH(24);
}

extern "C" void kernel_launch(void* const* d_in, const int* in_sizes, int n_in, void* d_out, int out_size, void* d_ws, size_t ws_size,
                              hipStream_t stream) {
    (void)hipFuncSetAttribute((const void*)s4ffn_kernel, hipFuncAttributeMaxDynamicSharedMemorySize, LDS_BYTES);
    s4ffn_kernel<<<256, 512, LDS_BYTES, stream>>>((const u16*)d_in[0], (const u16*)d_in[1], (const u16*)d_in[2], (const u16*)d_in[3],
        (const u16*)d_in[4], (const u16*)d_in[5], (const u16*)d_in[6], (const u16*)d_in[7], (const u16*)d_in[8],
        (const u16*)d_in[10], (const u16*)d_in[11], (u16*)d_out, (char*)d_ws);
}
```

```cpp
#include <hip/hip_runtime.h>
#include <stdint.h>

typedef unsigned short u16;
typedef unsigned int u32;
typedef unsigned long long u64;
typedef __attribute__((ext_vector_type(8))) __bf16 bf8;
typedef __attribute__((ext_vector_type(2))) __bf16 bf2;
typedef __attribute__((ext_vector_type(4))) short s4;
typedef __attribute__((ext_vector_type(4))) float f4;
typedef __attribute__((ext_vector_type(16))) float f16v;
typedef __attribute__((ext_vector_type(2))) float f2;
typedef __attribute__((ext_vector_type(4))) u32 u4;
typedef __attribute__((ext_vector_type(2))) u32 u2;
#define LAS __attribute__((address_space(3)))
#define DI __device__ __forceinline__
#define LDS(T, off) (*(LAS T*)(uintptr_t)(u32)(off))

constexpr u32 WS_EPOCH = 0;
constexpr u32 WS_STAT = 4096;
constexpr u32 WS_AR = WS_STAT + 16384;
constexpr u32 WS_XG = 65536;
constexpr u32 WS_YG = WS_XG + 3670016;
constexpr u32 WS_H = WS_YG + 3670016;
constexpr u32 WS_STAMP = 32u << 20;
constexpr u32 L_TAB = 0;
constexpr u32 L_CS = 10240;
constexpr u32 L_ST = 10752;
constexpr u32 L_SEL = 12288;
constexpr u32 L_SCR = 15360;
constexpr u32 L_KC = L_SCR + 2048;
constexpr u32 L_XE = L_SCR;
constexpr u32 L_U = 23552;
constexpr u32 L_RING = 32768;
constexpr u32 L_XT = L_RING + 98304;
constexpr u32 LDS_BYTES = 163840;
constexpr int SPIN_MAX = 1 << 16;
constexpr u32 PF_LINES = 188;

#define STAMP(i)
#define STAMPC(i)
#define STAMPF(i)
#define FSTAMP(s)
#define ACC_BEGIN()
#define ACC_END(i)
#define STAMP_DECL
#define STAMP_FLUSH(n)

DI float bflo(u32 v) { return __uint_as_float(v << 16); }
DI float bfhi(u32 v) { return __uint_as_float(v & 0xffff0000u); }
DI u32 pkbf(float a, float b) { f2 v = {a, b}; return __builtin_bit_cast(u32, __builtin_convertvector(v, bf2)); }
DI float rbf(float a) { return bflo(pkbf(a, a)); }
DI f4 mfma(u4 a, u4 b, f4 c) { return __builtin_amdgcn_mfma_f32_16x16x32_bf16(__builtin_bit_cast(bf8, a), __builtin_bit_cast(bf8, b), c, 0, 0, 0); }
DI f16v mfma32(u4 a, u4 b, f16v c) { return __builtin_amdgcn_mfma_f32_32x32x16_bf16(__builtin_bit_cast(bf8, a), __builtin_bit_cast(bf8, b), c, 0, 0, 0); }
DI u2 trread(u32 addr) { return __builtin_bit_cast(u2, __builtin_amdgcn_ds_read_tr16_b64_v4i16((LAS s4*)(uintptr_t)addr)); }
DI u4 trread2(u32 a, u32 b) { const u2 l = trread(a), h = trread(b); return u4{l.x, l.y, h.x, h.y}; }
template <int C> DI float dpp(float x) { return __builtin_bit_cast(float, __builtin_amdgcn_mov_dpp(__builtin_bit_cast(int, x), C, 0xf, 0xf, true)); }
DI float rsum(float x) { x += dpp<0xB1>(x); x += dpp<0x4E>(x); x += dpp<0x141>(x); x += dpp<0x140>(x); return x; }
DI float rmax(float x) { x = fmaxf(x, dpp<0xB1>(x)); x = fmaxf(x, dpp<0x4E>(x)); x = fmaxf(x, dpp<0x141>(x)); return fmaxf(x, dpp<0x140>(x)); }
DI float sw32(float a, float b) { auto s = __builtin_amdgcn_permlane32_swap(__float_as_uint(a), __float_as_uint(b), false, false); return __uint_as_float(s[0]) + __uint_as_float(s[1]); }
DI float sw16(float a, float b) { auto s = __builtin_amdgcn_permlane16_swap(__float_as_uint(a), __float_as_uint(b), false, false); return __uint_as_float(s[0]) + __uint_as_float(s[1]); }
DI float dot2(u32 a, u32 b, float c) { return __builtin_amdgcn_fdot2_f32_bf16(__builtin_bit_cast(bf2, a), __builtin_bit_cast(bf2, b), c, false); }
DI f4 self4(int c, f4 a, f4 b) { return c ? a : b; }
DI f2 cmul(f2 a, f2 b) { float x, y;
    asm("v_mul_f32 %0, %2, %4\n\tv_mul_f32 %1, %3, %4\n\tv_fma_f32 %0, -%3, %5, %0\n\tv_fmac_f32 %1, %2, %5" : "=&v"(x), "=&v"(y) : "v"(a.x), "v"(a.y), "v"(b.x), "v"(b.y));
    return f2{x, y}; }
DI f2 cmulc(f2 a, f2 b) { float x, y;
    asm("v_mul_f32 %0, %2, %4\n\tv_mul_f32 %1, %3, %4\n\tv_fmac_f32 %0, %3, %5\n\tv_fma_f32 %1, -%2, %5, %1" : "=&v"(x), "=&v"(y) : "v"(a.x), "v"(a.y), "v"(b.x), "v"(b.y));
    return f2{x, y}; }
DI float fmul1(float a, float b) { float r; asm("v_mul_f32 %0, %1, %2" : "=v"(r) : "v"(a), "v"(b)); return r; }
typedef __attribute__((ext_vector_type(2))) _Float16 h2;
typedef __attribute__((ext_vector_type(8))) _Float16 h8;
DI h2 cvth(f2 a) { return __builtin_convertvector(a, h2); }
DI h2 hmul(h2 a, h2 b) { const h2 bx = {b.x, b.x}, by = {-b.y, b.y}; return __builtin_elementwise_fma(__builtin_shufflevector(a, a, 1, 0), by, a * bx); }
DI h2 hmulc(h2 a, h2 b) { const h2 bx = {b.x, b.x}, by = {b.y, -b.y}; return __builtin_elementwise_fma(__builtin_shufflevector(a, a, 1, 0), by, a * bx); }
DI f4 mfmah(u4 a, u4 b, f4 c) { return __builtin_amdgcn_mfma_f32_16x16x32_f16(__builtin_bit_cast(h8, a), __builtin_bit_cast(h8, b), c, 0, 0, 0); }
DI u32 relu2(u32 v) { typedef __attribute__((ext_vector_type(2))) short s2; const s2 z = {0, 0}; return __builtin_bit_cast(u32, __builtin_elementwise_max(__builtin_bit_cast(s2, v), z)); }
DI f2 zpow(float a2, float rev, float p) {
    float e = __builtin_amdgcn_exp2f(a2 * p), r = __builtin_amdgcn_fractf(rev * p);
    float x, y;
    asm("s_nop 0\n\tv_mul_f32 %0, %2, %3\n\tv_mul_f32 %1, %2, %4" : "=&v"(x), "=&v"(y) : "v"(e), "v"(__builtin_amdgcn_cosf(r)), "v"(__builtin_amdgcn_sinf(r)));
    return f2{x, y};
}
DI f4 quad(const f16v& v, int q) { return f4{v[4 * q], v[4 * q + 1], v[4 * q + 2], v[4 * q + 3]}; }

#define SB() __builtin_amdgcn_sched_barrier(0)
#define BARRIER() asm volatile("s_waitcnt lgkmcnt(0)\n\ts_barrier" ::: "memory")
#define WAITV(n) asm volatile("s_waitcnt vmcnt(" #n ")" ::: "memory")
DI void dma16(u32 voff, const void* base, u32 lds) {
    asm volatile("s_mov_b32 m0, %2\n\ts_nop 0\n\tglobal_load_lds_dwordx4 %0, %1" :: "v"(voff), "s"(base), "s"(lds) : "memory");
}
#define SETM0(lds) asm volatile("s_mov_b32 m0, %0\n\ts_nop 0" :: "s"(lds) : "memory")
#define DMAO(voff, base, off) asm volatile("global_load_lds_dwordx4 %0, %1 offset:" #off :: "v"(voff), "s"(base) : "memory")
#define GLD4(dst, voff, base, mod) asm volatile("global_load_dwordx4 %0, %1, %2 " mod : "+v"(dst) : "v"(voff), "s"(base) : "memory")
#define GLD2(dst, voff, base, mod) asm volatile("global_load_dwordx2 %0, %1, %2 " mod : "+v"(dst) : "v"(voff), "s"(base) : "memory")
#define GLD1(dst, voff, base) asm volatile("global_load_dword %0, %1, %2" : "+v"(dst) : "v"(voff), "s"(base) : "memory")
#define GST4(voff, val, base, mod) asm volatile("global_store_dwordx4 %0, %1, %2 " mod "\n\ts_nop 1" :: "v"(voff), "v"(val), "s"(base) : "memory")
#define GST2(voff, val, base, mod) asm volatile("global_store_dwordx2 %0, %1, %2 " mod :: "v"(voff), "v"(val), "s"(base) : "memory")
#define RLD2(dst, voff, base) asm volatile("global_load_dwordx2 %0, %1, %2 sc1\n\ts_waitcnt vmcnt(0)" : "=&v"(dst) : "v"(voff), "s"(base) : "memory")
#define RLD4(dst, voff, base) asm volatile("global_load_dwordx4 %0, %1, %2 sc1\n\ts_waitcnt vmcnt(0)" : "=&v"(dst) : "v"(voff), "s"(base) : "memory")
#define PREFETCH(voff, base) asm volatile("global_load_dword v255, %0, %1" :: "v"(voff), "s"(base) : "v255", "memory")
#define PIN(x) asm volatile("" : "+v"(x))
#define UNDEF(x) asm volatile("" : "=v"(x))

struct ConstTab { u32 tix[3][128][4]; u32 sel[3][64][4]; u32 red[64][4]; };
constexpr int cidx(int l) {
    if (l < 0) return 16 + l;
    const int s = (l >= 256) + (l >= 512) + (l >= 1024);
    return 16 + 128 * s + (s ? (l + (1 << (s - 1)) - 1 + ((l >> s) & 1)) >> s : l);
}
constexpr ConstTab make_tab() {
    ConstTab c{};
    for (int t = 0; t < 128; ++t) {
        u32 v[12] = {};
        for (int i = 0; i < 5; ++i) {
            const int u = t + 128 * i < 607 ? t + 128 * i : 607, pi = u >= 304, ip = u - 304 * pi, top = (ip < 24 ? 1056 : 592) - pi - 2 * ip;
            v[i] = (u32)cidx(top > 1028 ? -1 : top) * 2 | (u32)cidx(top - 1 > 1028 ? -1 : top - 1) << 17;
        }
        v[5] = (404 + (t >> 1) + 2 * (t & 1)) * 0x20002u + 0x20000u;
        v[6] = v[5] + 64 * 0x20002u;
        for (int i = 0; i < 3; ++i) {
            const int u = t + 128 * i < 303 ? t + 128 * i : 303, q = u & 3, m = 2 * (52 + (u >> 2)) + (q < 2 ? 2 - 4 * q : 9 - 4 * q);
            v[7 + i] = (u32)cidx(m >= 129 ? 8 * m - (m == 256) : -1) * 2 | (u32)cidx(m - 2 >= 129 ? 8 * (m - 2) : -1) << 17;
        }
        for (int i = 0; i < 12; ++i) c.tix[i >> 2][t][i & 3] = v[i];
    }
    for (int l = 0; l < 64; ++l) {
        const int fl = l & 15, g = l >> 4, x2 = fl & 7;
        const int m2 = x2 < 2 ? -2 : x2 < 5 ? -1 : 0;
        for (int q = 0; q < 4; ++q)
            for (int h = 0; h < 2; ++h) {
                const int e2 = x2 - 2 * q - h, e = 8 * g + 2 * q + h - 8 - fl;
                c.sel[0][l][q] |= (u32)(((e2 >= -5) + (e2 >= -2) + (e2 >= 3) + (e2 >= 6) - 2 - m2) * 0x0202 + 0x0100) << (16 * h);
                c.sel[1][l][q] |= (u32)(e >= -3 && e <= 3 ? 0x3f80 : e >= -4 && e <= 4 ? 0x3f81 : 0) << (16 * h);
            }
        const int mul = fl < 8 ? 8 : fl < 12 ? 4 : 2, base = fl < 8 ? fl : fl < 12 ? 248 + fl : 384 + (fl & 1) + 64 * ((fl - 12) >> 1);
        c.red[l][0] = L_KC + (16 + base + mul * 4 * g) * 2; c.red[l][1] = 2 * mul;
        c.red[l][2] = L_KC + (fl < 4 ? (528 + 32 * fl + 4 * g) * 2 : fl == 4 && g == 0 ? 1312 : 1528);
        c.red[l][3] = L_KC + (fl < 4 ? (544 + 32 * fl + 4 * g) * 2 : 1528);
        c.sel[2][l][0] = L_U + 2 * (224 + 16 * fl + 8 * g);
        c.sel[2][l][1] = L_TAB + 4 * ((fl & 1) * 304 + ((16 - fl + 8 * g) >> 1));
        c.sel[2][l][2] = L_TAB + 2432 + 8 * (2 * (2 + (fl >> 3) - g) + m2 - 4);
        c.sel[2][l][3] = L_TAB + 3456 + 16 * (12 + fl - 4 * g);
    }
    return c;
}
static __device__ const ConstTab CTAB __attribute__((aligned(128))) = make_tab();

#define H_LDA(j, p) do { \
        if ((j) == 0) asm volatile("global_load_dwordx4 v[196:199], %0, %1 offset:0 sc1" :: "v"(l16), "s"((p) + 0) : "v196", "v197", "v198", "v199", "memory"); \
        if ((j) == 1) asm volatile("global_load_dwordx4 v[200:203], %0, %1 offset:2048 sc1" :: "v"(l16), "s"((p) + 0) : "v200", "v201", "v202", "v203", "memory"); \
        if ((j) == 2) asm volatile("global_load_dwordx4 v[204:207], %0, %1 offset:0 sc1" :: "v"(l16), "s"((p) + 4096) : "v204", "v205", "v206", "v207", "memory"); \
        if ((j) == 3) asm volatile("global_load_dwordx4 v[208:211], %0, %1 offset:2048 sc1" :: "v"(l16), "s"((p) + 4096) : "v208", "v209", "v210", "v211", "memory"); \
        if ((j) == 4) asm volatile("global_load_dwordx4 v[212:215], %0, %1 offset:0 sc1" :: "v"(l16), "s"((p) + 8192) : "v212", "v213", "v214", "v215", "memory"); \
        if ((j) == 5) asm volatile("global_load_dwordx4 v[216:219], %0, %1 offset:2048 sc1" :: "v"(l16), "s"((p) + 8192) : "v216", "v217", "v218", "v219", "memory"); \
        if ((j) == 6) asm volatile("global_load_dwordx4 v[220:223], %0, %1 offset:0 sc1" :: "v"(l16), "s"((p) + 12288) : "v220", "v221", "v222", "v223", "memory"); \
        } while (0)
#define H_LDB(j, p) do { \
        if ((j) == 0) asm volatile("global_load_dwordx4 v[224:227], %0, %1 offset:0 sc1" :: "v"(l16), "s"((p) + 0) : "v224", "v225", "v226", "v227", "memory"); \
        if ((j) == 1) asm volatile("global_load_dwordx4 v[228:231], %0, %1 offset:2048 sc1" :: "v"(l16), "s"((p) + 0) : "v228", "v229", "v230", "v231", "memory"); \
        if ((j) == 2) asm volatile("global_load_dwordx4 v[232:235], %0, %1 offset:0 sc1" :: "v"(l16), "s"((p) + 4096) : "v232", "v233", "v234", "v235", "memory"); \
        if ((j) == 3) asm volatile("global_load_dwordx4 v[236:239], %0, %1 offset:2048 sc1" :: "v"(l16), "s"((p) + 4096) : "v236", "v237", "v238", "v239", "memory"); \
        if ((j) == 4) asm volatile("global_load_dwordx4 v[240:243], %0, %1 offset:0 sc1" :: "v"(l16), "s"((p) + 8192) : "v240", "v241", "v242", "v243", "memory"); \
        if ((j) == 5) asm volatile("global_load_dwordx4 v[244:247], %0, %1 offset:2048 sc1" :: "v"(l16), "s"((p) + 8192) : "v244", "v245", "v246", "v247", "memory"); \
        if ((j) == 6) asm volatile("global_load_dwordx4 v[248:251], %0, %1 offset:0 sc1" :: "v"(l16), "s"((p) + 12288) : "v248", "v249", "v250", "v251", "memory"); \
        } while (0)
#define H_LDC(j, p) do { \
        if ((j) == 0) asm volatile("global_load_dwordx4 v[168:171], %0, %1 offset:0 sc1" :: "v"(l16), "s"((p) + 0) : "v168", "v169", "v170", "v171", "memory"); \
        if ((j) == 1) asm volatile("global_load_dwordx4 v[172:175], %0, %1 offset:2048 sc1" :: "v"(l16), "s"((p) + 0) : "v172", "v173", "v174", "v175", "memory"); \
        if ((j) == 2) asm volatile("global_load_dwordx4 v[176:179], %0, %1 offset:0 sc1" :: "v"(l16), "s"((p) + 4096) : "v176", "v177", "v178", "v179", "memory"); \
        if ((j) == 3) asm volatile("global_load_dwordx4 v[180:183], %0, %1 offset:2048 sc1" :: "v"(l16), "s"((p) + 4096) : "v180", "v181", "v182", "v183", "memory"); \
        if ((j) == 4) asm volatile("global_load_dwordx4 v[184:187], %0, %1 offset:0 sc1" :: "v"(l16), "s"((p) + 8192) : "v184", "v185", "v186", "v187", "memory"); \
        if ((j) == 5) asm volatile("global_load_dwordx4 v[188:191], %0, %1 offset:2048 sc1" :: "v"(l16), "s"((p) + 8192) : "v188", "v189", "v190", "v191", "memory"); \
        if ((j) == 6) asm volatile("global_load_dwordx4 v[192:195], %0, %1 offset:0 sc1" :: "v"(l16), "s"((p) + 12288) : "v192", "v193", "v194", "v195", "memory"); \
        } while (0)
struct ConvOps { u4 B[8], A0, A1; u2 v2, v3; };
template <int TS, int KK> DI void conv_read(ConvOps& o, int i, u32 u, u32 l, u32 q) {
    constexpr int ORD[2][8] = {{0, 3, 4, 7, 2, 6, 1, 5}, {1, 2, 5, 6, 0, 4, 3, 7}};
    auto two = [&](u32 a) { return u2{LDS(u32, a), LDS(u32, a + 4)}; };
    if (i == 0) { const u2 t = two(l + 704 - 64 * KK); o.A0.x = t.x; o.A0.y = t.y; }
    if (i == 1) { const u2 t = two(l + 712 - 64 * KK); o.A0.z = t.x; o.A0.w = t.y; }
    if (i >= 2 && i < 6) o.B[ORD[TS][i - 2]] = LDS(u4, u + 192 - 64 * KK + 512 * ORD[TS][i - 2]);
    if (i == 6) { const u2 t = two(l + 192 - 64 * KK); o.A1.x = t.x; o.A1.y = t.y; }
    if (i == 7) { const u2 t = two(l + 200 - 64 * KK); o.A1.z = t.x; o.A1.w = t.y; }
    if (i >= 8 && i < 12 - TS) o.B[ORD[TS][i - 4]] = LDS(u4, u + 192 - 64 * KK + 512 * ORD[TS][i - 4]);
    if (i == 12) { o.v2 = LDS(u2, q + 64 * KK); o.v3 = LDS(u2, q + 64 * KK + 512); }
}
template <int TS, int KK>
DI void conv_step(f4 (&acc)[4], const ConvOps& c, ConvOps& n, const u4& a2s, bool use2s, u32 u, u32 l, u32 q, const u4& sel2,
                  u32 vq0, u32 vq1, u32 vq2, u32 vq3, const char* src, u32 l16, const char* hrd) {
    constexpr int T[2][4] = {{0, 3, 4, 7}, {1, 2, 5, 6}};
    auto seg2 = [&](u2 v) { u4 r;
        r.x = __builtin_amdgcn_perm(v.y, v.x, sel2.x); r.y = __builtin_amdgcn_perm(v.y, v.x, sel2.y);
        r.z = __builtin_amdgcn_perm(v.y, v.x, sel2.z); r.w = __builtin_amdgcn_perm(v.y, v.x, sel2.w); return r; };
    u4 A2, A3;
    int m = 0;
#pragma unroll
    for (int d = 0; d < 4; ++d)
#pragma unroll
        for (int i = 0; i < 4; ++i)
            if (T[TS][i] >= d) {
                acc[i] = mfma(d == 0 ? c.A0 : d == 1 ? c.A1 : d == 2 ? A2 : A3, c.B[T[TS][i] - d], acc[i]);
                if (KK < 3) conv_read<TS, KK + 1>(n, m, u, l, q);
                if (m == 1) { A2 = seg2(c.v2); if (KK == 0 && use2s) A2 = a2s; PIN(A2); }
                if (m == 3) { A3 = seg2(c.v3); PIN(A3); }
                if (KK < 2) { if (m == 2) DMAO(vq0, src, 0); if (m == 5) DMAO(vq1, src, 1024); if (m == 8) DMAO(vq2, src, 2048); if (m == 11) DMAO(vq3, src, 3072); }
                if (KK == 3 && !(m & 1)) H_LDA(m >> 1, hrd);
                SB();
                ++m;
            }
}
template <int TS>
DI void conv_steps(f4 (&acc)[4], bool first, u32 u, u32 l, u32 q, u32 la, const u4& sel2, u32 vq0, u32 vq1, u32 vq2, u32 vq3, const char* src, u32 dst, u32 l16, const char* hrd) {
    ConvOps a, b;
#pragma unroll
    for (int i = 0; i < 13; ++i) conv_read<TS, 0>(a, i, u, l, q);
    const u4 a2s = {LDS(u32, la + 96), LDS(u32, la + 100), LDS(u32, la + 104), LDS(u32, la + 108)};
    SETM0(dst); __builtin_amdgcn_s_setprio(3); SB();
    conv_step<TS, 0>(acc, a, b, a2s, first, u, l, q, sel2, vq0, vq1, vq2, vq3, src, l16, hrd);
    SETM0(dst + 16384); __builtin_amdgcn_s_setprio(2); SB();
    conv_step<TS, 1>(acc, b, a, a2s, first, u, l, q, sel2, vq0, vq1, vq2, vq3, src + 4 * 7168, l16, hrd);
    __builtin_amdgcn_s_setprio(1);
    conv_step<TS, 2>(acc, a, b, a2s, first, u, l, q, sel2, vq0, vq1, vq2, vq3, src, l16, hrd);
    __builtin_amdgcn_s_setprio(0);
    conv_step<TS, 3>(acc, b, a, a2s, first, u, l, q, sel2, vq0, vq1, vq2, vq3, src, l16, hrd);
}

template <int TS, int S0>
DI void conv_far(f4 (&acc)[4], u32 ub, u32 la, u32 te, const u4& box) {
    constexpr int T2 = TS ? 5 : 4, T3 = TS ? 6 : 7, NS = S0 + 2 < T3 - 3 ? S0 + 2 : T3 - 3;
    const u4 b9 = box & 0xfffefffeu, b7 = box & ~((box & 0x00010001u) * 0xffffu);
    u4 Ue[NS], Uo[NS], Tt[4];
#pragma unroll
    for (int s = S0; s < NS; ++s) { Ue[s] = LDS(u4, ub - 16 + 512 * s); Uo[s] = LDS(u4, ub + 512 * s); }
#pragma unroll
    for (int s = 0; s < T3 - 3 - S0; ++s) Tt[s] = LDS(u4, te + 256 * s);
    { const u4 a4 = {LDS(u32, la), LDS(u32, la + 4), LDS(u32, la + 8), LDS(u32, la + 12)};
      if (S0) acc[3] = mfma(a4, Uo[T3 - 4], acc[3]); else acc[2] = mfma(a4, Uo[T2 - 4], acc[2]); }
#pragma unroll
    for (int s = S0; s < NS; ++s) {
        const f4 e = mfma(Ue[s], b9, f4{0, 0, 0, 0}), o = mfma(Uo[s], b7, f4{0, 0, 0, 0});
        const u4 z = {pkbf(e.x, e.y), pkbf(e.z, e.w), pkbf(o.x, o.y), pkbf(o.z, o.w)};
        acc[3] = mfma(z, Tt[T3 - 4 - s], acc[3]);
        if (s <= T2 - 4) acc[2] = mfma(z, Tt[T2 - 4 - s], acc[2]);
    }
}

__global__ __launch_bounds__(512, 2) void s4ffn_kernel(const u16* __restrict__ x, const u16* __restrict__ Are, const u16* __restrict__ Aim,
        const u16* __restrict__ Bre, const u16* __restrict__ Bim, const u16* __restrict__ Cre, const u16* __restrict__ Cim,
        const u16* __restrict__ logdt, const u16* __restrict__ Dsk, const u16* __restrict__ w1, const u16* __restrict__ w2,
        u16* __restrict__ out, char* __restrict__ ws) {
    const int tid = threadIdx.x, lane = tid & 63, wave = __builtin_amdgcn_readfirstlane(tid >> 6);
    const u32 b = blockIdx.x, xcd = b & 7, idx = b >> 3;
    const int fl = lane & 15, g = lane >> 4, l31 = lane & 31, hh = lane >> 5;
    STAMP_DECL;
    {
        u64 pc; asm volatile("s_getpc_b64 %0" : "=s"(pc));
        pc &= ~127ull;
        if (tid < 8 && idx + tid * 32 < PF_LINES) PREFETCH((idx + tid * 32) * 128, pc);
    }
    STAMP(0); STAMPC(22);
    asm volatile("" :: "s"(x), "s"(Are), "s"(Aim), "s"(Bre), "s"(Bim), "s"(Cre), "s"(Cim), "s"(logdt), "s"(Dsk), "s"(w1), "s"(w2), "s"(out), "s"(ws));
    u32 tag;
    const u32 w4 = wave & 3;
    u4 xr0, xr1; u32 pq0, pq1, pq2, pq3, pq4, pq5;
    if (wave < 4)
        asm volatile("v_mov_b64 v[232:233], 0\n\tv_mov_b64 v[234:235], 0\n\tv_mov_b64 v[236:237], 0\n\tv_mov_b64 v[238:239], 0\n\ts_mov_b32 exec_hi, 0xffffff\n\t"
                     "global_load_dwordx4 v[232:235], %0, %1\n\tglobal_load_dwordx4 v[236:239], %0, %1 offset:896\n\ts_mov_b32 exec_hi, -1"
                     :: "v"(lane * 16), "s"((const char*)x + (b * 8 + 2 * w4) * 896) : "v232", "v233", "v234", "v235", "v236", "v237", "v238", "v239", "memory");
    else
        asm volatile("global_load_dword v240, %0, %1\n\tglobal_load_dword v241, %0, %2\n\tglobal_load_dword v242, %0, %3\n\t"
                     "global_load_dword v243, %0, %4\n\tglobal_load_dword v244, %0, %5\n\tglobal_load_dword v245, %0, %6"
                     :: "v"(min(28 * xcd + idx, 223u) * 1024 + w4 * 256 + lane * 4), "s"(Are), "s"(Aim), "s"(Bre), "s"(Bim), "s"(Cre), "s"(Cim)
                     : "v240", "v241", "v242", "v243", "v244", "v245", "memory");
    asm volatile("s_load_dword s84, %0, 0x0\n\ts_load_dword s85, %1, %3\n\ts_load_dword s86, %2, %3" :: "s"(ws), "s"(logdt), "s"(Dsk), "s"(min(28 * xcd + idx, 223u) * 4) : "s84", "s85", "s86");
    u32 ldt2, dsk2;
#define SCAL_WAIT() asm volatile("s_waitcnt lgkmcnt(0)" : "={s84}"(tag), "={s85}"(ldt2), "={s86}"(dsk2) :: "memory")
#define ROWS_WAIT(n) asm volatile("s_waitcnt vmcnt(" #n ")" : "={v[232:235]}"(xr0), "={v[236:239]}"(xr1) :: "memory")
#define PARS_WAIT(n) asm volatile("s_waitcnt vmcnt(" #n ")" : "={v240}"(pq0), "={v241}"(pq1), "={v242}"(pq2), "={v243}"(pq3), "={v244}"(pq4), "={v245}"(pq5) :: "memory")
#define LN_ROWS() do { \
        float s0 = 0, s1 = 0, q0 = 0, q1 = 0; \
        _Pragma("unroll") for (int i = 0; i < 4; ++i) { s0 = dot2(xr0[i], 0x3f803f80u, s0); s1 = dot2(xr1[i], 0x3f803f80u, s1); q0 = dot2(xr0[i], xr0[i], q0); q1 = dot2(xr1[i], xr1[i], q1); } \
        const u32 v_ = __float_as_uint(rsum(sw16(sw32(s0, s1), sw32(q0, q1))) * (1.f / 448.f)); \
        auto e_ = __builtin_amdgcn_permlane16_swap(v_, v_, false, false);     \
        const float mean = __uint_as_float(e_[0]), var = fmaxf(__uint_as_float(e_[1]) - mean * mean, 0.f); \
        const float mb = rbf(mean), sd_ = rbf(__builtin_amdgcn_sqrtf(rbf(rbf(var) + 1e-5f))), a_ = __builtin_amdgcn_rcpf(sd_), c_ = -mb * a_; \
        const u32 st_ = (__float_as_uint(mb) >> 16) | __float_as_uint(sd_); \
        auto a2_ = __builtin_amdgcn_permlane32_swap(__float_as_uint(a_), __float_as_uint(a_), false, false); \
        auto c2_ = __builtin_amdgcn_permlane32_swap(__float_as_uint(c_), __float_as_uint(c_), false, false); \
        u4 o0, o1; \
        _Pragma("unroll") for (int i = 0; i < 4; ++i) { \
            o0[i] = pkbf(fmaf(bflo(xr0[i]), __uint_as_float(a2_[0]), __uint_as_float(c2_[0])), fmaf(bfhi(xr0[i]), __uint_as_float(a2_[0]), __uint_as_float(c2_[0]))); \
            o1[i] = pkbf(fmaf(bflo(xr1[i]), __uint_as_float(a2_[1]), __uint_as_float(c2_[1])), fmaf(bfhi(xr1[i]), __uint_as_float(a2_[1]), __uint_as_float(c2_[1]))); } \
        if (lane < 56) { LDS(u4, L_XT + w4 * 1920 + lane * 16) = o0; LDS(u4, L_XT + w4 * 1920 + 960 + lane * 16) = o1; } \
        if (l31 == 0) LDS(u32, L_XT + w4 * 1920 + hh * 960 + 896) = st_; } while (0)
#define LN_PUBLISH() do { \
        const u32 a_ = L_XT + (fl >> 2) * 960 + w4 * 128 + g * 32 + (lane & 3) * 8; \
        const u4 v0 = trread2(a_, a_ + 3840), v1 = trread2(a_ + 512, a_ + 3840 + 512); \
        GST4(lane * 16, v0, ws + WS_XG + b * 7168 + w4 * 1024, "sc1"); \
        if (w4 < 3) GST4(lane * 16, v1, ws + WS_XG + b * 7168 + w4 * 1024 + 4096, "sc1"); \
        else if (lane < 2) { const u32 s_ = L_XT + lane * 3840 + 896; const u4 st = {LDS(u32, s_), LDS(u32, s_ + 960), LDS(u32, s_ + 1920), LDS(u32, s_ + 2880)}; \
            GST4(lane * 16, st, ws + WS_STAT + b * 32, "sc1"); } } while (0)
    if (idx >= 28) {
        auto prefetch = [&](u32 k, u32 j) {
            const u32 n = ((k * 4 + idx - 28) * 8 + j) * 64 + lane, m = n - 7168;
            const u32 o = n < 7168 ? ((n / 224 & 3) + (n < 3584 ? 4 : 0)) * 200704 + (n / 896 & 3) * 28672 + n % 224 * 128 : m / 672 * 200704 + 114688 + m % 672 * 128;
            PREFETCH(min(o, 1605504u), w2);
        };
        if (wave < 4) {
            ROWS_WAIT(0);
            STAMP(1);
            LN_ROWS();
            BARRIER();
            __builtin_amdgcn_s_sleep(8);
            prefetch(1, wave); prefetch(1, wave + 4);
        } else {
            prefetch(0, wave - 4); prefetch(0, wave);
            BARRIER();
            LN_PUBLISH();
            WAITV(0);
        }
        SCAL_WAIT();
        BARRIER();
        tag += 1;
        STAMP(3);
        if (tid == 256) { const u2 ar = {tag, tag}; GST2(0, ar, ws + WS_AR + b * 8, "sc1"); }
#pragma unroll 1
        for (u32 k = 2; k < 7; ++k) prefetch(k, wave);
        if (b == 224 && wave == 0) {
            u4 t0, t1;
#pragma unroll 1
            for (int sp = 0; sp < SPIN_MAX; ++sp) {
                RLD4(t0, lane * 16, ws + WS_AR); RLD4(t1, lane * 16, ws + WS_AR + 1024);
                if (!__any(((t0.x ^ tag) | (t0.z ^ tag) | (t1.x ^ tag) | (t1.z ^ tag)) != 0)) break;
            }
            if (lane == 0) { const u2 e = {tag, tag}; GST2(0, e, ws + WS_EPOCH, "sc1"); }
        }
        WAITV(0);
        STAMP(5); STAMP_FLUSH(24);
        return;
    }
    const bool isF = wave < 4;
    const u32 q7 = idx / 7, r7 = idx - 7 * q7;
    const u32 rp = 4 * (xcd >> 1) + q7, cc = 7 * (xcd & 1) + r7, P = 4 * xcd + q7, ct = r7, dp = 28 * xcd + idx;
    const u32 rg = w4 >> 1, ch = wave & 1, hi4 = wave >> 2;
    u32 kt = (r7 + 2 * q7) % 7;
    const int r8 = lane >> 3, p8 = lane & 7, rw = (lane >> 2) & 7;
    u32 vo0 = (32 * w4 + r8) * 896 + 16 * (p8 ^ (r8 >> 1)), vo1 = ((vo0 + 8 * 896) ^ 64) + 2048, vo2 = vo0 + 16 * 896 + 1024, vo3 = vo1 + 16 * 896 - 2048;
    u32 vo4 = (16 * w4 + rw) * 3584 + 64 * hh + 16 * ((lane & 3) ^ (rw >> 2)), vo5 = vo4 + 128 + 2048, vo6 = ((vo4 + 8 * 3584) ^ 32) + 1024, vo7 = vo6 + 128 - 1024;
    vo0 += 3072; vo4 += 3072;
    const char *xb = (const char*)x + rp * 114688 - 3072, *wb = (const char*)w1 + cc * 256 - 3072;
    const char *pa, *pw;
#define RING_SET() do { asm volatile("" : "+s"(kt)); pa = xb + kt * 128; pw = wb + kt * 229376; kt = kt == 6 ? 0 : kt + 1; } while (0)
#define DMA_X(i) do { if ((i) == 0) DMAO(vo0, pa, 0); if ((i) == 1) DMAO(vo1, pa, 1024); if ((i) == 2) DMAO(vo2, pa, 2048); if ((i) == 3) DMAO(vo3, pa, 3072); } while (0)
#define DMA_W(i) do { if ((i) == 0) DMAO(vo4, pw, 0); if ((i) == 1) DMAO(vo5, pw, 1024); if ((i) == 2) DMAO(vo6, pw, 2048); if ((i) == 3) DMAO(vo7, pw, 3072); } while (0)
#define DMA_X4(l_) do { SETM0(l_); DMA_X(0); DMA_X(1); DMA_X(2); DMA_X(3); } while (0)
#define DMA_W4(l_) do { SETM0((l_) + 16384); DMA_W(0); DMA_W(1); DMA_W(2); DMA_W(3); } while (0)
    const u32 lw = L_RING + w4 * 4096;
    u4 sel2, sel3; u32 cv_ub, cv_la, cv_q2, cv_p3, hm0, hm2;
#define SEL_READ() do { sel2 = LDS(u4, L_SEL + lane * 16); sel3 = LDS(u4, L_SEL + 1024 + lane * 16); const u4 c_ = LDS(u4, L_SEL + 2048 + lane * 16); \
        cv_ub = c_.x + rg * 4608; cv_la = c_.y + rg * 5120; cv_q2 = c_.z + rg * 5120; cv_p3 = c_.w + rg * 5120; } while (0)
#define SCALARS_DONE() do { tag += 1; const u32 pat = 1 + tag % 15; hm0 = (pat & 1) << 15 | (pat & 2) << 30; hm2 = (pat & 4) << 13 | (pat & 8) << 28; } while (0)
    const char* hb = ws + WS_H + ((2 * rp + rg) * 112 + 8 * cc + 4 * ch) * 2048;
    const u32 vw2 = r8 * 896 + 16 * (p8 ^ (4 * ((r8 >> 1) & 1)));
    const char* w2b = (const char*)w2 + wave * (224 * 896) + ct * 128;
    const u32 pb0 = lw + (isF ? 32768 : 98304), pb1 = lw + (isF ? 65536 : 0);
    u4 Bw[14][2];
    const u32 aw2a = pb0 + (4 * hh + (fl >> 2)) * 128 + 16 * ((2 * (g & 1) + ((lane & 3) >> 1)) ^ (4 * ((fl >> 3) & 1))) + 8 * (lane & 1);
    const u32 aw2b = aw2a ^ 64, aw2c = aw2a - pb0 + pb1, aw2d = aw2c ^ 64;
#define W2_RD(j, p, n) do { const u32 o_ = ((n) >> 1) * 16384 + ((n) & 1) * 2048; \
        Bw[j][0] = trread2(((p) ? aw2c : aw2a) + o_, ((p) ? aw2c : aw2a) + o_ + 1024); Bw[j][1] = trread2(((p) ? aw2d : aw2b) + o_, ((p) ? aw2d : aw2b) + o_ + 1024); } while (0)
    const u32 vq0 = vw2 + 3072, vq1 = vw2 + 7168 + 2048, vq2 = vw2 + 14336 + 1024, vq3 = vw2 + 21504;
#define W2_4(src, dst) do { SETM0(dst); DMAO(vq0, src, 0); DMAO(vq1, src, 1024); DMAO(vq2, src, 2048); DMAO(vq3, src, 3072); } while (0)
#define W2_PIECE(n, dst) do { W2_4(w2b - 3072 + (n) * 57344, dst); W2_4(w2b - 3072 + (n) * 57344 + 28672, (dst) + 16384); } while (0)
#define W2_CH_IN(n) do { W2_4(w2b - 3072 + (n) * 28672, pb0 + ((n) & 1) * 16384); SETM0(L_SCR + w4 * 4352); } while (0)
#define W2_CH_OUT(n) do { W2_RD(2 * (n), 0, 2 * ((n) & 1)); W2_RD(2 * (n) + 1, 0, 2 * ((n) & 1) + 1); \
        PIN(Bw[2 * (n)][0]); PIN(Bw[2 * (n)][1]); PIN(Bw[2 * (n) + 1][0]); PIN(Bw[2 * (n) + 1][1]); } while (0)
#define W2_DRAIN(n, p) do { W2_RD(4 * (n), p, 0); W2_RD(4 * (n) + 1, p, 1); W2_RD(4 * (n) + 2, p, 2); W2_RD(4 * (n) + 3, p, 3); \
        _Pragma("unroll") for (int j = 4 * (n); j < 4 * (n) + 4; ++j) { PIN(Bw[j][0]); PIN(Bw[j][1]); } } while (0)
    f4 cacc[4];
#pragma unroll
    for (int i = 0; i < 4; ++i) cacc[i] = f4{0, 0, 0, 0};
    if (isF) {
        __builtin_amdgcn_s_setprio(2); RING_SET(); DMA_X4(lw); DMA_W4(lw); __builtin_amdgcn_s_setprio(1); RING_SET();
        DMA_X4(lw + 32768);
        __builtin_amdgcn_s_setprio(0);
        f16v acc[2][2]; f4 cs = {0, 0, 0, 0};
#pragma unroll
        for (int i = 0; i < 16; ++i) { acc[0][0][i] = 0; acc[0][1][i] = 0; acc[1][0][i] = 0; acc[1][1][i] = 0; }
        const u32 o1 = ((g ^ lane) & 1) ? 0 : 0x3f803f80u; u4 ones = {o1, o1, o1, o1};
        u32 ax0 = L_RING + (64 * rg + l31) * 128 + 16 * (hh ^ ((l31 >> 1) & 7)), ax1 = ax0 ^ 32, ax2 = ax0 ^ 64, ax3 = ax0 ^ 96;
        u32 aw0 = L_RING + 16384 + 2048 * hh + 1024 * ch + 512 * rg + 64 * (fl >> 2) + 16 * ((2 * (g & 1) + ((lane & 3) >> 1)) ^ (2 * hh)) + 8 * (lane & 1);
        u32 aw1 = aw0 ^ 272, aw2 = aw0 ^ 512, aw3 = aw0 ^ 784;
        u4 X0[4], W0[4], X1[4], W1[4];
#define G1_RD(i, X, W, a0, a1, o) do { if ((i) == 0) { X[0] = LDS(u4, a0); W[0] = trread2(aw0 + (o), aw1 + (o)); } \
        if ((i) == 1) { X[1] = LDS(u4, a0 + 4096); W[1] = trread2(aw2 + (o), aw3 + (o)); } \
        if ((i) == 2) { X[2] = LDS(u4, a1); W[2] = trread2(aw0 + (o) + 4096, aw1 + (o) + 4096); } \
        if ((i) == 3) { X[3] = LDS(u4, a1 + 4096); W[3] = trread2(aw2 + (o) + 4096, aw3 + (o) + 4096); } } while (0)
#define G1_PHASE(X, W, RD, DM, EX) do { \
        acc[0][0] = mfma32(W[0], X[0], acc[0][0]); RD(0); SB(); \
        acc[0][1] = mfma32(W[0], X[1], acc[0][1]); RD(1); SB(); \
        cs = mfma(W[0], ones, cs); SB(); \
        acc[1][0] = mfma32(W[1], X[0], acc[1][0]); DM(0); EX(0); SB(); \
        acc[1][1] = mfma32(W[1], X[1], acc[1][1]); RD(2); SB(); \
        acc[0][0] = mfma32(W[2], X[2], acc[0][0]); DM(1); RD(3); SB(); \
        cs = mfma(W[2], ones, cs); SB(); \
        acc[0][1] = mfma32(W[2], X[3], acc[0][1]); DM(2); EX(1); SB(); \
        acc[1][0] = mfma32(W[3], X[2], acc[1][0]); EX(2); SB(); \
        acc[1][1] = mfma32(W[3], X[3], acc[1][1]); DM(3); EX(3); SB(); } while (0)
#define RDA(i) G1_RD(i, X1, W1, ax2, ax3, 8192)
#define RDB(i) G1_RD(i, X0, W0, ax0, ax1, 0)
#define NONE(i)
#define EXA(i) do { if ((i) == 0) { ax0 ^= m_; ax1 ^= m_; asm volatile("" : "+s"(kt)); pa = s < 4 ? xb + kt * 128 : w2b - 3072 + (s - 4) * 57344; } \
        if ((i) == 1) { aw0 ^= m_; aw1 ^= m_; ax2 ^= m_; } if ((i) == 2) { aw2 ^= m_; aw3 ^= m_; ax3 ^= m_; \
            if (s == 4) { vo0 = vw2 + 3072; vo1 = vw2 + 7168 + 2048; vo2 = vw2 + 14336 + 1024; vo3 = vw2 + 21504; } } \
        if ((i) == 3) { ld = lw + sl * 32768; SETM0(ld); } } while (0)
#define EXB(i) do { if ((i) == 0) { pw = s < 4 ? wb + kt * 229376 : pa; kt = kt == 6 ? 0 : kt + 1; } \
        if ((i) == 1) { sl = sl == 2 ? 0 : sl + 1; m_ = 0x8000u * (sl == 0 ? 3 : sl); } \
        if ((i) == 2) { if (s == 4) { vo4 = vw2 + 28672 + 3072; vo5 = vw2 + 35840 + 2048; vo6 = vw2 + 43008 + 1024; vo7 = vw2 + 50176; } } \
        if ((i) == 3) SETM0(ld + 16384); } while (0)
        PIN(acc[0][0]); PIN(acc[0][1]); PIN(acc[1][0]); PIN(acc[1][1]);
        PIN(ax0); PIN(ax1); PIN(ax2); PIN(ax3); PIN(aw0); PIN(aw1); PIN(aw2); PIN(aw3); PIN(ones);
        __builtin_amdgcn_s_setprio(2);
        ROWS_WAIT(12);
        STAMP(1);
        DMA_W4(lw + 32768);
        RING_SET();
        LN_ROWS();
        SCAL_WAIT();
        SCALARS_DONE();
        __builtin_amdgcn_s_setprio(0);
        STAMP(2);
        WAITV(8);
        BARRIER();
        RDB(0); RDB(1); RDB(2); RDB(3);
        DMA_X4(lw + 65536);
        SETM0(lw + 65536 + 16384);
        STAMP(3);
        u32 ld, sl = 0, m_ = 0x18000;
#pragma unroll 1
        for (u32 s = 0; s < 6; ++s) {
            G1_PHASE(X0, W0, RDA, DMA_W, EXA);
            FSTAMP(s);
            WAITV(8); BARRIER();
            SB();
            G1_PHASE(X1, W1, RDB, DMA_X, EXB);
        }
        G1_PHASE(X0, W0, RDA, DMA_W, NONE);
        G1_PHASE(X1, W1, NONE, NONE, NONE);
        if (fl < 2) LDS(f4, L_CS + (64 * ch + 32 * rg + 16 * fl + 4 * g) * 4) = cs;
        STAMP(4);
        BARRIER();
#pragma unroll
        for (int tt = 0; tt < 2; ++tt) {
            const u32 sv = LDS(u32, L_ST + (64 * rg + 32 * tt + l31) * 4);
            const float m = -bflo(sv), rs = __builtin_amdgcn_rcpf(bfhi(sv));
            const f2 m2 = {m, m}, rs2 = {rs, rs};
#pragma unroll
            for (int c = 0; c < 2; ++c)
#pragma unroll
                for (int k = 0; k < 2; ++k) {
                    u4 o;
#pragma unroll
                    for (int q = 0; q < 2; ++q) {
                        const f4 cv = LDS(f4, L_CS + ((64 * ch + 32 * rg + 4 * hh) * 4 ^ (128 * c)) + (2 * k + q) * 32), av = quad(acc[c][tt], 2 * k + q);
                        const f2 v0 = __builtin_elementwise_fma(m2, f2{cv.x, cv.y}, f2{av.x, av.y}) * rs2, v1 = __builtin_elementwise_fma(m2, f2{cv.z, cv.w}, f2{av.z, av.w}) * rs2;
                        o[2 * q] = relu2(pkbf(v0.x, v0.y)) | (q ? hm2 : hm0); o[2 * q + 1] = relu2(pkbf(v1.x, v1.y));
                    }
                    const char* hp = hb + (2 * (c ^ rg) + k) * 2048;
                    if (tt) GST4(lane * 16, o, hp, "offset:1024 sc1"); else GST4(lane * 16, o, hp, "sc1");
                }
        }
        STAMP(12);
        SEL_READ();
        STAMP(5);
        BARRIER();
        WAITV(8);
        W2_DRAIN(0, 0); W2_DRAIN(1, 1);
        W2_4(w2b - 3072 + 6 * 28672, pb1);
        STAMP(6);
    } else {
        __builtin_amdgcn_s_setprio(1);
        SCAL_WAIT();
        SCALARS_DONE();
        STAMP(1);
        const float dt = rbf(expf(rg ? bfhi(ldt2) : bflo(ldt2)));
        u32 sr = L_SCR + w4 * 4352 + fl * 272 + g * 16; PIN(sr);
        asm volatile("s_mov_b32 m0, %0" :: "s"(L_SCR + w4 * 4352));
        PARS_WAIT(0);
        STAMP(2);
        f2 c0 = cmulc(f2{bflo(pq4), -bflo(pq5)}, f2{dt * bflo(pq2), dt * bflo(pq3)}), c1 = cmulc(f2{bfhi(pq4), -bfhi(pq5)}, f2{dt * bfhi(pq2), dt * bfhi(pq3)});
        float isc;
        {
            float m = rmax(fmaxf(fmaxf(fabsf(c0.x), fabsf(c0.y)), fmaxf(fabsf(c1.x), fabsf(c1.y))));
            auto s1 = __builtin_amdgcn_permlane16_swap(__float_as_uint(m), __float_as_uint(m), false, false); u32 e = max(s1[0], s1[1]);
            auto s2 = __builtin_amdgcn_permlane32_swap(e, e, false, false); e = max(s2[0], s2[1]) & 0x7f800000u;
            isc = __uint_as_float(e);
            const float sc = __uint_as_float(0x7f000000u - e);
            c0.x *= sc; c0.y *= sc; c1.x *= sc; c1.y *= sc;
        }
        f4 dk[4];
#pragma unroll
        for (int t = 0; t < 4; ++t) dk[t] = f4{0, 0, 0, 0};
        u2 arf; u32 stv; u4 xg0, xg1;
        const u32 xo = ((tid & 255) >> 1) * 7168 + (tid & 1) * 16;
#define SBAR() do { SB(); ACC_BEGIN(); asm volatile("s_barrier" ::: "memory"); ACC_END(21); SB(); } while (0)
#define PUT(e, v) asm volatile("ds_write_addtid_b32 %0 offset:%1" :: "v"(v), "i"((e) * 272) : "memory")
#define RD4(A) _Pragma("unroll") for (int s = 0; s < 4; ++s) A[s] = LDS(u4, sr + s * 64)
#define MM2(h, s) do { dk[2 * (h)] = mfmah(A[s], B0[s], dk[2 * (h)]); dk[2 * (h) + 1] = mfmah(A[s], B1[s], dk[2 * (h) + 1]); } while (0)
        u4 A[4], B0[4], B1[4];
        f2 z1, z8, z32, z256;
        h2 w8, w16, w24, a32, a64, a96, a128;
#define KG_POW(hi) do { const float a2 = fmul1(dt, (hi) ? bfhi(pq0) : bflo(pq0)) * 1.44269504089f, rev = fmul1(dt, (hi) ? bfhi(pq1) : bflo(pq1)) * 0.15915494309f; \
        z1 = zpow(a2, rev, 1.f); z8 = zpow(a2, rev, 8.f); z32 = zpow(a2, rev, 32.f); z256 = zpow(a2, rev, 256.f); } while (0)
        f2 z512, e4, e5; h2 f1, f2_, f4_, g0, g1, g2, g3;
#define KG_COLS_A(c, PEND) do { \
        const f2 z2 = cmul(z1, z1), e1 = cmulc(c, z256), z16 = cmul(z8, z8), z64 = cmul(z32, z32); z512 = cmul(z256, z256); \
        if (PEND) MM2(1, 0); \
        const f2 z4 = cmul(z2, z2), e2 = cmulc(c, z512), e3 = cmulc(e1, z512), z128 = cmul(z64, z64); \
        if (PEND) MM2(1, 1); \
        f1 = cvth(z1); f2_ = cvth(z2); f4_ = cvth(z4); g0 = cvth(c); g1 = cvth(e1); g2 = cvth(e2); g3 = cvth(e3); \
        if (PEND) MM2(1, 2); \
        e4 = cmulc(e2, z512); e5 = cmulc(e3, z512); \
        if (PEND) MM2(1, 3); \
        w8 = cvth(z8); w16 = cvth(z16); w24 = hmul(w8, w16); a32 = cvth(z32); a64 = cvth(z64); a96 = hmul(a32, a64); a128 = cvth(z128); } while (0)
#define KG_COLS_B() do { \
        const h2 f3 = hmul(f1, f2_), f6 = hmul(f2_, f4_), g04 = hmulc(g0, f4_); \
        PUT(0, g0); PUT(1, hmulc(g0, f1)); PUT(2, hmulc(g0, f2_)); PUT(3, hmulc(g0, f3)); \
        PUT(4, g04); PUT(5, hmulc(g04, f1)); PUT(6, hmulc(g0, f6)); PUT(7, hmulc(g04, f3)); \
        const f2 e6 = cmulc(e4, z512), e7 = cmulc(e5, z512); \
        PUT(8, g1); PUT(9, hmulc(g1, f2_)); PUT(10, hmulc(g1, f4_)); PUT(11, hmulc(g1, f6)); \
        const f2 e8 = cmulc(e6, z512); \
        PUT(12, g2); PUT(13, hmulc(g2, f4_)); PUT(14, g3); PUT(15, hmulc(g3, f4_)); \
        RD4(B0); \
        PUT(0, cvth(e4)); PUT(1, cvth(e5)); PUT(2, cvth(e6)); PUT(3, cvth(e7)); PUT(4, cvth(e8)); \
        RD4(B1); } while (0)
#define KG_QUAD(e, a) do { PUT(e, a); PUT((e) + 1, hmul(a, w8)); PUT((e) + 2, hmul(a, w16)); PUT((e) + 3, hmul(a, w24)); } while (0)
#define KG_ROWS_A() do { const h2 one = {1, 0}; PUT(0, one); PUT(1, w8); PUT(2, w16); PUT(3, w24); KG_QUAD(4, a32); KG_QUAD(8, a64); KG_QUAD(12, a96); RD4(A); } while (0)
#define KG_ROWS_B() do { const h2 a160 = hmul(a128, a32), a192 = hmul(a128, a64), a224 = hmul(a128, a96); \
        KG_QUAD(0, a128); MM2(0, 0); KG_QUAD(4, a160); MM2(0, 1); KG_QUAD(8, a192); MM2(0, 2); KG_QUAD(12, a224); MM2(0, 3); RD4(A); } while (0)
        KG_POW(0); KG_COLS_A(c0, 0); KG_COLS_B(); KG_ROWS_A();
        STAMP(3);
        SBAR();
        LN_PUBLISH();
        if (lane < 4) PREFETCH(lane == 0 ? WS_H + ((2 * rp + rg) * 112 + 8 * cc) * 2048 : lane == 1 ? WS_H + P * 229376 + w4 * 57344 : lane == 2 ? WS_YG + w4 * 917504 + dp * 512 : WS_YG + (P * 448 + 64 * ct) * 256, ws);
        if (lane == 0) PREFETCH((P * 64 + w4 * 16) * 896, out);
        KG_ROWS_B();
        STAMPF(15); SBAR();
        { const u32 i0 = min(w4, 2u); SETM0(L_TAB + i0 * 2048); DMAO(lane * 16, (const char*)&CTAB + i0 * 2048, 0); DMAO(lane * 16, (const char*)&CTAB + i0 * 2048, 1024);
          dma16(lane * 16, (const char*)&CTAB + 6144 + i0 * 1024, L_SEL + i0 * 1024); }
        u4 red; UNDEF(red); GLD4(red, lane * 16, (const char*)&CTAB + 9216, "");
        W2_CH_IN(0);
        KG_POW(1);
        STAMPF(16); SBAR();
        W2_CH_IN(1);
        KG_COLS_A(c1, 1);
        asm volatile("s_waitcnt vmcnt(0)" : "+v"(red) :: "memory");
        STAMPF(17); SBAR();
        if (tid == 256) { const u2 ar = {tag, tag}; GST2(0, ar, ws + WS_AR + b * 8, "sc1"); }
        W2_CH_OUT(0); W2_CH_IN(2);
        KG_COLS_B();
        STAMPF(18); SBAR();
        W2_CH_OUT(1); W2_CH_IN(3);
        KG_ROWS_A();
        STAMPF(19); SBAR();
        WAITV(4); W2_CH_OUT(2);
        UNDEF(arf); GLD2(arf, (tid & 255) * 8, ws + WS_AR, "sc1");
        KG_ROWS_B();
        MM2(1, 0); MM2(1, 1); MM2(1, 2); MM2(1, 3);
        { const u32 sw = L_SCR + w4 * 4352 + lane * 16;
          LDS(f4, sw) = self4(ch, dk[0], dk[2]) * isc; LDS(f4, sw + 1024) = self4(ch, dk[1], dk[3]) * isc; }
        asm volatile("s_waitcnt vmcnt(0)" : "+v"(arf) :: "memory");
#pragma unroll 1
        for (int sp = 0; __any(arf.x != tag) && sp < SPIN_MAX; ++sp) RLD2(arf, (tid & 255) * 8, ws + WS_AR);
        STAMP(12);
        SB(); BARRIER();
        UNDEF(stv); UNDEF(xg0); UNDEF(xg1);
        asm volatile("global_load_dword %0, %1, %2 sc1" : "+v"(stv) : "v"((tid & 127) * 4), "s"(ws + WS_STAT + rp * 512) : "memory");
        GLD4(xg0, xo, ws + WS_XG + dp * 32, "sc1"); GLD4(xg1, xo, ws + WS_XG + dp * 32 + 128 * 7168, "sc1");
        {
            const u32 a = L_SCR + (w4 ^ 1) * 4352 + lane * 16;
            const f4 s0 = self4(ch, dk[2], dk[0]) * isc + LDS(f4, a), s1 = self4(ch, dk[3], dk[1]) * isc + LDS(f4, a + 1024);
            const u32 o = red.x + ch * 16 * red.y + rg * 4352, p01 = pkbf(s0.x, s0.y), p23 = pkbf(s0.z, s0.w);
            LDS(u16, o) = (u16)p01; LDS(u16, o + red.y) = (u16)(p01 >> 16); LDS(u16, o + 2 * red.y) = (u16)p23; LDS(u16, o + 3 * red.y) = (u16)(p23 >> 16);
            LDS(u2, (ch ? red.w : red.z) + rg * 4352) = u2{pkbf(s1.x, s1.y), pkbf(s1.z, s1.w)};
            if (lane == 0) LDS(u4, L_KC + rg * 4352 + ch * 16) = u4{0, 0, 0, 0};
        }
        const u4 tixa = LDS(u4, L_TAB + (tid & 127) * 16), tixb = LDS(u4, L_TAB + 2048 + (tid & 127) * 16), tixc = LDS(u4, L_TAB + 4096 + (tid & 127) * 16);
        SEL_READ();
        asm volatile("s_waitcnt vmcnt(2)" : "+v"(stv) :: "memory");
        if (w4 < 2) LDS(u32, L_ST + (tid & 127) * 4) = stv;
        __builtin_amdgcn_s_setprio(0);
        W2_CH_OUT(3);
        STAMP(4);
        BARRIER();
        W2_4(w2b - 3072 + 6 * 28672, pb1);
        {
            u32 kc = L_KC + rg * 4352, td = L_TAB + rg * 5120 + (tid & 127) * 4; PIN(td);
            u32 v[10];
#pragma unroll
            for (int i = 0; i < 10; ++i) { const u32 tx = i < 4 ? tixa[i & 3] : i < 8 ? tixb[i & 3] : tixc[i & 3]; v[i] = (u32)LDS(u16, kc + (tx & 0xffff)) | ((u32)LDS(u16, kc + (tx >> 16)) << 16); }
#pragma unroll
            for (int i = 0; i < 4; ++i) LDS(u32, td + 512 * i) = v[i];
            if ((tid & 127) < 96) LDS(u32, td + 2048) = v[4];
            LDS(u32, td + 2432) = v[5]; LDS(u32, td + 2944) = v[6]; LDS(u32, td + 3456) = v[7]; LDS(u32, td + 3968) = v[8]; LDS(u32, td + 4480) = v[9];
        }
        {
            asm volatile("s_waitcnt vmcnt(4)" : "+v"(xg0), "+v"(xg1) :: "memory");
            u32 a = L_U + 480 + (tid & 1) * 4608 + ((tid & 255) >> 1) * 16; PIN(a);
            LDS(u4, a) = xg0; LDS(u4, a + 2048) = xg1;
            if ((tid & 255) < 60) LDS(u4, a - 480) = u4{0, 0, 0, 0};
        }
        STAMP(5);
        BARRIER();
        STAMP(6);
    }
    u4 hA0, hA1, hA2, hA3, hA4, hA5, hA6, hB0, hB1, hB2, hB3, hB4, hB5, hB6, hC0, hC1, hC2, hC3, hC4, hC5, hC6;
    const char* hrd = ws + WS_H + (P * 112 + wave * 14) * 2048;
    u32 l16 = lane * 16; PIN(l16);
#define H_TAKE_hA(n, NW) do { const char* p_ = hrd + ((n) & 1) * 14336 + ((n) >> 1) * 1024; u32 t0_, t1_, t2_, sp_; asm volatile( \
        "s_waitcnt vmcnt(" #NW ")\n\ts_mov_b32 %10, 0x10000\n" "1:\n\t" \
        "v_bitop3_b32 %8, v196, v200, %16 bitop3:0x7e\n\tv_bitop3_b32 %9, v204, v208, %16 bitop3:0x7e\n\t" \
        "v_or3_b32 %7, v221, %8, %9\n\t" \
        "v_bitop3_b32 %8, v212, v216, %16 bitop3:0x7e\n\tv_xor_b32 %9, %16, v220\n\tv_or3_b32 %7, %7, %8, %9\n\t" \
        "v_or3_b32 %8, v197, v201, v205\n\tv_or3_b32 %9, v209, v213, v217\n\tv_or3_b32 %7, %7, %8, %9\n\t" \
        "v_bitop3_b32 %8, v198, v202, %17 bitop3:0x7e\n\tv_bitop3_b32 %9, v206, v210, %17 bitop3:0x7e\n\t" \
        "v_or3_b32 %7, %7, %8, %9\n\tv_or_b32 %7, %7, v223\n\t" \
        "v_bitop3_b32 %8, v214, v218, %17 bitop3:0x7e\n\tv_xor_b32 %9, %17, v222\n\tv_or3_b32 %7, %7, %8, %9\n\t" \
        "v_or3_b32 %8, v199, v203, v207\n\tv_or3_b32 %9, v211, v215, v219\n\tv_or3_b32 %7, %7, %8, %9\n\t" \
        "v_and_b32 %7, 0x80008000, %7\n\tv_cmp_ne_u32 vcc, 0, %7\n\ts_cbranch_vccz 2f\n\t" \
        "global_load_dwordx4 v[196:199], %11, %12 sc1\n\t" \
        "global_load_dwordx4 v[200:203], %11, %12 offset:2048 sc1\n\t" \
        "global_load_dwordx4 v[204:207], %11, %13 sc1\n\t" \
        "global_load_dwordx4 v[208:211], %11, %13 offset:2048 sc1\n\t" \
        "global_load_dwordx4 v[212:215], %11, %14 sc1\n\t" \
        "global_load_dwordx4 v[216:219], %11, %14 offset:2048 sc1\n\t" \
        "global_load_dwordx4 v[220:223], %11, %15 sc1\n\t" \
        "s_sub_u32 %10, %10, 1\n\ts_waitcnt vmcnt(0)\n\ts_cbranch_scc0 1b\n" "2:" \
        : "={v[196:199]}"(hA0), "={v[200:203]}"(hA1), "={v[204:207]}"(hA2), "={v[208:211]}"(hA3), "={v[212:215]}"(hA4), "={v[216:219]}"(hA5), "={v[220:223]}"(hA6), "=&v"(t0_), "=&v"(t1_), "=&v"(t2_), "=&s"(sp_) \
        : "v"(l16), "s"(p_), "s"(p_ + 4096), "s"(p_ + 8192), "s"(p_ + 12288), "s"(hm0), "s"(hm2) : "vcc", "scc", "memory"); } while (0)
#define H_TAKE_hB(n, NW) do { const char* p_ = hrd + ((n) & 1) * 14336 + ((n) >> 1) * 1024; u32 t0_, t1_, t2_, sp_; asm volatile( \
        "s_waitcnt vmcnt(" #NW ")\n\ts_mov_b32 %10, 0x10000\n" "1:\n\t" \
        "v_bitop3_b32 %8, v224, v228, %16 bitop3:0x7e\n\tv_bitop3_b32 %9, v232, v236, %16 bitop3:0x7e\n\t" \
        "v_or3_b32 %7, v249, %8, %9\n\t" \
        "v_bitop3_b32 %8, v240, v244, %16 bitop3:0x7e\n\tv_xor_b32 %9, %16, v248\n\tv_or3_b32 %7, %7, %8, %9\n\t" \
        "v_or3_b32 %8, v225, v229, v233\n\tv_or3_b32 %9, v237, v241, v245\n\tv_or3_b32 %7, %7, %8, %9\n\t" \
        "v_bitop3_b32 %8, v226, v230, %17 bitop3:0x7e\n\tv_bitop3_b32 %9, v234, v238, %17 bitop3:0x7e\n\t" \
        "v_or3_b32 %7, %7, %8, %9\n\tv_or_b32 %7, %7, v251\n\t" \
        "v_bitop3_b32 %8, v242, v246, %17 bitop3:0x7e\n\tv_xor_b32 %9, %17, v250\n\tv_or3_b32 %7, %7, %8, %9\n\t" \
        "v_or3_b32 %8, v227, v231, v235\n\tv_or3_b32 %9, v239, v243, v247\n\tv_or3_b32 %7, %7, %8, %9\n\t" \
        "v_and_b32 %7, 0x80008000, %7\n\tv_cmp_ne_u32 vcc, 0, %7\n\ts_cbranch_vccz 2f\n\t" \
        "global_load_dwordx4 v[224:227], %11, %12 sc1\n\t" \
        "global_load_dwordx4 v[228:231], %11, %12 offset:2048 sc1\n\t" \
        "global_load_dwordx4 v[232:235], %11, %13 sc1\n\t" \
        "global_load_dwordx4 v[236:239], %11, %13 offset:2048 sc1\n\t" \
        "global_load_dwordx4 v[240:243], %11, %14 sc1\n\t" \
        "global_load_dwordx4 v[244:247], %11, %14 offset:2048 sc1\n\t" \
        "global_load_dwordx4 v[248:251], %11, %15 sc1\n\t" \
        "s_sub_u32 %10, %10, 1\n\ts_waitcnt vmcnt(0)\n\ts_cbranch_scc0 1b\n" "2:" \
        : "={v[224:227]}"(hB0), "={v[228:231]}"(hB1), "={v[232:235]}"(hB2), "={v[236:239]}"(hB3), "={v[240:243]}"(hB4), "={v[244:247]}"(hB5), "={v[248:251]}"(hB6), "=&v"(t0_), "=&v"(t1_), "=&v"(t2_), "=&s"(sp_) \
        : "v"(l16), "s"(p_), "s"(p_ + 4096), "s"(p_ + 8192), "s"(p_ + 12288), "s"(hm0), "s"(hm2) : "vcc", "scc", "memory"); } while (0)
#define H_TAKE_hC(n, NW) do { const char* p_ = hrd + ((n) & 1) * 14336 + ((n) >> 1) * 1024; u32 t0_, t1_, t2_, sp_; asm volatile( \
        "s_waitcnt vmcnt(" #NW ")\n\ts_mov_b32 %10, 0x10000\n" "1:\n\t" \
        "v_bitop3_b32 %8, v168, v172, %16 bitop3:0x7e\n\tv_bitop3_b32 %9, v176, v180, %16 bitop3:0x7e\n\t" \
        "v_or3_b32 %7, v193, %8, %9\n\t" \
        "v_bitop3_b32 %8, v184, v188, %16 bitop3:0x7e\n\tv_xor_b32 %9, %16, v192\n\tv_or3_b32 %7, %7, %8, %9\n\t" \
        "v_or3_b32 %8, v169, v173, v177\n\tv_or3_b32 %9, v181, v185, v189\n\tv_or3_b32 %7, %7, %8, %9\n\t" \
        "v_bitop3_b32 %8, v170, v174, %17 bitop3:0x7e\n\tv_bitop3_b32 %9, v178, v182, %17 bitop3:0x7e\n\t" \
        "v_or3_b32 %7, %7, %8, %9\n\tv_or_b32 %7, %7, v195\n\t" \
        "v_bitop3_b32 %8, v186, v190, %17 bitop3:0x7e\n\tv_xor_b32 %9, %17, v194\n\tv_or3_b32 %7, %7, %8, %9\n\t" \
        "v_or3_b32 %8, v171, v175, v179\n\tv_or3_b32 %9, v183, v187, v191\n\tv_or3_b32 %7, %7, %8, %9\n\t" \
        "v_and_b32 %7, 0x80008000, %7\n\tv_cmp_ne_u32 vcc, 0, %7\n\ts_cbranch_vccz 2f\n\t" \
        "global_load_dwordx4 v[168:171], %11, %12 sc1\n\t" \
        "global_load_dwordx4 v[172:175], %11, %12 offset:2048 sc1\n\t" \
        "global_load_dwordx4 v[176:179], %11, %13 sc1\n\t" \
        "global_load_dwordx4 v[180:183], %11, %13 offset:2048 sc1\n\t" \
        "global_load_dwordx4 v[184:187], %11, %14 sc1\n\t" \
        "global_load_dwordx4 v[188:191], %11, %14 offset:2048 sc1\n\t" \
        "global_load_dwordx4 v[192:195], %11, %15 sc1\n\t" \
        "s_sub_u32 %10, %10, 1\n\ts_waitcnt vmcnt(0)\n\ts_cbranch_scc0 1b\n" "2:" \
        : "={v[168:171]}"(hC0), "={v[172:175]}"(hC1), "={v[176:179]}"(hC2), "={v[180:183]}"(hC3), "={v[184:187]}"(hC4), "={v[188:191]}"(hC5), "={v[192:195]}"(hC6), "=&v"(t0_), "=&v"(t1_), "=&v"(t2_), "=&s"(sp_) \
        : "v"(l16), "s"(p_), "s"(p_ + 4096), "s"(p_ + 8192), "s"(p_ + 12288), "s"(hm0), "s"(hm2) : "vcc", "scc", "memory"); } while (0)
    {
        const int kh = hi4, ts = ch;
        dma16(r8 * 896 + 16 * (p8 ^ (wave & 6)), (const char*)x + (P * 64 + wave * 8) * 896 + ct * 128, L_XE + wave * 1024);
        STAMP(13);
        if (isF) { if (ch) conv_far<1, 2>(cacc, cv_ub, cv_la, cv_p3, sel3); else conv_far<0, 2>(cacc, cv_ub, cv_la, cv_p3, sel3); }
        else { if (ch) conv_far<1, 0>(cacc, cv_ub, cv_la, cv_p3, sel3); else conv_far<0, 0>(cacc, cv_ub, cv_la, cv_p3, sel3); }
        STAMP(14);
        {
            u32 u = cv_ub - 192 - 256 * hi4, l = cv_la + 416 - 256 * hi4, q = cv_q2 + 256 * hi4; PIN(u); PIN(l); PIN(q);
            if (ch) conv_steps<1>(cacc, isF, u, l, q, cv_la, sel2, vq0, vq1, vq2, vq3, w2b + 16 * 7168 - 3072, pb0, l16, hrd);
            else conv_steps<0>(cacc, isF, u, l, q, cv_la, sel2, vq0, vq1, vq2, vq3, w2b + 16 * 7168 - 3072, pb0, l16, hrd);
        }
        STAMP(7);
        WAITV(15);
        BARRIER();
        u32 ra = lw + (isF ? 0 : 65536) + 16384 + lane * 16; PIN(ra);
        LDS(f4, ra) = self4(kh, cacc[0], cacc[2]);
        LDS(f4, ra + 1024) = self4(kh, cacc[1], cacc[3]);
        H_LDB(0, hrd + 14336); H_LDB(1, hrd + 14336); H_LDB(2, hrd + 14336); H_LDB(3, hrd + 14336); H_LDB(4, hrd + 14336); H_LDB(5, hrd + 14336); H_LDB(6, hrd + 14336);
        ra = pb1 + 16384 + lane * 16; PIN(ra);
        BARRIER();
        const float dk = rg ? bfhi(dsk2) : bflo(dsk2);
#pragma unroll
        for (int i = 0; i < 2; ++i) {
            const int tau = kh ? (ts ? 5 + i : 4 + 3 * i) : (ts ? 1 + i : 3 * i);
            const f4 y = self4(kh, cacc[2 + i], cacc[i]) + LDS(f4, ra + i * 1024);
            const u2 uu = LDS(u2, L_U + rg * 4608 + 480 + (256 * tau + 16 * fl + 4 * g) * 2);
            const u32 y01 = pkbf(y.x, y.y), y23 = pkbf(y.z, y.w);
            const u32 s01 = pkbf(bflo(uu.x) * dk, bfhi(uu.x) * dk), s23 = pkbf(bflo(uu.y) * dk, bfhi(uu.y) * dk);
            const u4 o = {pkbf(bflo(y01) + bflo(s01), bfhi(y01) + bfhi(s01)), tag, pkbf(bflo(y23) + bflo(s23), bfhi(y23) + bfhi(s23)), tag};
            GST4((fl >> 2) * 114688 + ((fl & 3) + 4 * g) * 16, o, ws + WS_YG + (4 * tau * 448 + 2 * dp + rg) * 256, "sc1");
        }
    }
    STAMP(8);
    u32 pw0 = pb0 + lane * 16, pw1 = (pb1 + lane * 16) ^ 128; PIN(pw0); PIN(pw1);
#define H_MM1(h, j, o) do { a0 = mfma32(h##j, Bw[(o) + j][0], a0); a1 = mfma32(h##j, Bw[(o) + j][1], a1); } while (0)
#define H_MMA(h, o) do { H_MM1(h, 0, o); H_MM1(h, 1, o); H_MM1(h, 2, o); H_MM1(h, 3, o); H_MM1(h, 4, o); H_MM1(h, 5, o); H_MM1(h, 6, o); } while (0)
#define W2_SGN(j) do { Bw[j][0] ^= u4{hm0, 0, hm2, 0}; Bw[j][1] ^= u4{hm0, 0, hm2, 0}; PIN(Bw[j][0]); PIN(Bw[j][1]); } while (0)
#define H_MMS(h, o) do { H_MM1(h, 0, o); W2_SGN((o) + 1); SB(); H_MM1(h, 1, o); W2_SGN((o) + 2); SB(); H_MM1(h, 2, o); W2_SGN((o) + 3); SB(); H_MM1(h, 3, o); W2_SGN((o) + 4); SB(); \
        H_MM1(h, 4, o); W2_SGN((o) + 5); SB(); H_MM1(h, 5, o); W2_SGN((o) + 6); SB(); H_MM1(h, 6, o); } while (0)
#define H_ZERO() _Pragma("unroll") for (int i = 0; i < 16; ++i) { a0[i] = 0; a1[i] = 0; }
#define H_PUT(pw) _Pragma("unroll") for (int q = 0; q < 4; ++q) { const u32 a_ = ((pw) ^ (q >> 1) * 64) + q * 1024; LDS(f4, a_) = quad(a0, q); LDS(f4, a_ + 16384) = quad(a1, q); }
    f16v a0, a1;
    H_ZERO();
    __builtin_amdgcn_s_setprio(3);
    W2_SGN(0); W2_SGN(7);
    H_TAKE_hA(0, 9);
    {
        const char* p3 = hrd + 1024;
        H_MM1(hA, 0, 0); W2_SGN(1); W2_RD(8, 0, 0); SB(); H_LDC(0, p3); H_MM1(hA, 1, 0); W2_SGN(2); W2_RD(9, 0, 1); SB(); H_LDC(1, p3);
        H_MM1(hA, 2, 0); W2_SGN(3); W2_RD(10, 0, 2); SB(); H_LDC(2, p3); H_MM1(hA, 3, 0); W2_SGN(4); W2_RD(11, 0, 3); SB(); H_LDC(3, p3);
        H_MM1(hA, 4, 0); W2_SGN(5); W2_RD(12, 1, 0); SB(); H_LDC(4, p3); H_MM1(hA, 5, 0); W2_SGN(6); W2_RD(13, 1, 1); SB(); H_LDC(5, p3);
        H_MM1(hA, 6, 0); SB(); H_LDC(6, p3);
    }
    STAMP(9);
    __builtin_amdgcn_s_setprio(2);
    H_TAKE_hB(1, 9);
    {
        const char* p3 = hrd + 14336 + 1024;
        H_MM1(hB, 0, 7); W2_SGN(8); SB(); H_LDA(0, p3); H_MM1(hB, 1, 7); W2_SGN(9); SB(); H_LDA(1, p3); H_MM1(hB, 2, 7); W2_SGN(10); SB(); H_LDA(2, p3);
        H_MM1(hB, 3, 7); W2_SGN(11); SB(); H_LDA(3, p3); H_MM1(hB, 4, 7); W2_SGN(12); SB(); H_LDA(4, p3); H_MM1(hB, 5, 7); W2_SGN(13); SB(); H_LDA(5, p3);
        H_MM1(hB, 6, 7); SB(); H_LDA(6, p3);
    }
    H_PUT(pw0);
    H_ZERO();
    __builtin_amdgcn_s_setprio(1);
    H_TAKE_hC(2, 7); H_MMA(hC, 0);
    const u32 al = lane & 3, dc = 16 * rg + (lane >> 2);
    const u32 yo = (32 * hi4 + dc) * 256 + 16 * al + 128 * ch;
    const char* yb = ws + WS_YG + (P * 448 + 64 * ct) * 256;
    u4 yv0, yv1;
    asm volatile("global_load_dwordx4 v[168:171], %0, %1 sc1\n\tglobal_load_dwordx4 v[172:175], %0, %1 offset:64 sc1" :: "v"(yo), "s"(yb)
                 : "v168", "v169", "v170", "v171", "v172", "v173", "v174", "v175", "memory");
    u32 ax = L_XE + (16 * al + 8 * ch) * 128 + ((64 * hi4 + 2 * dc) ^ (32 * al)); PIN(ax);
    const u32 o_ = L_RING + hi4 * 16384 + (2 * (al & 1) + ch) * 1024 + (dc ^ (4 * al)) * 16;
    u32 aF = o_ + (al & 2 ? 65536 : 32768), aS = o_ + (al & 2 ? 0 : 98304); PIN(aF); PIN(aS);
    u32 xe[8];
#pragma unroll
    for (int i = 0; i < 8; ++i) xe[i] = LDS(u16, ax + i * 128);
    __builtin_amdgcn_s_setprio(0);
    H_TAKE_hA(3, 2);
    a0 = mfma32(hA0, Bw[7][0], a0); a0 = mfma32(hA1, Bw[8][0], a0); a0 = mfma32(hA2, Bw[9][0], a0); a0 = mfma32(hA3, Bw[10][0], a0);
    a0 = mfma32(hA4, Bw[11][0], a0); a0 = mfma32(hA5, Bw[12][0], a0); a0 = mfma32(hA6, Bw[13][0], a0);
    SB();
    a1 = mfma32(hA0, Bw[7][1], a1); SB();
    {
        u32 t0_, sp_;
        asm volatile("s_waitcnt vmcnt(0)\n\ts_mov_b32 %3, 0x10000\n" "1:\n\t"
                     "v_bitop3_b32 %2, v169, v171, %6 bitop3:0x7e\n\tv_bitop3_b32 %2, %2, v173, %6 bitop3:0xf6\n\tv_bitop3_b32 %2, %2, v175, %6 bitop3:0xf6\n\t"
                     "v_cmp_ne_u32 vcc, 0, %2\n\ts_cbranch_vccz 2f\n\t"
                     "global_load_dwordx4 v[168:171], %4, %5 sc1\n\tglobal_load_dwordx4 v[172:175], %4, %5 offset:64 sc1\n\t"
                     "s_sub_u32 %3, %3, 1\n\ts_waitcnt vmcnt(0)\n\ts_cbranch_scc0 1b\n" "2:"
                     : "={v[168:171]}"(yv0), "={v[172:175]}"(yv1), "=&v"(t0_), "=&s"(sp_) : "v"(yo), "s"(yb), "s"(tag) : "vcc", "scc", "memory");
    }
    u32 pre[4];
    a1 = mfma32(hA1, Bw[8][1], a1); pre[0] = pkbf(bflo(xe[0]) + bflo(yv0.x), bflo(xe[1]) + bfhi(yv0.x)); PIN(pre[0]); LDS(f4, pw1) = quad(a0, 0); SB();
    a1 = mfma32(hA2, Bw[9][1], a1); pre[1] = pkbf(bflo(xe[2]) + bflo(yv0.z), bflo(xe[3]) + bfhi(yv0.z)); PIN(pre[1]); LDS(f4, pw1 + 1024) = quad(a0, 1); SB();
    a1 = mfma32(hA3, Bw[10][1], a1); pre[2] = pkbf(bflo(xe[4]) + bflo(yv1.x), bflo(xe[5]) + bfhi(yv1.x)); PIN(pre[2]); LDS(f4, (pw1 ^ 64) + 2048) = quad(a0, 2); SB();
    a1 = mfma32(hA4, Bw[11][1], a1); pre[3] = pkbf(bflo(xe[6]) + bflo(yv1.z), bflo(xe[7]) + bfhi(yv1.z)); PIN(pre[3]); LDS(f4, (pw1 ^ 64) + 3072) = quad(a0, 3); SB();
    a1 = mfma32(hA5, Bw[12][1], a1); a1 = mfma32(hA6, Bw[13][1], a1);
#pragma unroll
    for (int q = 0; q < 4; ++q) LDS(f4, (pw1 ^ (q >> 1) * 64) + q * 1024 + 16384) = quad(a1, q);
    BARRIER();
    STAMP(10);
#pragma unroll
    for (int i = 0; i < 2; ++i) {
        const f4 r = ((LDS(f4, aF + i * 512) + LDS(f4, aF + i * 512 + 4096)) + (LDS(f4, aF + i * 512 + 8192) + LDS(f4, aF + i * 512 + 12288))) +
                     ((LDS(f4, aS + i * 512) + LDS(f4, aS + i * 512 + 4096)) + (LDS(f4, aS + i * 512 + 8192) + LDS(f4, aS + i * 512 + 12288)));
        const u32 a01 = pre[2 * i], a23 = pre[2 * i + 1], f01 = pkbf(r.x, r.y), f23 = pkbf(r.z, r.w);
        const u32 o01 = pkbf(bflo(a01) + bflo(f01), bfhi(a01) + bfhi(f01)), o23 = pkbf(bflo(a23) + bflo(f23), bfhi(a23) + bfhi(f23));
        LDS(u16, ax + i * 512) = (u16)o01; LDS(u16, ax + i * 512 + 128) = (u16)(o01 >> 16); LDS(u16, ax + i * 512 + 256) = (u16)o23; LDS(u16, ax + i * 512 + 384) = (u16)(o23 >> 16);
    }
    BARRIER();
    { const u4 ov = LDS(u4, L_XE + tid * 16); GST4(r8 * 896 + 16 * (p8 ^ (wave & 6)), ov, (char*)out + (P * 64 + wave * 8) * 896 + ct * 128, "sc1"); }
    STAMP(11); STAMPC(23);
    STAMP_FLUSH(24);
}

extern "C" void kernel_launch(void* const* d_in, const int* in_sizes, int n_in, void* d_out, int out_size, void* d_ws, size_t ws_size,
                              hipStream_t stream) {
    (void)hipFuncSetAttribute((const void*)s4ffn_kernel, hipFuncAttributeMaxDynamicSharedMemorySize, LDS_BYTES);
    s4ffn_kernel<<<256, 512, LDS_BYTES, stream>>>((const u16*)d_in[0], (const u16*)d_in[1], (const u16*)d_in[2], (const u16*)d_in[3],
        (const u16*)d_in[4], (const u16*)d_in[5], (const u16*)d_in[6], (const u16*)d_in[7], (const u16*)d_in[8],
        (const u16*)d_in[10], (const u16*)d_in[11], (u16*)d_out, (char*)d_ws);
}
```

```cpp
#include <hip/hip_runtime.h>
#include <stdint.h>

typedef unsigned short u16;
typedef unsigned int u32;
typedef unsigned long long u64;
typedef __attribute__((ext_vector_type(8))) __bf16 bf8;
typedef __attribute__((ext_vector_type(2))) __bf16 bf2;
typedef __attribute__((ext_vector_type(4))) short s4;
typedef __attribute__((ext_vector_type(4))) float f4;
typedef __attribute__((ext_vector_type(16))) float f16v;
typedef __attribute__((ext_vector_type(2))) float f2;
typedef __attribute__((ext_vector_type(4))) u32 u4;
typedef __attribute__((ext_vector_type(2))) u32 u2;
#define LAS __attribute__((address_space(3)))
#define DI __device__ __forceinline__
#define LDS(T, off) (*(LAS T*)(uintptr_t)(u32)(off))

constexpr u32 WS_EPOCH = 0;
constexpr u32 WS_STAT = 4096;
constexpr u32 WS_AR = WS_STAT + 16384;
constexpr u32 WS_XG = 65536;
constexpr u32 WS_YG = WS_XG + 3670016;
constexpr u32 WS_H = WS_YG + 3670016;
constexpr u32 WS_STAMP = 32u << 20;
constexpr u32 L_TAB = 0;
constexpr u32 L_CS = 10240;
constexpr u32 L_ST = 10752;
constexpr u32 L_SEL = 12288;
constexpr u32 L_SCR = 15360;
constexpr u32 L_KC = L_SCR + 2048;
constexpr u32 L_XE = L_SCR;
constexpr u32 L_U = 23552;
constexpr u32 L_RING = 32768;
constexpr u32 L_XT = L_RING + 98304;
constexpr u32 LDS_BYTES = 163840;
constexpr int SPIN_MAX = 1 << 16;
constexpr u32 PF_LINES = 188;

#define STAMP(i)
#define STAMPC(i)
#define STAMPF(i)
#define FSTAMP(s)
#define ACC_BEGIN()
#define ACC_END(i)
#define STAMP_DECL
#define STAMP_FLUSH(n)

DI float bflo(u32 v) { return __uint_as_float(v << 16); }
DI float bfhi(u32 v) { return __uint_as_float(v & 0xffff0000u); }
DI u32 pkbf(float a, float b) { f2 v = {a, b}; return __builtin_bit_cast(u32, __builtin_convertvector(v, bf2)); }
DI float rbf(float a) { return bflo(pkbf(a, a)); }
DI f4 mfma(u4 a, u4 b, f4 c) { return __builtin_amdgcn_mfma_f32_16x16x32_bf16(__builtin_bit_cast(bf8, a), __builtin_bit_cast(bf8, b), c, 0, 0, 0); }
DI f16v mfma32(u4 a, u4 b, f16v c) { return __builtin_amdgcn_mfma_f32_32x32x16_bf16(__builtin_bit_cast(bf8, a), __builtin_bit_cast(bf8, b), c, 0, 0, 0); }
DI u2 trread(u32 addr) { return __builtin_bit_cast(u2, __builtin_amdgcn_ds_read_tr16_b64_v4i16((LAS s4*)(uintptr_t)addr)); }
DI u4 trread2(u32 a, u32 b) { const u2 l = trread(a), h = trread(b); return u4{l.x, l.y, h.x, h.y}; }
template <int C> DI float dpp(float x) { return __builtin_bit_cast(float, __builtin_amdgcn_mov_dpp(__builtin_bit_cast(int, x), C, 0xf, 0xf, true)); }
DI float rsum(float x) { x += dpp<0xB1>(x); x += dpp<0x4E>(x); x += dpp<0x141>(x); x += dpp<0x140>(x); return x; }
DI float rmax(float x) { x = fmaxf(x, dpp<0xB1>(x)); x = fmaxf(x, dpp<0x4E>(x)); x = fmaxf(x, dpp<0x141>(x)); return fmaxf(x, dpp<0x140>(x)); }
DI float sw32(float a, float b) { auto s = __builtin_amdgcn_permlane32_swap(__float_as_uint(a), __float_as_uint(b), false, false); return __uint_as_float(s[0]) + __uint_as_float(s[1]); }
DI float sw16(float a, float b) { auto s = __builtin_amdgcn_permlane16_swap(__float_as_uint(a), __float_as_uint(b), false, false); return __uint_as_float(s[0]) + __uint_as_float(s[1]); }
DI float dot2(u32 a, u32 b, float c) { return __builtin_amdgcn_fdot2_f32_bf16(__builtin_bit_cast(bf2, a), __builtin_bit_cast(bf2, b), c, false); }
DI f4 self4(int c, f4 a, f4 b) { return c ? a : b; }
DI f2 cmul(f2 a, f2 b) { float x, y;
    asm("v_mul_f32 %0, %2, %4\n\tv_mul_f32 %1, %3, %4\n\tv_fma_f32 %0, -%3, %5, %0\n\tv_fmac_f32 %1, %2, %5" : "=&v"(x), "=&v"(y) : "v"(a.x), "v"(a.y), "v"(b.x), "v"(b.y));
    return f2{x, y}; }
DI f2 cmulc(f2 a, f2 b) { float x, y;
    asm("v_mul_f32 %0, %2, %4\n\tv_mul_f32 %1, %3, %4\n\tv_fmac_f32 %0, %3, %5\n\tv_fma_f32 %1, -%2, %5, %1" : "=&v"(x), "=&v"(y) : "v"(a.x), "v"(a.y), "v"(b.x), "v"(b.y));
    return f2{x, y}; }
DI float fmul1(float a, float b) { float r; asm("v_mul_f32 %0, %1, %2" : "=v"(r) : "v"(a), "v"(b)); return r; }
typedef __attribute__((ext_vector_type(2))) _Float16 h2;
typedef __attribute__((ext_vector_type(8))) _Float16 h8;
DI h2 cvth(f2 a) { return __builtin_convertvector(a, h2); }
DI h2 hmul(h2 a, h2 b) { const h2 bx = {b.x, b.x}, by = {-b.y, b.y}; return __builtin_elementwise_fma(__builtin_shufflevector(a, a, 1, 0), by, a * bx); }
DI h2 hmulc(h2 a, h2 b) { const h2 bx = {b.x, b.x}, by = {b.y, -b.y}; return __builtin_elementwise_fma(__builtin_shufflevector(a, a, 1, 0), by, a * bx); }
DI f4 mfmah(u4 a, u4 b, f4 c) { return __builtin_amdgcn_mfma_f32_16x16x32_f16(__builtin_bit_cast(h8, a), __builtin_bit_cast(h8, b), c, 0, 0, 0); }
DI u32 relu2(u32 v) { typedef __attribute__((ext_vector_type(2))) short s2; const s2 z = {0, 0}; return __builtin_bit_cast(u32, __builtin_elementwise_max(__builtin_bit_cast(s2, v), z)); }
DI f2 zpow(float a2, float rev, float p) {
    float e = __builtin_amdgcn_exp2f(a2 * p), r = __builtin_amdgcn_fractf(rev * p);
    float x, y;
    asm("s_nop 0\n\tv_mul_f32 %0, %2, %3\n\tv_mul_f32 %1, %2, %4" : "=&v"(x), "=&v"(y) : "v"(e), "v"(__builtin_amdgcn_cosf(r)), "v"(__builtin_amdgcn_sinf(r)));
    return f2{x, y};
}
DI f4 quad(const f16v& v, int q) { return f4{v[4 * q], v[4 * q + 1], v[4 * q + 2], v[4 * q + 3]}; }

#define SB() __builtin_amdgcn_sched_barrier(0)
#define BARRIER() asm volatile("s_waitcnt lgkmcnt(0)\n\ts_barrier" ::: "memory")
#define WAITV(n) asm volatile("s_waitcnt vmcnt(" #n ")" ::: "memory")
DI void dma16(u32 voff, const void* base, u32 lds) {
    asm volatile("s_mov_b32 m0, %2\n\ts_nop 0\n\tglobal_load_lds_dwordx4 %0, %1" :: "v"(voff), "s"(base), "s"(lds) : "memory");
}
#define SETM0(lds) asm volatile("s_mov_b32 m0, %0\n\ts_nop 0" :: "s"(lds) : "memory")
#define DMAO(voff, base, off) asm volatile("global_load_lds_dwordx4 %0, %1 offset:" #off :: "v"(voff), "s"(base) : "memory")
#define GLD4(dst, voff, base, mod) asm volatile("global_load_dwordx4 %0, %1, %2 " mod : "+v"(dst) : "v"(voff), "s"(base) : "memory")
#define GLD2(dst, voff, base, mod) asm volatile("global_load_dwordx2 %0, %1, %2 " mod : "+v"(dst) : "v"(voff), "s"(base) : "memory")
#define GLD1(dst, voff, base) asm volatile("global_load_dword %0, %1, %2" : "+v"(dst) : "v"(voff), "s"(base) : "memory")
#define GST4(voff, val, base, mod) asm volatile("global_store_dwordx4 %0, %1, %2 " mod "\n\ts_nop 1" :: "v"(voff), "v"(val), "s"(base) : "memory")
#define GST2(voff, val, base, mod) asm volatile("global_store_dwordx2 %0, %1, %2 " mod :: "v"(voff), "v"(val), "s"(base) : "memory")
#define RLD2(dst, voff, base) asm volatile("global_load_dwordx2 %0, %1, %2 sc1\n\ts_waitcnt vmcnt(0)" : "=&v"(dst) : "v"(voff), "s"(base) : "memory")
#define RLD4(dst, voff, base) asm volatile("global_load_dwordx4 %0, %1, %2 sc1\n\ts_waitcnt vmcnt(0)" : "=&v"(dst) : "v"(voff), "s"(base) : "memory")
#define PREFETCH(voff, base) asm volatile("global_load_dword v255, %0, %1" :: "v"(voff), "s"(base) : "v255", "memory")
#define PIN(x) asm volatile("" : "+v"(x))
#define UNDEF(x) asm volatile("" : "=v"(x))

struct ConstTab { u32 tix[3][128][4]; u32 sel[3][64][4]; u32 red[64][4]; };
constexpr int cidx(int l) {
    if (l < 0) return 16 + l;
    const int s = (l >= 256) + (l >= 512) + (l >= 1024);
    return 16 + 128 * s + (s ? (l + (1 << (s - 1)) - 1 + ((l >> s) & 1)) >> s : l);
}
constexpr ConstTab make_tab() {
    ConstTab c{};
    for (int t = 0; t < 128; ++t) {
        u32 v[12] = {};
        for (int i = 0; i < 5; ++i) {
            const int u = t + 128 * i < 607 ? t + 128 * i : 607, pi = u >= 304, ip = u - 304 * pi, top = (ip < 24 ? 1056 : 592) - pi - 2 * ip;
            v[i] = (u32)cidx(top > 1028 ? -1 : top) * 2 | (u32)cidx(top - 1 > 1028 ? -1 : top - 1) << 17;
        }
        v[5] = (404 + (t >> 1) + 2 * (t & 1)) * 0x20002u + 0x20000u;
        v[6] = v[5] + 64 * 0x20002u;
        for (int i = 0; i < 3; ++i) {
            const int u = t + 128 * i < 303 ? t + 128 * i : 303, q = u & 3, m = 2 * (52 + (u >> 2)) + (q < 2 ? 2 - 4 * q : 9 - 4 * q);
            v[7 + i] = (u32)cidx(m >= 129 ? 8 * m - (m == 256) : -1) * 2 | (u32)cidx(m - 2 >= 129 ? 8 * (m - 2) : -1) << 17;
        }
        for (int i = 0; i < 12; ++i) c.tix[i >> 2][t][i & 3] = v[i];
    }
    for (int l = 0; l < 64; ++l) {
        const int fl = l & 15, g = l >> 4, x2 = fl & 7;
        const int m2 = x2 < 2 ? -2 : x2 < 5 ? -1 : 0;
        for (int q = 0; q < 4; ++q)
            for (int h = 0; h < 2; ++h) {
                const int e2 = x2 - 2 * q - h, e = 8 * g + 2 * q + h - 8 - fl;
                c.sel[0][l][q] |= (u32)(((e2 >= -5) + (e2 >= -2) + (e2 >= 3) + (e2 >= 6) - 2 - m2) * 0x0202 + 0x0100) << (16 * h);
                c.sel[1][l][q] |= (u32)(e >= -3 && e <= 3 ? 0x3f80 : e >= -4 && e <= 4 ? 0x3f81 : 0) << (16 * h);
            }
        const int mul = fl < 8 ? 8 : fl < 12 ? 4 : 2, base = fl < 8 ? fl : fl < 12 ? 248 + fl : 384 + (fl & 1) + 64 * ((fl - 12) >> 1);
        c.red[l][0] = L_KC + (16 + base + mul * 4 * g) * 2; c.red[l][1] = 2 * mul;
        c.red[l][2] = L_KC + (fl < 4 ? (528 + 32 * fl + 4 * g) * 2 : fl == 4 && g == 0 ? 1312 : 1528);
        c.red[l][3] = L_KC + (fl < 4 ? (544 + 32 * fl + 4 * g) * 2 : 1528);
        c.sel[2][l][0] = L_U + 2 * (224 + 16 * fl + 8 * g);
        c.sel[2][l][1] = L_TAB + 4 * ((fl & 1) * 304 + ((16 - fl + 8 * g) >> 1));
        c.sel[2][l][2] = L_TAB + 2432 + 8 * (2 * (2 + (fl >> 3) - g) + m2 - 4);
        c.sel[2][l][3] = L_TAB + 3456 + 16 * (12 + fl - 4 * g);
    }
    return c;
}
static __device__ const ConstTab CTAB __attribute__((aligned(128))) = make_tab();

#define H_LDA(j, p) do { \
        if ((j) == 0) asm volatile("global_load_dwordx4 v[196:199], %0, %1 offset:0 sc1" :: "v"(l16), "s"((p) + 0) : "v196", "v197", "v198", "v199", "memory"); \
        if ((j) == 1) asm volatile("global_load_dwordx4 v[200:203], %0, %1 offset:2048 sc1" :: "v"(l16), "s"((p) + 0) : "v200", "v201", "v202", "v203", "memory"); \
        if ((j) == 2) asm volatile("global_load_dwordx4 v[204:207], %0, %1 offset:0 sc1" :: "v"(l16), "s"((p) + 4096) : "v204", "v205", "v206", "v207", "memory"); \
        if ((j) == 3) asm volatile("global_load_dwordx4 v[208:211], %0, %1 offset:2048 sc1" :: "v"(l16), "s"((p) + 4096) : "v208", "v209", "v210", "v211", "memory"); \
        if ((j) == 4) asm volatile("global_load_dwordx4 v[212:215], %0, %1 offset:0 sc1" :: "v"(l16), "s"((p) + 8192) : "v212", "v213", "v214", "v215", "memory"); \
        if ((j) == 5) asm volatile("global_load_dwordx4 v[216:219], %0, %1 offset:2048 sc1" :: "v"(l16), "s"((p) + 8192) : "v216", "v217", "v218", "v219", "memory"); \
        if ((j) == 6) asm volatile("global_load_dwordx4 v[220:223], %0, %1 offset:0 sc1" :: "v"(l16), "s"((p) + 12288) : "v220", "v221", "v222", "v223", "memory"); \
        } while (0)
#define H_LDB(j, p) do { \
        if ((j) == 0) asm volatile("global_load_dwordx4 v[224:227], %0, %1 offset:0 sc1" :: "v"(l16), "s"((p) + 0) : "v224", "v225", "v226", "v227", "memory"); \
        if ((j) == 1) asm volatile("global_load_dwordx4 v[228:231], %0, %1 offset:2048 sc1" :: "v"(l16), "s"((p) + 0) : "v228", "v229", "v230", "v231", "memory"); \
        if ((j) == 2) asm volatile("global_load_dwordx4 v[232:235], %0, %1 offset:0 sc1" :: "v"(l16), "s"((p) + 4096) : "v232", "v233", "v234", "v235", "memory"); \
        if ((j) == 3) asm volatile("global_load_dwordx4 v[236:239], %0, %1 offset:2048 sc1" :: "v"(l16), "s"((p) + 4096) : "v236", "v237", "v238", "v239", "memory"); \
        if ((j) == 4) asm volatile("global_load_dwordx4 v[240:243], %0, %1 offset:0 sc1" :: "v"(l16), "s"((p) + 8192) : "v240", "v241", "v242", "v243", "memory"); \
        if ((j) == 5) asm volatile("global_load_dwordx4 v[244:247], %0, %1 offset:2048 sc1" :: "v"(l16), "s"((p) + 8192) : "v244", "v245", "v246", "v247", "memory"); \
        if ((j) == 6) asm volatile("global_load_dwordx4 v[248:251], %0, %1 offset:0 sc1" :: "v"(l16), "s"((p) + 12288) : "v248", "v249", "v250", "v251", "memory"); \
        } while (0)
#define H_LDC(j, p) do { \
        if ((j) == 0) asm volatile("global_load_dwordx4 v[168:171], %0, %1 offset:0 sc1" :: "v"(l16), "s"((p) + 0) : "v168", "v169", "v170", "v171", "memory"); \
        if ((j) == 1) asm volatile("global_load_dwordx4 v[172:175], %0, %1 offset:2048 sc1" :: "v"(l16), "s"((p) + 0) : "v172", "v173", "v174", "v175", "memory"); \
        if ((j) == 2) asm volatile("global_load_dwordx4 v[176:179], %0, %1 offset:0 sc1" :: "v"(l16), "s"((p) + 4096) : "v176", "v177", "v178", "v179", "memory"); \
        if ((j) == 3) asm volatile("global_load_dwordx4 v[180:183], %0, %1 offset:2048 sc1" :: "v"(l16), "s"((p) + 4096) : "v180", "v181", "v182", "v183", "memory"); \
        if ((j) == 4) asm volatile("global_load_dwordx4 v[184:187], %0, %1 offset:0 sc1" :: "v"(l16), "s"((p) + 8192) : "v184", "v185", "v186", "v187", "memory"); \
        if ((j) == 5) asm volatile("global_load_dwordx4 v[188:191], %0, %1 offset:2048 sc1" :: "v"(l16), "s"((p) + 8192) : "v188", "v189", "v190", "v191", "memory"); \
        if ((j) == 6) asm volatile("global_load_dwordx4 v[192:195], %0, %1 offset:0 sc1" :: "v"(l16), "s"((p) + 12288) : "v192", "v193", "v194", "v195", "memory"); \
        } while (0)
struct ConvOps { u4 B[8], A0, A1; u2 v2, v3; };
template <int TS, int KK> DI void conv_read(ConvOps& o, int i, u32 u, u32 l, u32 q) {
    constexpr int ORD[2][8] = {{0, 3, 4, 7, 2, 6, 1, 5}, {1, 2, 5, 6, 0, 4, 3, 7}};
    auto two = [&](u32 a) { return u2{LDS(u32, a), LDS(u32, a + 4)}; };
    if (i == 0) { const u2 t = two(l + 704 - 64 * KK); o.A0.x = t.x; o.A0.y = t.y; }
    if (i == 1) { const u2 t = two(l + 712 - 64 * KK); o.A0.z = t.x; o.A0.w = t.y; }
    if (i >= 2 && i < 6) o.B[ORD[TS][i - 2]] = LDS(u4, u + 192 - 64 * KK + 512 * ORD[TS][i - 2]);
    if (i == 6) { const u2 t = two(l + 192 - 64 * KK); o.A1.x = t.x; o.A1.y = t.y; }
    if (i == 7) { const u2 t = two(l + 200 - 64 * KK); o.A1.z = t.x; o.A1.w = t.y; }
    if (i >= 8 && i < 12 - TS) o.B[ORD[TS][i - 4]] = LDS(u4, u + 192 - 64 * KK + 512 * ORD[TS][i - 4]);
    if (i == 12) { o.v2 = LDS(u2, q + 64 * KK); o.v3 = LDS(u2, q + 64 * KK + 512); }
}
template <int TS, int KK>
DI void conv_step(f4 (&acc)[4], const ConvOps& c, ConvOps& n, const u4& a2s, bool use2s, u32 u, u32 l, u32 q, const u4& sel2,
                  u32 vq0, u32 vq1, u32 vq2, u32 vq3, const char* src, u32 l16, const char* hrd) {
    constexpr int T[2][4] = {{0, 3, 4, 7}, {1, 2, 5, 6}};
    auto seg2 = [&](u2 v) { u4 r;
        r.x = __builtin_amdgcn_perm(v.y, v.x, sel2.x); r.y = __builtin_amdgcn_perm(v.y, v.x, sel2.y);
        r.z = __builtin_amdgcn_perm(v.y, v.x, sel2.z); r.w = __builtin_amdgcn_perm(v.y, v.x, sel2.w); return r; };
    u4 A2, A3;
    int m = 0;
#pragma unroll
    for (int d = 0; d < 4; ++d)
#pragma unroll
        for (int i = 0; i < 4; ++i)
            if (T[TS][i] >= d) {
                acc[i] = mfma(d == 0 ? c.A0 : d == 1 ? c.A1 : d == 2 ? A2 : A3, c.B[T[TS][i] - d], acc[i]);
                if (KK < 3) conv_read<TS, KK + 1>(n, m, u, l, q);
                if (m == 1) { A2 = seg2(c.v2); if (KK == 0 && use2s) A2 = a2s; PIN(A2); }
                if (m == 3) { A3 = seg2(c.v3); PIN(A3); }
                if (KK < 2) { if (m == 2) DMAO(vq0, src, 0); if (m == 5) DMAO(vq1, src, 1024); if (m == 8) DMAO(vq2, src, 2048); if (m == 11) DMAO(vq3, src, 3072); }
                if (KK == 3 && !(m & 1)) H_LDA(m >> 1, hrd);
                SB();
                ++m;
            }
}
template <int TS>
DI void conv_steps(f4 (&acc)[4], bool first, u32 u, u32 l, u32 q, u32 la, const u4& sel2, u32 vq0, u32 vq1, u32 vq2, u32 vq3, const char* src, u32 dst, u32 l16, const char* hrd) {
    ConvOps a, b;
#pragma unroll
    for (int i = 0; i < 13; ++i) conv_read<TS, 0>(a, i, u, l, q);
    const u4 a2s = {LDS(u32, la + 96), LDS(u32, la + 100), LDS(u32, la + 104), LDS(u32, la + 108)};
    SETM0(dst); __builtin_amdgcn_s_setprio(3); SB();
    conv_step<TS, 0>(acc, a, b, a2s, first, u, l, q, sel2, vq0, vq1, vq2, vq3, src, l16, hrd);
    SETM0(dst + 16384); __builtin_amdgcn_s_setprio(2); SB();
    conv_step<TS, 1>(acc, b, a, a2s, first, u, l, q, sel2, vq0, vq1, vq2, vq3, src + 4 * 7168, l16, hrd);
    __builtin_amdgcn_s_setprio(1);
    conv_step<TS, 2>(acc, a, b, a2s, first, u, l, q, sel2, vq0, vq1, vq2, vq3, src, l16, hrd);
    __builtin_amdgcn_s_setprio(0);
    conv_step<TS, 3>(acc, b, a, a2s, first, u, l, q, sel2, vq0, vq1, vq2, vq3, src, l16, hrd);
}

template <int TS, int S0>
DI void conv_far(f4 (&acc)[4], u32 ub, u32 la, u32 te, const u4& box) {
    constexpr int T2 = TS ? 5 : 4, T3 = TS ? 6 : 7, NS = S0 + 2 < T3 - 3 ? S0 + 2 : T3 - 3;
    const u4 b9 = box & 0xfffefffeu, b7 = box & ~((box & 0x00010001u) * 0xffffu);
    u4 Ue[NS], Uo[NS], Tt[4];
#pragma unroll
    for (int s = S0; s < NS; ++s) { Ue[s] = LDS(u4, ub - 16 + 512 * s); Uo[s] = LDS(u4, ub + 512 * s); }
#pragma unroll
    for (int s = 0; s < T3 - 3 - S0; ++s) Tt[s] = LDS(u4, te + 256 * s);
    { const u4 a4 = {LDS(u32, la), LDS(u32, la + 4), LDS(u32, la + 8), LDS(u32, la + 12)};
      if (S0) acc[3] = mfma(a4, Uo[T3 - 4], acc[3]); else acc[2] = mfma(a4, Uo[T2 - 4], acc[2]); }
#pragma unroll
    for (int s = S0; s < NS; ++s) {
        const f4 e = mfma(Ue[s], b9, f4{0, 0, 0, 0}), o = mfma(Uo[s], b7, f4{0, 0, 0, 0});
        const u4 z = {pkbf(e.x, e.y), pkbf(e.z, e.w), pkbf(o.x, o.y), pkbf(o.z, o.w)};
        acc[3] = mfma(z, Tt[T3 - 4 - s], acc[3]);
        if (s <= T2 - 4) acc[2] = mfma(z, Tt[T2 - 4 - s], acc[2]);
    }
}

__global__ __launch_bounds__(512, 2) void s4ffn_kernel(const u16* __restrict__ x, const u16* __restrict__ Are, const u16* __restrict__ Aim,
        const u16* __restrict__ Bre, const u16* __restrict__ Bim, const u16* __restrict__ Cre, const u16* __restrict__ Cim,
        const u16* __restrict__ logdt, const u16* __restrict__ Dsk, const u16* __restrict__ w1, const u16* __restrict__ w2,
        u16* __restrict__ out, char* __restrict__ ws) {
    const int tid = threadIdx.x, lane = tid & 63, wave = __builtin_amdgcn_readfirstlane(tid >> 6);
    const u32 b = blockIdx.x, xcd = b & 7, idx = b >> 3;
    const int fl = lane & 15, g = lane >> 4, l31 = lane & 31, hh = lane >> 5;
    STAMP_DECL;
    const u32 tk0 = (u32)__builtin_amdgcn_s_memrealtime(); u32 tk1 = 0;
    {
        u64 pc; asm volatile("s_getpc_b64 %0" : "=s"(pc));
        pc &= ~127ull;
        if (tid < 8 && idx + tid * 32 < PF_LINES) PREFETCH((idx + tid * 32) * 128, pc);
    }
    STAMP(0); STAMPC(22);
    asm volatile("" :: "s"(x), "s"(Are), "s"(Aim), "s"(Bre), "s"(Bim), "s"(Cre), "s"(Cim), "s"(logdt), "s"(Dsk), "s"(w1), "s"(w2), "s"(out), "s"(ws));
    u32 tag;
    const u32 w4 = wave & 3;
    u4 xr0, xr1; u32 pq0, pq1, pq2, pq3, pq4, pq5;
    if (wave < 4)
        asm volatile("v_mov_b64 v[232:233], 0\n\tv_mov_b64 v[234:235], 0\n\tv_mov_b64 v[236:237], 0\n\tv_mov_b64 v[238:239], 0\n\ts_mov_b32 exec_hi, 0xffffff\n\t"
                     "global_load_dwordx4 v[232:235], %0, %1\n\tglobal_load_dwordx4 v[236:239], %0, %1 offset:896\n\ts_mov_b32 exec_hi, -1"
                     :: "v"(lane * 16), "s"((const char*)x + (b * 8 + 2 * w4) * 896) : "v232", "v233", "v234", "v235", "v236", "v237", "v238", "v239", "memory");
    else
        asm volatile("global_load_dword v240, %0, %1\n\tglobal_load_dword v241, %0, %2\n\tglobal_load_dword v242, %0, %3\n\t"
                     "global_load_dword v243, %0, %4\n\tglobal_load_dword v244, %0, %5\n\tglobal_load_dword v245, %0, %6"
                     :: "v"(min(28 * xcd + idx, 223u) * 1024 + w4 * 256 + lane * 4), "s"(Are), "s"(Aim), "s"(Bre), "s"(Bim), "s"(Cre), "s"(Cim)
                     : "v240", "v241", "v242", "v243", "v244", "v245", "memory");
    asm volatile("s_load_dword s84, %0, 0x0\n\ts_load_dword s85, %1, %3\n\ts_load_dword s86, %2, %3" :: "s"(ws), "s"(logdt), "s"(Dsk), "s"(min(28 * xcd + idx, 223u) * 4) : "s84", "s85", "s86");
    u32 ldt2, dsk2;
#define SCAL_WAIT() asm volatile("s_waitcnt lgkmcnt(0)" : "={s84}"(tag), "={s85}"(ldt2), "={s86}"(dsk2) :: "memory")
#define ROWS_WAIT(n) asm volatile("s_waitcnt vmcnt(" #n ")" : "={v[232:235]}"(xr0), "={v[236:239]}"(xr1) :: "memory")
#define PARS_WAIT(n) asm volatile("s_waitcnt vmcnt(" #n ")" : "={v240}"(pq0), "={v241}"(pq1), "={v242}"(pq2), "={v243}"(pq3), "={v244}"(pq4), "={v245}"(pq5) :: "memory")
#define LN_ROWS() do { \
        float s0 = 0, s1 = 0, q0 = 0, q1 = 0; \
        _Pragma("unroll") for (int i = 0; i < 4; ++i) { s0 = dot2(xr0[i], 0x3f803f80u, s0); s1 = dot2(xr1[i], 0x3f803f80u, s1); q0 = dot2(xr0[i], xr0[i], q0); q1 = dot2(xr1[i], xr1[i], q1); } \
        const u32 v_ = __float_as_uint(rsum(sw16(sw32(s0, s1), sw32(q0, q1))) * (1.f / 448.f)); \
        auto e_ = __builtin_amdgcn_permlane16_swap(v_, v_, false, false);     \
        const float mean = __uint_as_float(e_[0]), var = fmaxf(__uint_as_float(e_[1]) - mean * mean, 0.f); \
        const float mb = rbf(mean), sd_ = rbf(__builtin_amdgcn_sqrtf(rbf(rbf(var) + 1e-5f))), a_ = __builtin_amdgcn_rcpf(sd_), c_ = -mb * a_; \
        const u32 st_ = (__float_as_uint(mb) >> 16) | __float_as_uint(sd_); \
        auto a2_ = __builtin_amdgcn_permlane32_swap(__float_as_uint(a_), __float_as_uint(a_), false, false); \
        auto c2_ = __builtin_amdgcn_permlane32_swap(__float_as_uint(c_), __float_as_uint(c_), false, false); \
        u4 o0, o1; \
        _Pragma("unroll") for (int i = 0; i < 4; ++i) { \
            o0[i] = pkbf(fmaf(bflo(xr0[i]), __uint_as_float(a2_[0]), __uint_as_float(c2_[0])), fmaf(bfhi(xr0[i]), __uint_as_float(a2_[0]), __uint_as_float(c2_[0]))); \
            o1[i] = pkbf(fmaf(bflo(xr1[i]), __uint_as_float(a2_[1]), __uint_as_float(c2_[1])), fmaf(bfhi(xr1[i]), __uint_as_float(a2_[1]), __uint_as_float(c2_[1]))); } \
        if (lane < 56) { LDS(u4, L_XT + w4 * 1920 + lane * 16) = o0; LDS(u4, L_XT + w4 * 1920 + 960 + lane * 16) = o1; } \
        if (l31 == 0) LDS(u32, L_XT + w4 * 1920 + hh * 960 + 896) = st_; } while (0)
#define LN_PUBLISH() do { \
        const u32 a_ = L_XT + (fl >> 2) * 960 + w4 * 128 + g * 32 + (lane & 3) * 8; \
        const u4 v0 = trread2(a_, a_ + 3840), v1 = trread2(a_ + 512, a_ + 3840 + 512); \
        GST4(lane * 16, v0, ws + WS_XG + b * 7168 + w4 * 1024, "sc1"); \
        if (w4 < 3) GST4(lane * 16, v1, ws + WS_XG + b * 7168 + w4 * 1024 + 4096, "sc1"); \
        else if (lane < 2) { const u32 s_ = L_XT + lane * 3840 + 896; const u4 st = {LDS(u32, s_), LDS(u32, s_ + 960), LDS(u32, s_ + 1920), LDS(u32, s_ + 2880)}; \
            GST4(lane * 16, st, ws + WS_STAT + b * 32, "sc1"); } } while (0)
    if (idx >= 28) {
        auto prefetch = [&](u32 k, u32 j) {
            const u32 n = ((k * 4 + idx - 28) * 8 + j) * 64 + lane, m = n - 7168;
            const u32 o = n < 7168 ? ((n / 224 & 3) + (n < 3584 ? 4 : 0)) * 200704 + (n / 896 & 3) * 28672 + n % 224 * 128 : m / 672 * 200704 + 114688 + m % 672 * 128;
            PREFETCH(min(o, 1605504u), w2);
        };
        if (wave < 4) {
            ROWS_WAIT(0);
            STAMP(1);
            LN_ROWS();
            BARRIER();
            __builtin_amdgcn_s_sleep(8);
            prefetch(1, wave); prefetch(1, wave + 4);
        } else {
            prefetch(0, wave - 4); prefetch(0, wave);
            BARRIER();
            LN_PUBLISH();
            WAITV(0);
        }
        SCAL_WAIT();
        BARRIER();
        tag += 1;
        STAMP(3);
        if (tid == 256) { const u2 ar = {tag, tk0}; GST2(0, ar, ws + WS_AR + b * 8, "sc1"); }
#pragma unroll 1
        for (u32 k = 2; k < 7; ++k) prefetch(k, wave);
        if (b == 224 && wave == 0) {
            u4 t0, t1;
#pragma unroll 1
            for (int sp = 0; sp < SPIN_MAX; ++sp) {
                RLD4(t0, lane * 16, ws + WS_AR); RLD4(t1, lane * 16, ws + WS_AR + 1024);
                if (!__any(((t0.x ^ tag) | (t0.z ^ tag) | (t1.x ^ tag) | (t1.z ^ tag)) != 0)) break;
            }
            if (lane == 0) { const u2 e = {tag, tag}; GST2(0, e, ws + WS_EPOCH, "sc1"); }
        }
        WAITV(0);
        STAMP(5); STAMP_FLUSH(24);
        return;
    }
    const bool isF = wave < 4;
    const u32 q7 = idx / 7, r7 = idx - 7 * q7;
    const u32 rp = 4 * (xcd >> 1) + q7, cc = 7 * (xcd & 1) + r7, P = 4 * xcd + q7, ct = r7, dp = 28 * xcd + idx;
    const u32 rg = w4 >> 1, ch = wave & 1, hi4 = wave >> 2;
    u32 kt = (r7 + 2 * q7) % 7;
    const int r8 = lane >> 3, p8 = lane & 7, rw = (lane >> 2) & 7;
    u32 vo0 = (32 * w4 + r8) * 896 + 16 * (p8 ^ (r8 >> 1)), vo1 = ((vo0 + 8 * 896) ^ 64) + 2048, vo2 = vo0 + 16 * 896 + 1024, vo3 = vo1 + 16 * 896 - 2048;
    u32 vo4 = (16 * w4 + rw) * 3584 + 64 * hh + 16 * ((lane & 3) ^ (rw >> 2)), vo5 = vo4 + 128 + 2048, vo6 = ((vo4 + 8 * 3584) ^ 32) + 1024, vo7 = vo6 + 128 - 1024;
    vo0 += 3072; vo4 += 3072;
    const char *xb = (const char*)x + rp * 114688 - 3072, *wb = (const char*)w1 + cc * 256 - 3072;
    const char *pa, *pw;
#define RING_SET() do { asm volatile("" : "+s"(kt)); pa = xb + kt * 128; pw = wb + kt * 229376; kt = kt == 6 ? 0 : kt + 1; } while (0)
#define DMA_X(i) do { if ((i) == 0) DMAO(vo0, pa, 0); if ((i) == 1) DMAO(vo1, pa, 1024); if ((i) == 2) DMAO(vo2, pa, 2048); if ((i) == 3) DMAO(vo3, pa, 3072); } while (0)
#define DMA_W(i) do { if ((i) == 0) DMAO(vo4, pw, 0); if ((i) == 1) DMAO(vo5, pw, 1024); if ((i) == 2) DMAO(vo6, pw, 2048); if ((i) == 3) DMAO(vo7, pw, 3072); } while (0)
#define DMA_X4(l_) do { SETM0(l_); DMA_X(0); DMA_X(1); DMA_X(2); DMA_X(3); } while (0)
#define DMA_W4(l_) do { SETM0((l_) + 16384); DMA_W(0); DMA_W(1); DMA_W(2); DMA_W(3); } while (0)
    const u32 lw = L_RING + w4 * 4096;
    u4 sel2, sel3; u32 cv_ub, cv_la, cv_q2, cv_p3, hm0, hm2;
#define SEL_READ() do { sel2 = LDS(u4, L_SEL + lane * 16); sel3 = LDS(u4, L_SEL + 1024 + lane * 16); const u4 c_ = LDS(u4, L_SEL + 2048 + lane * 16); \
        cv_ub = c_.x + rg * 4608; cv_la = c_.y + rg * 5120; cv_q2 = c_.z + rg * 5120; cv_p3 = c_.w + rg * 5120; } while (0)
#define SCALARS_DONE() do { tag += 1; const u32 pat = 1 + tag % 15; hm0 = (pat & 1) << 15 | (pat & 2) << 30; hm2 = (pat & 4) << 13 | (pat & 8) << 28; } while (0)
    const char* hb = ws + WS_H + ((2 * rp + rg) * 112 + 8 * cc + 4 * ch) * 2048;
    const u32 vw2 = r8 * 896 + 16 * (p8 ^ (4 * ((r8 >> 1) & 1)));
    const char* w2b = (const char*)w2 + wave * (224 * 896) + ct * 128;
    const u32 pb0 = lw + (isF ? 32768 : 98304), pb1 = lw + (isF ? 65536 : 0);
    u4 Bw[14][2];
    const u32 aw2a = pb0 + (4 * hh + (fl >> 2)) * 128 + 16 * ((2 * (g & 1) + ((lane & 3) >> 1)) ^ (4 * ((fl >> 3) & 1))) + 8 * (lane & 1);
    const u32 aw2b = aw2a ^ 64, aw2c = aw2a - pb0 + pb1, aw2d = aw2c ^ 64;
#define W2_RD(j, p, n) do { const u32 o_ = ((n) >> 1) * 16384 + ((n) & 1) * 2048; \
        Bw[j][0] = trread2(((p) ? aw2c : aw2a) + o_, ((p) ? aw2c : aw2a) + o_ + 1024); Bw[j][1] = trread2(((p) ? aw2d : aw2b) + o_, ((p) ? aw2d : aw2b) + o_ + 1024); } while (0)
    const u32 vq0 = vw2 + 3072, vq1 = vw2 + 7168 + 2048, vq2 = vw2 + 14336 + 1024, vq3 = vw2 + 21504;
#define W2_4(src, dst) do { SETM0(dst); DMAO(vq0, src, 0); DMAO(vq1, src, 1024); DMAO(vq2, src, 2048); DMAO(vq3, src, 3072); } while (0)
#define W2_PIECE(n, dst) do { W2_4(w2b - 3072 + (n) * 57344, dst); W2_4(w2b - 3072 + (n) * 57344 + 28672, (dst) + 16384); } while (0)
#define W2_CH_IN(n) do { W2_4(w2b - 3072 + (n) * 28672, pb0 + ((n) & 1) * 16384); SETM0(L_SCR + w4 * 4352); } while (0)
#define W2_CH_OUT(n) do { W2_RD(2 * (n), 0, 2 * ((n) & 1)); W2_RD(2 * (n) + 1, 0, 2 * ((n) & 1) + 1); \
        PIN(Bw[2 * (n)][0]); PIN(Bw[2 * (n)][1]); PIN(Bw[2 * (n) + 1][0]); PIN(Bw[2 * (n) + 1][1]); } while (0)
#define W2_DRAIN(n, p) do { W2_RD(4 * (n), p, 0); W2_RD(4 * (n) + 1, p, 1); W2_RD(4 * (n) + 2, p, 2); W2_RD(4 * (n) + 3, p, 3); \
        _Pragma("unroll") for (int j = 4 * (n); j < 4 * (n) + 4; ++j) { PIN(Bw[j][0]); PIN(Bw[j][1]); } } while (0)
    f4 cacc[4];
#pragma unroll
    for (int i = 0; i < 4; ++i) cacc[i] = f4{0, 0, 0, 0};
    if (isF) {
        __builtin_amdgcn_s_setprio(2); RING_SET(); DMA_X4(lw); DMA_W4(lw); __builtin_amdgcn_s_setprio(1); RING_SET();
        DMA_X4(lw + 32768);
        __builtin_amdgcn_s_setprio(0);
        f16v acc[2][2]; f4 cs = {0, 0, 0, 0};
#pragma unroll
        for (int i = 0; i < 16; ++i) { acc[0][0][i] = 0; acc[0][1][i] = 0; acc[1][0][i] = 0; acc[1][1][i] = 0; }
        const u32 o1 = ((g ^ lane) & 1) ? 0 : 0x3f803f80u; u4 ones = {o1, o1, o1, o1};
        u32 ax0 = L_RING + (64 * rg + l31) * 128 + 16 * (hh ^ ((l31 >> 1) & 7)), ax1 = ax0 ^ 32, ax2 = ax0 ^ 64, ax3 = ax0 ^ 96;
        u32 aw0 = L_RING + 16384 + 2048 * hh + 1024 * ch + 512 * rg + 64 * (fl >> 2) + 16 * ((2 * (g & 1) + ((lane & 3) >> 1)) ^ (2 * hh)) + 8 * (lane & 1);
        u32 aw1 = aw0 ^ 272, aw2 = aw0 ^ 512, aw3 = aw0 ^ 784;
        u4 X0[4], W0[4], X1[4], W1[4];
#define G1_RD(i, X, W, a0, a1, o) do { if ((i) == 0) { X[0] = LDS(u4, a0); W[0] = trread2(aw0 + (o), aw1 + (o)); } \
        if ((i) == 1) { X[1] = LDS(u4, a0 + 4096); W[1] = trread2(aw2 + (o), aw3 + (o)); } \
        if ((i) == 2) { X[2] = LDS(u4, a1); W[2] = trread2(aw0 + (o) + 4096, aw1 + (o) + 4096); } \
        if ((i) == 3) { X[3] = LDS(u4, a1 + 4096); W[3] = trread2(aw2 + (o) + 4096, aw3 + (o) + 4096); } } while (0)
#define G1_PHASE(X, W, RD, DM, EX) do { \
        acc[0][0] = mfma32(W[0], X[0], acc[0][0]); RD(0); SB(); \
        acc[0][1] = mfma32(W[0], X[1], acc[0][1]); RD(1); SB(); \
        cs = mfma(W[0], ones, cs); SB(); \
        acc[1][0] = mfma32(W[1], X[0], acc[1][0]); DM(0); EX(0); SB(); \
        acc[1][1] = mfma32(W[1], X[1], acc[1][1]); RD(2); SB(); \
        acc[0][0] = mfma32(W[2], X[2], acc[0][0]); DM(1); RD(3); SB(); \
        cs = mfma(W[2], ones, cs); SB(); \
        acc[0][1] = mfma32(W[2], X[3], acc[0][1]); DM(2); EX(1); SB(); \
        acc[1][0] = mfma32(W[3], X[2], acc[1][0]); EX(2); SB(); \
        acc[1][1] = mfma32(W[3], X[3], acc[1][1]); DM(3); EX(3); SB(); } while (0)
#define RDA(i) G1_RD(i, X1, W1, ax2, ax3, 8192)
#define RDB(i) G1_RD(i, X0, W0, ax0, ax1, 0)
#define NONE(i)
#define EXA(i) do { if ((i) == 0) { ax0 ^= m_; ax1 ^= m_; asm volatile("" : "+s"(kt)); pa = s < 4 ? xb + kt * 128 : w2b - 3072 + (s - 4) * 57344; } \
        if ((i) == 1) { aw0 ^= m_; aw1 ^= m_; ax2 ^= m_; } if ((i) == 2) { aw2 ^= m_; aw3 ^= m_; ax3 ^= m_; \
            if (s == 4) { vo0 = vw2 + 3072; vo1 = vw2 + 7168 + 2048; vo2 = vw2 + 14336 + 1024; vo3 = vw2 + 21504; } } \
        if ((i) == 3) { ld = lw + sl * 32768; SETM0(ld); } } while (0)
#define EXB(i) do { if ((i) == 0) { pw = s < 4 ? wb + kt * 229376 : pa; kt = kt == 6 ? 0 : kt + 1; } \
        if ((i) == 1) { sl = sl == 2 ? 0 : sl + 1; m_ = 0x8000u * (sl == 0 ? 3 : sl); } \
        if ((i) == 2) { if (s == 4) { vo4 = vw2 + 28672 + 3072; vo5 = vw2 + 35840 + 2048; vo6 = vw2 + 43008 + 1024; vo7 = vw2 + 50176; } } \
        if ((i) == 3) SETM0(ld + 16384); } while (0)
        PIN(acc[0][0]); PIN(acc[0][1]); PIN(acc[1][0]); PIN(acc[1][1]);
        PIN(ax0); PIN(ax1); PIN(ax2); PIN(ax3); PIN(aw0); PIN(aw1); PIN(aw2); PIN(aw3); PIN(ones);
        __builtin_amdgcn_s_setprio(2);
        ROWS_WAIT(12);
        STAMP(1);
        DMA_W4(lw + 32768);
        RING_SET();
        LN_ROWS();
        SCAL_WAIT();
        SCALARS_DONE();
        __builtin_amdgcn_s_setprio(0);
        STAMP(2);
        WAITV(8);
        BARRIER();
        RDB(0); RDB(1); RDB(2); RDB(3);
        DMA_X4(lw + 65536);
        SETM0(lw + 65536 + 16384);
        STAMP(3);
        u32 ld, sl = 0, m_ = 0x18000;
#pragma unroll 1
        for (u32 s = 0; s < 6; ++s) {
            G1_PHASE(X0, W0, RDA, DMA_W, EXA);
            FSTAMP(s);
            WAITV(8); BARRIER();
            SB();
            G1_PHASE(X1, W1, RDB, DMA_X, EXB);
        }
        G1_PHASE(X0, W0, RDA, DMA_W, NONE);
        G1_PHASE(X1, W1, NONE, NONE, NONE);
        if (fl < 2) LDS(f4, L_CS + (64 * ch + 32 * rg + 16 * fl + 4 * g) * 4) = cs;
        STAMP(4);
        BARRIER();
#pragma unroll
        for (int tt = 0; tt < 2; ++tt) {
            const u32 sv = LDS(u32, L_ST + (64 * rg + 32 * tt + l31) * 4);
            const float m = -bflo(sv), rs = __builtin_amdgcn_rcpf(bfhi(sv));
            const f2 m2 = {m, m}, rs2 = {rs, rs};
#pragma unroll
            for (int c = 0; c < 2; ++c)
#pragma unroll
                for (int k = 0; k < 2; ++k) {
                    u4 o;
#pragma unroll
                    for (int q = 0; q < 2; ++q) {
                        const f4 cv = LDS(f4, L_CS + ((64 * ch + 32 * rg + 4 * hh) * 4 ^ (128 * c)) + (2 * k + q) * 32), av = quad(acc[c][tt], 2 * k + q);
                        const f2 v0 = __builtin_elementwise_fma(m2, f2{cv.x, cv.y}, f2{av.x, av.y}) * rs2, v1 = __builtin_elementwise_fma(m2, f2{cv.z, cv.w}, f2{av.z, av.w}) * rs2;
                        o[2 * q] = relu2(pkbf(v0.x, v0.y)) | (q ? hm2 : hm0); o[2 * q + 1] = relu2(pkbf(v1.x, v1.y));
                    }
                    const char* hp = hb + (2 * (c ^ rg) + k) * 2048;
                    if (tt) GST4(lane * 16, o, hp, "offset:1024 sc1"); else GST4(lane * 16, o, hp, "sc1");
                }
        }
        STAMP(12);
        SEL_READ();
        STAMP(5);
        BARRIER();
        WAITV(8);
        W2_DRAIN(0, 0); W2_DRAIN(1, 1);
        W2_4(w2b - 3072 + 6 * 28672, pb1);
        STAMP(6);
    } else {
        __builtin_amdgcn_s_setprio(1);
        SCAL_WAIT();
        SCALARS_DONE();
        STAMP(1);
        const float dt = rbf(expf(rg ? bfhi(ldt2) : bflo(ldt2)));
        u32 sr = L_SCR + w4 * 4352 + fl * 272 + g * 16; PIN(sr);
        asm volatile("s_mov_b32 m0, %0" :: "s"(L_SCR + w4 * 4352));
        PARS_WAIT(0);
        STAMP(2);
        f2 c0 = cmulc(f2{bflo(pq4), -bflo(pq5)}, f2{dt * bflo(pq2), dt * bflo(pq3)}), c1 = cmulc(f2{bfhi(pq4), -bfhi(pq5)}, f2{dt * bfhi(pq2), dt * bfhi(pq3)});
        float isc;
        {
            float m = rmax(fmaxf(fmaxf(fabsf(c0.x), fabsf(c0.y)), fmaxf(fabsf(c1.x), fabsf(c1.y))));
            auto s1 = __builtin_amdgcn_permlane16_swap(__float_as_uint(m), __float_as_uint(m), false, false); u32 e = max(s1[0], s1[1]);
            auto s2 = __builtin_amdgcn_permlane32_swap(e, e, false, false); e = max(s2[0], s2[1]) & 0x7f800000u;
            isc = __uint_as_float(e);
            const float sc = __uint_as_float(0x7f000000u - e);
            c0.x *= sc; c0.y *= sc; c1.x *= sc; c1.y *= sc;
        }
        f4 dk[4];
#pragma unroll
        for (int t = 0; t < 4; ++t) dk[t] = f4{0, 0, 0, 0};
        u2 arf; u32 stv; u4 xg0, xg1;
        const u32 xo = ((tid & 255) >> 1) * 7168 + (tid & 1) * 16;
#define SBAR() do { SB(); ACC_BEGIN(); asm volatile("s_barrier" ::: "memory"); ACC_END(21); SB(); } while (0)
#define PUT(e, v) asm volatile("ds_write_addtid_b32 %0 offset:%1" :: "v"(v), "i"((e) * 272) : "memory")
#define RD4(A) _Pragma("unroll") for (int s = 0; s < 4; ++s) A[s] = LDS(u4, sr + s * 64)
#define MM2(h, s) do { dk[2 * (h)] = mfmah(A[s], B0[s], dk[2 * (h)]); dk[2 * (h) + 1] = mfmah(A[s], B1[s], dk[2 * (h) + 1]); } while (0)
        u4 A[4], B0[4], B1[4];
        f2 z1, z8, z32, z256;
        h2 w8, w16, w24, a32, a64, a96, a128;
#define KG_POW(hi) do { const float a2 = fmul1(dt, (hi) ? bfhi(pq0) : bflo(pq0)) * 1.44269504089f, rev = fmul1(dt, (hi) ? bfhi(pq1) : bflo(pq1)) * 0.15915494309f; \
        z1 = zpow(a2, rev, 1.f); z8 = zpow(a2, rev, 8.f); z32 = zpow(a2, rev, 32.f); z256 = zpow(a2, rev, 256.f); } while (0)
        f2 z512, e4, e5; h2 f1, f2_, f4_, g0, g1, g2, g3;
#define KG_COLS_A(c, PEND) do { \
        const f2 z2 = cmul(z1, z1), e1 = cmulc(c, z256), z16 = cmul(z8, z8), z64 = cmul(z32, z32); z512 = cmul(z256, z256); \
        if (PEND) MM2(1, 0); \
        const f2 z4 = cmul(z2, z2), e2 = cmulc(c, z512), e3 = cmulc(e1, z512), z128 = cmul(z64, z64); \
        if (PEND) MM2(1, 1); \
        f1 = cvth(z1); f2_ = cvth(z2); f4_ = cvth(z4); g0 = cvth(c); g1 = cvth(e1); g2 = cvth(e2); g3 = cvth(e3); \
        if (PEND) MM2(1, 2); \
        e4 = cmulc(e2, z512); e5 = cmulc(e3, z512); \
        if (PEND) MM2(1, 3); \
        w8 = cvth(z8); w16 = cvth(z16); w24 = hmul(w8, w16); a32 = cvth(z32); a64 = cvth(z64); a96 = hmul(a32, a64); a128 = cvth(z128); } while (0)
#define KG_COLS_B() do { \
        const h2 f3 = hmul(f1, f2_), f6 = hmul(f2_, f4_), g04 = hmulc(g0, f4_); \
        PUT(0, g0); PUT(1, hmulc(g0, f1)); PUT(2, hmulc(g0, f2_)); PUT(3, hmulc(g0, f3)); \
        PUT(4, g04); PUT(5, hmulc(g04, f1)); PUT(6, hmulc(g0, f6)); PUT(7, hmulc(g04, f3)); \
        const f2 e6 = cmulc(e4, z512), e7 = cmulc(e5, z512); \
        PUT(8, g1); PUT(9, hmulc(g1, f2_)); PUT(10, hmulc(g1, f4_)); PUT(11, hmulc(g1, f6)); \
        const f2 e8 = cmulc(e6, z512); \
        PUT(12, g2); PUT(13, hmulc(g2, f4_)); PUT(14, g3); PUT(15, hmulc(g3, f4_)); \
        RD4(B0); \
        PUT(0, cvth(e4)); PUT(1, cvth(e5)); PUT(2, cvth(e6)); PUT(3, cvth(e7)); PUT(4, cvth(e8)); \
        RD4(B1); } while (0)
#define KG_QUAD(e, a) do { PUT(e, a); PUT((e) + 1, hmul(a, w8)); PUT((e) + 2, hmul(a, w16)); PUT((e) + 3, hmul(a, w24)); } while (0)
#define KG_ROWS_A() do { const h2 one = {1, 0}; PUT(0, one); PUT(1, w8); PUT(2, w16); PUT(3, w24); KG_QUAD(4, a32); KG_QUAD(8, a64); KG_QUAD(12, a96); RD4(A); } while (0)
#define KG_ROWS_B() do { const h2 a160 = hmul(a128, a32), a192 = hmul(a128, a64), a224 = hmul(a128, a96); \
        KG_QUAD(0, a128); MM2(0, 0); KG_QUAD(4, a160); MM2(0, 1); KG_QUAD(8, a192); MM2(0, 2); KG_QUAD(12, a224); MM2(0, 3); RD4(A); } while (0)
        KG_POW(0); KG_COLS_A(c0, 0); KG_COLS_B(); KG_ROWS_A();
        STAMP(3);
        SBAR();
        LN_PUBLISH();
        KG_ROWS_B();
        STAMPF(15); SBAR();
        { const u32 i0 = min(w4, 2u); SETM0(L_TAB + i0 * 2048); DMAO(lane * 16, (const char*)&CTAB + i0 * 2048, 0); DMAO(lane * 16, (const char*)&CTAB + i0 * 2048, 1024);
          dma16(lane * 16, (const char*)&CTAB + 6144 + i0 * 1024, L_SEL + i0 * 1024); }
        u4 red; UNDEF(red); GLD4(red, lane * 16, (const char*)&CTAB + 9216, "");
        W2_CH_IN(0);
        KG_POW(1);
        STAMPF(16); SBAR();
        W2_CH_IN(1);
        KG_COLS_A(c1, 1);
        asm volatile("s_waitcnt vmcnt(0)" : "+v"(red) :: "memory");
        STAMPF(17); SBAR();
        if (tid == 256) { const u2 ar = {tag, tk0}; GST2(0, ar, ws + WS_AR + b * 8, "sc1"); }
        W2_CH_OUT(0); W2_CH_IN(2);
        KG_COLS_B();
        STAMPF(18); SBAR();
        W2_CH_OUT(1); W2_CH_IN(3);
        KG_ROWS_A();
        STAMPF(19); SBAR();
        WAITV(4); W2_CH_OUT(2);
        UNDEF(arf); GLD2(arf, (tid & 255) * 8, ws + WS_AR, "sc1");
        KG_ROWS_B();
        MM2(1, 0); MM2(1, 1); MM2(1, 2); MM2(1, 3);
        { const u32 sw = L_SCR + w4 * 4352 + lane * 16;
          LDS(f4, sw) = self4(ch, dk[0], dk[2]) * isc; LDS(f4, sw + 1024) = self4(ch, dk[1], dk[3]) * isc; }
        asm volatile("s_waitcnt vmcnt(0)" : "+v"(arf) :: "memory");
#pragma unroll 1
        for (int sp = 0; __any(arf.x != tag) && sp < SPIN_MAX; ++sp) RLD2(arf, (tid & 255) * 8, ws + WS_AR);
        STAMP(12);
        SB(); BARRIER();
        UNDEF(stv); UNDEF(xg0); UNDEF(xg1);
        asm volatile("global_load_dword %0, %1, %2 sc1" : "+v"(stv) : "v"((tid & 127) * 4), "s"(ws + WS_STAT + rp * 512) : "memory");
        GLD4(xg0, xo, ws + WS_XG + dp * 32, "sc1"); GLD4(xg1, xo, ws + WS_XG + dp * 32 + 128 * 7168, "sc1");
        {
            const u32 a = L_SCR + (w4 ^ 1) * 4352 + lane * 16;
            const f4 s0 = self4(ch, dk[2], dk[0]) * isc + LDS(f4, a), s1 = self4(ch, dk[3], dk[1]) * isc + LDS(f4, a + 1024);
            const u32 o = red.x + ch * 16 * red.y + rg * 4352, p01 = pkbf(s0.x, s0.y), p23 = pkbf(s0.z, s0.w);
            LDS(u16, o) = (u16)p01; LDS(u16, o + red.y) = (u16)(p01 >> 16); LDS(u16, o + 2 * red.y) = (u16)p23; LDS(u16, o + 3 * red.y) = (u16)(p23 >> 16);
            LDS(u2, (ch ? red.w : red.z) + rg * 4352) = u2{pkbf(s1.x, s1.y), pkbf(s1.z, s1.w)};
            if (lane == 0) LDS(u4, L_KC + rg * 4352 + ch * 16) = u4{0, 0, 0, 0};
        }
        const u4 tixa = LDS(u4, L_TAB + (tid & 127) * 16), tixb = LDS(u4, L_TAB + 2048 + (tid & 127) * 16), tixc = LDS(u4, L_TAB + 4096 + (tid & 127) * 16);
        SEL_READ();
        asm volatile("s_waitcnt vmcnt(2)" : "+v"(stv) :: "memory");
        if (w4 < 2) LDS(u32, L_ST + (tid & 127) * 4) = stv;
        __builtin_amdgcn_s_setprio(0);
        W2_CH_OUT(3);
        STAMP(4);
        BARRIER();
        W2_4(w2b - 3072 + 6 * 28672, pb1);
        {
            u32 kc = L_KC + rg * 4352, td = L_TAB + rg * 5120 + (tid & 127) * 4; PIN(td);
            u32 v[10];
#pragma unroll
            for (int i = 0; i < 10; ++i) { const u32 tx = i < 4 ? tixa[i & 3] : i < 8 ? tixb[i & 3] : tixc[i & 3]; v[i] = (u32)LDS(u16, kc + (tx & 0xffff)) | ((u32)LDS(u16, kc + (tx >> 16)) << 16); }
#pragma unroll
            for (int i = 0; i < 4; ++i) LDS(u32, td + 512 * i) = v[i];
            if ((tid & 127) < 96) LDS(u32, td + 2048) = v[4];
            LDS(u32, td + 2432) = v[5]; LDS(u32, td + 2944) = v[6]; LDS(u32, td + 3456) = v[7]; LDS(u32, td + 3968) = v[8]; LDS(u32, td + 4480) = v[9];
        }
        {
            asm volatile("s_waitcnt vmcnt(4)" : "+v"(xg0), "+v"(xg1) :: "memory");
            u32 a = L_U + 480 + (tid & 1) * 4608 + ((tid & 255) >> 1) * 16; PIN(a);
            LDS(u4, a) = xg0; LDS(u4, a + 2048) = xg1;
            if ((tid & 255) < 60) LDS(u4, a - 480) = u4{0, 0, 0, 0};
        }
        STAMP(5);
        BARRIER();
        STAMP(6);
    }
    u4 hA0, hA1, hA2, hA3, hA4, hA5, hA6, hB0, hB1, hB2, hB3, hB4, hB5, hB6, hC0, hC1, hC2, hC3, hC4, hC5, hC6;
    const char* hrd = ws + WS_H + (P * 112 + wave * 14) * 2048;
    u32 l16 = lane * 16; PIN(l16);
#define H_TAKE_hA(n, NW) do { const char* p_ = hrd + ((n) & 1) * 14336 + ((n) >> 1) * 1024; u32 t0_, t1_, t2_, sp_; asm volatile( \
        "s_waitcnt vmcnt(" #NW ")\n\ts_mov_b32 %10, 0x10000\n" "1:\n\t" \
        "v_bitop3_b32 %8, v196, v200, %16 bitop3:0x7e\n\tv_bitop3_b32 %9, v204, v208, %16 bitop3:0x7e\n\t" \
        "v_or3_b32 %7, v221, %8, %9\n\t" \
        "v_bitop3_b32 %8, v212, v216, %16 bitop3:0x7e\n\tv_xor_b32 %9, %16, v220\n\tv_or3_b32 %7, %7, %8, %9\n\t" \
        "v_or3_b32 %8, v197, v201, v205\n\tv_or3_b32 %9, v209, v213, v217\n\tv_or3_b32 %7, %7, %8, %9\n\t" \
        "v_bitop3_b32 %8, v198, v202, %17 bitop3:0x7e\n\tv_bitop3_b32 %9, v206, v210, %17 bitop3:0x7e\n\t" \
        "v_or3_b32 %7, %7, %8, %9\n\tv_or_b32 %7, %7, v223\n\t" \
        "v_bitop3_b32 %8, v214, v218, %17 bitop3:0x7e\n\tv_xor_b32 %9, %17, v222\n\tv_or3_b32 %7, %7, %8, %9\n\t" \
        "v_or3_b32 %8, v199, v203, v207\n\tv_or3_b32 %9, v211, v215, v219\n\tv_or3_b32 %7, %7, %8, %9\n\t" \
        "v_and_b32 %7, 0x80008000, %7\n\tv_cmp_ne_u32 vcc, 0, %7\n\ts_cbranch_vccz 2f\n\t" \
        "global_load_dwordx4 v[196:199], %11, %12 sc1\n\t" \
        "global_load_dwordx4 v[200:203], %11, %12 offset:2048 sc1\n\t" \
        "global_load_dwordx4 v[204:207], %11, %13 sc1\n\t" \
        "global_load_dwordx4 v[208:211], %11, %13 offset:2048 sc1\n\t" \
        "global_load_dwordx4 v[212:215], %11, %14 sc1\n\t" \
        "global_load_dwordx4 v[216:219], %11, %14 offset:2048 sc1\n\t" \
        "global_load_dwordx4 v[220:223], %11, %15 sc1\n\t" \
        "s_sub_u32 %10, %10, 1\n\ts_waitcnt vmcnt(0)\n\ts_cbranch_scc0 1b\n" "2:" \
        : "={v[196:199]}"(hA0), "={v[200:203]}"(hA1), "={v[204:207]}"(hA2), "={v[208:211]}"(hA3), "={v[212:215]}"(hA4), "={v[216:219]}"(hA5), "={v[220:223]}"(hA6), "=&v"(t0_), "=&v"(t1_), "=&v"(t2_), "=&s"(sp_) \
        : "v"(l16), "s"(p_), "s"(p_ + 4096), "s"(p_ + 8192), "s"(p_ + 12288), "s"(hm0), "s"(hm2) : "vcc", "scc", "memory"); } while (0)
#define H_TAKE_hB(n, NW) do { const char* p_ = hrd + ((n) & 1) * 14336 + ((n) >> 1) * 1024; u32 t0_, t1_, t2_, sp_; asm volatile( \
        "s_waitcnt vmcnt(" #NW ")\n\ts_mov_b32 %10, 0x10000\n" "1:\n\t" \
        "v_bitop3_b32 %8, v224, v228, %16 bitop3:0x7e\n\tv_bitop3_b32 %9, v232, v236, %16 bitop3:0x7e\n\t" \
        "v_or3_b32 %7, v249, %8, %9\n\t" \
        "v_bitop3_b32 %8, v240, v244, %16 bitop3:0x7e\n\tv_xor_b32 %9, %16, v248\n\tv_or3_b32 %7, %7, %8, %9\n\t" \
        "v_or3_b32 %8, v225, v229, v233\n\tv_or3_b32 %9, v237, v241, v245\n\tv_or3_b32 %7, %7, %8, %9\n\t" \
        "v_bitop3_b32 %8, v226, v230, %17 bitop3:0x7e\n\tv_bitop3_b32 %9, v234, v238, %17 bitop3:0x7e\n\t" \
        "v_or3_b32 %7, %7, %8, %9\n\tv_or_b32 %7, %7, v251\n\t" \
        "v_bitop3_b32 %8, v242, v246, %17 bitop3:0x7e\n\tv_xor_b32 %9, %17, v250\n\tv_or3_b32 %7, %7, %8, %9\n\t" \
        "v_or3_b32 %8, v227, v231, v235\n\tv_or3_b32 %9, v239, v243, v247\n\tv_or3_b32 %7, %7, %8, %9\n\t" \
        "v_and_b32 %7, 0x80008000, %7\n\tv_cmp_ne_u32 vcc, 0, %7\n\ts_cbranch_vccz 2f\n\t" \
        "global_load_dwordx4 v[224:227], %11, %12 sc1\n\t" \
        "global_load_dwordx4 v[228:231], %11, %12 offset:2048 sc1\n\t" \
        "global_load_dwordx4 v[232:235], %11, %13 sc1\n\t" \
        "global_load_dwordx4 v[236:239], %11, %13 offset:2048 sc1\n\t" \
        "global_load_dwordx4 v[240:243], %11, %14 sc1\n\t" \
        "global_load_dwordx4 v[244:247], %11, %14 offset:2048 sc1\n\t" \
        "global_load_dwordx4 v[248:251], %11, %15 sc1\n\t" \
        "s_sub_u32 %10, %10, 1\n\ts_waitcnt vmcnt(0)\n\ts_cbranch_scc0 1b\n" "2:" \
        : "={v[224:227]}"(hB0), "={v[228:231]}"(hB1), "={v[232:235]}"(hB2), "={v[236:239]}"(hB3), "={v[240:243]}"(hB4), "={v[244:247]}"(hB5), "={v[248:251]}"(hB6), "=&v"(t0_), "=&v"(t1_), "=&v"(t2_), "=&s"(sp_) \
        : "v"(l16), "s"(p_), "s"(p_ + 4096), "s"(p_ + 8192), "s"(p_ + 12288), "s"(hm0), "s"(hm2) : "vcc", "scc", "memory"); } while (0)
#define H_TAKE_hC(n, NW) do { const char* p_ = hrd + ((n) & 1) * 14336 + ((n) >> 1) * 1024; u32 t0_, t1_, t2_, sp_; asm volatile( \
        "s_waitcnt vmcnt(" #NW ")\n\ts_mov_b32 %10, 0x10000\n" "1:\n\t" \
        "v_bitop3_b32 %8, v168, v172, %16 bitop3:0x7e\n\tv_bitop3_b32 %9, v176, v180, %16 bitop3:0x7e\n\t" \
        "v_or3_b32 %7, v193, %8, %9\n\t" \
        "v_bitop3_b32 %8, v184, v188, %16 bitop3:0x7e\n\tv_xor_b32 %9, %16, v192\n\tv_or3_b32 %7, %7, %8, %9\n\t" \
        "v_or3_b32 %8, v169, v173, v177\n\tv_or3_b32 %9, v181, v185, v189\n\tv_or3_b32 %7, %7, %8, %9\n\t" \
        "v_bitop3_b32 %8, v170, v174, %17 bitop3:0x7e\n\tv_bitop3_b32 %9, v178, v182, %17 bitop3:0x7e\n\t" \
        "v_or3_b32 %7, %7, %8, %9\n\tv_or_b32 %7, %7, v195\n\t" \
        "v_bitop3_b32 %8, v186, v190, %17 bitop3:0x7e\n\tv_xor_b32 %9, %17, v194\n\tv_or3_b32 %7, %7, %8, %9\n\t" \
        "v_or3_b32 %8, v171, v175, v179\n\tv_or3_b32 %9, v183, v187, v191\n\tv_or3_b32 %7, %7, %8, %9\n\t" \
        "v_and_b32 %7, 0x80008000, %7\n\tv_cmp_ne_u32 vcc, 0, %7\n\ts_cbranch_vccz 2f\n\t" \
        "global_load_dwordx4 v[168:171], %11, %12 sc1\n\t" \
        "global_load_dwordx4 v[172:175], %11, %12 offset:2048 sc1\n\t" \
        "global_load_dwordx4 v[176:179], %11, %13 sc1\n\t" \
        "global_load_dwordx4 v[180:183], %11, %13 offset:2048 sc1\n\t" \
        "global_load_dwordx4 v[184:187], %11, %14 sc1\n\t" \
        "global_load_dwordx4 v[188:191], %11, %14 offset:2048 sc1\n\t" \
        "global_load_dwordx4 v[192:195], %11, %15 sc1\n\t" \
        "s_sub_u32 %10, %10, 1\n\ts_waitcnt vmcnt(0)\n\ts_cbranch_scc0 1b\n" "2:" \
        : "={v[168:171]}"(hC0), "={v[172:175]}"(hC1), "={v[176:179]}"(hC2), "={v[180:183]}"(hC3), "={v[184:187]}"(hC4), "={v[188:191]}"(hC5), "={v[192:195]}"(hC6), "=&v"(t0_), "=&v"(t1_), "=&v"(t2_), "=&s"(sp_) \
        : "v"(l16), "s"(p_), "s"(p_ + 4096), "s"(p_ + 8192), "s"(p_ + 12288), "s"(hm0), "s"(hm2) : "vcc", "scc", "memory"); } while (0)
    {
        const int kh = hi4, ts = ch;
        dma16(r8 * 896 + 16 * (p8 ^ (wave & 6)), (const char*)x + (P * 64 + wave * 8) * 896 + ct * 128, L_XE + wave * 1024);
        STAMP(13);
        if (isF) { if (ch) conv_far<1, 2>(cacc, cv_ub, cv_la, cv_p3, sel3); else conv_far<0, 2>(cacc, cv_ub, cv_la, cv_p3, sel3); }
        else { if (ch) conv_far<1, 0>(cacc, cv_ub, cv_la, cv_p3, sel3); else conv_far<0, 0>(cacc, cv_ub, cv_la, cv_p3, sel3); }
        STAMP(14);
        {
            u32 u = cv_ub - 192 - 256 * hi4, l = cv_la + 416 - 256 * hi4, q = cv_q2 + 256 * hi4; PIN(u); PIN(l); PIN(q);
            if (ch) conv_steps<1>(cacc, isF, u, l, q, cv_la, sel2, vq0, vq1, vq2, vq3, w2b + 16 * 7168 - 3072, pb0, l16, hrd);
            else conv_steps<0>(cacc, isF, u, l, q, cv_la, sel2, vq0, vq1, vq2, vq3, w2b + 16 * 7168 - 3072, pb0, l16, hrd);
        }
        STAMP(7);
        WAITV(15);
        BARRIER();
        u32 ra = lw + (isF ? 0 : 65536) + 16384 + lane * 16; PIN(ra);
        LDS(f4, ra) = self4(kh, cacc[0], cacc[2]);
        LDS(f4, ra + 1024) = self4(kh, cacc[1], cacc[3]);
        H_LDB(0, hrd + 14336); H_LDB(1, hrd + 14336); H_LDB(2, hrd + 14336); H_LDB(3, hrd + 14336); H_LDB(4, hrd + 14336); H_LDB(5, hrd + 14336); H_LDB(6, hrd + 14336);
        ra = pb1 + 16384 + lane * 16; PIN(ra);
        BARRIER();
        const float dk = rg ? bfhi(dsk2) : bflo(dsk2);
#pragma unroll
        for (int i = 0; i < 2; ++i) {
            const int tau = kh ? (ts ? 5 + i : 4 + 3 * i) : (ts ? 1 + i : 3 * i);
            const f4 y = self4(kh, cacc[2 + i], cacc[i]) + LDS(f4, ra + i * 1024);
            const u2 uu = LDS(u2, L_U + rg * 4608 + 480 + (256 * tau + 16 * fl + 4 * g) * 2);
            const u32 y01 = pkbf(y.x, y.y), y23 = pkbf(y.z, y.w);
            const u32 s01 = pkbf(bflo(uu.x) * dk, bfhi(uu.x) * dk), s23 = pkbf(bflo(uu.y) * dk, bfhi(uu.y) * dk);
            const u4 o = {pkbf(bflo(y01) + bflo(s01), bfhi(y01) + bfhi(s01)), tag, pkbf(bflo(y23) + bflo(s23), bfhi(y23) + bfhi(s23)), tag};
            GST4((fl >> 2) * 114688 + ((fl & 3) + 4 * g) * 16, o, ws + WS_YG + (4 * tau * 448 + 2 * dp + rg) * 256, "sc1");
        }
    }
    STAMP(8);
    u32 pw0 = pb0 + lane * 16, pw1 = (pb1 + lane * 16) ^ 128; PIN(pw0); PIN(pw1);
#define H_MM1(h, j, o) do { a0 = mfma32(h##j, Bw[(o) + j][0], a0); a1 = mfma32(h##j, Bw[(o) + j][1], a1); } while (0)
#define H_MMA(h, o) do { H_MM1(h, 0, o); H_MM1(h, 1, o); H_MM1(h, 2, o); H_MM1(h, 3, o); H_MM1(h, 4, o); H_MM1(h, 5, o); H_MM1(h, 6, o); } while (0)
#define W2_SGN(j) do { Bw[j][0] ^= u4{hm0, 0, hm2, 0}; Bw[j][1] ^= u4{hm0, 0, hm2, 0}; PIN(Bw[j][0]); PIN(Bw[j][1]); } while (0)
#define H_MMS(h, o) do { H_MM1(h, 0, o); W2_SGN((o) + 1); SB(); H_MM1(h, 1, o); W2_SGN((o) + 2); SB(); H_MM1(h, 2, o); W2_SGN((o) + 3); SB(); H_MM1(h, 3, o); W2_SGN((o) + 4); SB(); \
        H_MM1(h, 4, o); W2_SGN((o) + 5); SB(); H_MM1(h, 5, o); W2_SGN((o) + 6); SB(); H_MM1(h, 6, o); } while (0)
#define H_ZERO() _Pragma("unroll") for (int i = 0; i < 16; ++i) { a0[i] = 0; a1[i] = 0; }
#define H_PUT(pw) _Pragma("unroll") for (int q = 0; q < 4; ++q) { const u32 a_ = ((pw) ^ (q >> 1) * 64) + q * 1024; LDS(f4, a_) = quad(a0, q); LDS(f4, a_ + 16384) = quad(a1, q); }
    f16v a0, a1;
    H_ZERO();
    __builtin_amdgcn_s_setprio(3);
    W2_SGN(0); W2_SGN(7);
    H_TAKE_hA(0, 9);
    {
        const char* p3 = hrd + 1024;
        H_MM1(hA, 0, 0); W2_SGN(1); W2_RD(8, 0, 0); SB(); H_LDC(0, p3); H_MM1(hA, 1, 0); W2_SGN(2); W2_RD(9, 0, 1); SB(); H_LDC(1, p3);
        H_MM1(hA, 2, 0); W2_SGN(3); W2_RD(10, 0, 2); SB(); H_LDC(2, p3); H_MM1(hA, 3, 0); W2_SGN(4); W2_RD(11, 0, 3); SB(); H_LDC(3, p3);
        H_MM1(hA, 4, 0); W2_SGN(5); W2_RD(12, 1, 0); SB(); H_LDC(4, p3); H_MM1(hA, 5, 0); W2_SGN(6); W2_RD(13, 1, 1); SB(); H_LDC(5, p3);
        H_MM1(hA, 6, 0); SB(); H_LDC(6, p3);
    }
    STAMP(9);
    __builtin_amdgcn_s_setprio(2);
    H_TAKE_hB(1, 9);
    {
        const char* p3 = hrd + 14336 + 1024;
        H_MM1(hB, 0, 7); W2_SGN(8); SB(); H_LDA(0, p3); H_MM1(hB, 1, 7); W2_SGN(9); SB(); H_LDA(1, p3); H_MM1(hB, 2, 7); W2_SGN(10); SB(); H_LDA(2, p3);
        H_MM1(hB, 3, 7); W2_SGN(11); SB(); H_LDA(3, p3); H_MM1(hB, 4, 7); W2_SGN(12); SB(); H_LDA(4, p3); H_MM1(hB, 5, 7); W2_SGN(13); SB(); H_LDA(5, p3);
        H_MM1(hB, 6, 7); SB(); H_LDA(6, p3);
    }
    H_PUT(pw0);
    H_ZERO();
    __builtin_amdgcn_s_setprio(1);
    H_TAKE_hC(2, 7); H_MMA(hC, 0);
    const u32 al = lane & 3, dc = 16 * rg + (lane >> 2);
    const u32 yo = (32 * hi4 + dc) * 256 + 16 * al + 128 * ch;
    const char* yb = ws + WS_YG + (P * 448 + 64 * ct) * 256;
    u4 yv0, yv1;
    asm volatile("global_load_dwordx4 v[168:171], %0, %1 sc1\n\tglobal_load_dwordx4 v[172:175], %0, %1 offset:64 sc1" :: "v"(yo), "s"(yb)
                 : "v168", "v169", "v170", "v171", "v172", "v173", "v174", "v175", "memory");
    u32 ax = L_XE + (16 * al + 8 * ch) * 128 + ((64 * hi4 + 2 * dc) ^ (32 * al)); PIN(ax);
    const u32 o_ = L_RING + hi4 * 16384 + (2 * (al & 1) + ch) * 1024 + (dc ^ (4 * al)) * 16;
    u32 aF = o_ + (al & 2 ? 65536 : 32768), aS = o_ + (al & 2 ? 0 : 98304); PIN(aF); PIN(aS);
    u32 xe[8];
#pragma unroll
    for (int i = 0; i < 8; ++i) xe[i] = LDS(u16, ax + i * 128);
    __builtin_amdgcn_s_setprio(0);
    H_TAKE_hA(3, 2);
    a0 = mfma32(hA0, Bw[7][0], a0); a0 = mfma32(hA1, Bw[8][0], a0); a0 = mfma32(hA2, Bw[9][0], a0); a0 = mfma32(hA3, Bw[10][0], a0);
    a0 = mfma32(hA4, Bw[11][0], a0); a0 = mfma32(hA5, Bw[12][0], a0); a0 = mfma32(hA6, Bw[13][0], a0);
    SB();
    a1 = mfma32(hA0, Bw[7][1], a1); SB();
    {
        u32 t0_, sp_;
        asm volatile("s_waitcnt vmcnt(0)\n\ts_mov_b32 %3, 0x10000\n" "1:\n\t"
                     "v_bitop3_b32 %2, v169, v171, %6 bitop3:0x7e\n\tv_bitop3_b32 %2, %2, v173, %6 bitop3:0xf6\n\tv_bitop3_b32 %2, %2, v175, %6 bitop3:0xf6\n\t"
                     "v_cmp_ne_u32 vcc, 0, %2\n\ts_cbranch_vccz 2f\n\t"
                     "global_load_dwordx4 v[168:171], %4, %5 sc1\n\tglobal_load_dwordx4 v[172:175], %4, %5 offset:64 sc1\n\t"
                     "s_sub_u32 %3, %3, 1\n\ts_waitcnt vmcnt(0)\n\ts_cbranch_scc0 1b\n" "2:"
                     : "={v[168:171]}"(yv0), "={v[172:175]}"(yv1), "=&v"(t0_), "=&s"(sp_) : "v"(yo), "s"(yb), "s"(tag) : "vcc", "scc", "memory");
    }
    u32 pre[4];
    a1 = mfma32(hA1, Bw[8][1], a1); pre[0] = pkbf(bflo(xe[0]) + bflo(yv0.x), bflo(xe[1]) + bfhi(yv0.x)); PIN(pre[0]); LDS(f4, pw1) = quad(a0, 0); SB();
    a1 = mfma32(hA2, Bw[9][1], a1); pre[1] = pkbf(bflo(xe[2]) + bflo(yv0.z), bflo(xe[3]) + bfhi(yv0.z)); PIN(pre[1]); LDS(f4, pw1 + 1024) = quad(a0, 1); SB();
    a1 = mfma32(hA3, Bw[10][1], a1); pre[2] = pkbf(bflo(xe[4]) + bflo(yv1.x), bflo(xe[5]) + bfhi(yv1.x)); PIN(pre[2]); LDS(f4, (pw1 ^ 64) + 2048) = quad(a0, 2); SB();
    a1 = mfma32(hA4, Bw[11][1], a1); pre[3] = pkbf(bflo(xe[6]) + bflo(yv1.z), bflo(xe[7]) + bfhi(yv1.z)); PIN(pre[3]); LDS(f4, (pw1 ^ 64) + 3072) = quad(a0, 3); SB();
    a1 = mfma32(hA5, Bw[12][1], a1); a1 = mfma32(hA6, Bw[13][1], a1);
#pragma unroll
    for (int q = 0; q < 4; ++q) LDS(f4, (pw1 ^ (q >> 1) * 64) + q * 1024 + 16384) = quad(a1, q);
    BARRIER();
    STAMP(10);
#pragma unroll
    for (int i = 0; i < 2; ++i) {
        const f4 r = ((LDS(f4, aF + i * 512) + LDS(f4, aF + i * 512 + 4096)) + (LDS(f4, aF + i * 512 + 8192) + LDS(f4, aF + i * 512 + 12288))) +
                     ((LDS(f4, aS + i * 512) + LDS(f4, aS + i * 512 + 4096)) + (LDS(f4, aS + i * 512 + 8192) + LDS(f4, aS + i * 512 + 12288)));
        const u32 a01 = pre[2 * i], a23 = pre[2 * i + 1], f01 = pkbf(r.x, r.y), f23 = pkbf(r.z, r.w);
        const u32 o01 = pkbf(bflo(a01) + bflo(f01), bfhi(a01) + bfhi(f01)), o23 = pkbf(bflo(a23) + bflo(f23), bfhi(a23) + bfhi(f23));
        LDS(u16, ax + i * 512) = (u16)o01; LDS(u16, ax + i * 512 + 128) = (u16)(o01 >> 16); LDS(u16, ax + i * 512 + 256) = (u16)o23; LDS(u16, ax + i * 512 + 384) = (u16)(o23 >> 16);
    }
    BARRIER();
    { const u4 ov = LDS(u4, L_XE + tid * 16); GST4(r8 * 896 + 16 * (p8 ^ (wave & 6)), ov, (char*)out + (P * 64 + wave * 8) * 896 + ct * 128, "sc1"); }
tk1 = (u32)__builtin_amdgcn_s_memrealtime(); __builtin_amdgcn_s_waitcnt(0xC07F);    STAMP(11); STAMPC(23); if (tid == 0) { const u2 dn = {tag, tk1}; GST2(0, dn, ws + WS_AR + 4096 + b * 8, "sc1"); }
    STAMP_FLUSH(24);
}

__global__ void kB(char* ws) {
    int t = threadIdx.x;
    const u32 ta = ((u32*)(ws + WS_AR))[1];
    int st = (int)(((u32*)(ws + WS_AR))[t * 2 + 1] - ta), en = t < 224 ? (int)(((u32*)(ws + WS_AR + 4096))[t * 2 + 1] - ta) : -100000;
    __shared__ int smn, smx; if (t == 0) { smn = 1 << 30; smx = -100000; } __syncthreads();
    atomicMin(&smn, st); atomicMax(&smx, en); __syncthreads();
    u64 e_ = __builtin_amdgcn_s_memrealtime() + (u64)(smx - smn) * 0; while (__builtin_amdgcn_s_memrealtime() < e_) __builtin_amdgcn_s_sleep(8);
}

extern "C" void kernel_launch(void* const* d_in, const int* in_sizes, int n_in, void* d_out, int out_size, void* d_ws, size_t ws_size,
                              hipStream_t stream) {
    (void)hipFuncSetAttribute((const void*)s4ffn_kernel, hipFuncAttributeMaxDynamicSharedMemorySize, LDS_BYTES);
    s4ffn_kernel<<<256, 512, LDS_BYTES, stream>>>((const u16*)d_in[0], (const u16*)d_in[1], (const u16*)d_in[2], (const u16*)d_in[3],
        (const u16*)d_in[4], (const u16*)d_in[5], (const u16*)d_in[6], (const u16*)d_in[7], (const u16*)d_in[8],
        (const u16*)d_in[10], (const u16*)d_in[11], (u16*)d_out, (char*)d_ws);
    kB<<<1, 256, 0, stream>>>((char*)d_ws);
}
```

```cpp
#include <hip/hip_runtime.h>
#include <stdint.h>

typedef unsigned short u16;
typedef unsigned int u32;
typedef unsigned long long u64;
typedef __attribute__((ext_vector_type(8))) __bf16 bf8;
typedef __attribute__((ext_vector_type(2))) __bf16 bf2;
typedef __attribute__((ext_vector_type(4))) short s4;
typedef __attribute__((ext_vector_type(4))) float f4;
typedef __attribute__((ext_vector_type(16))) float f16v;
typedef __attribute__((ext_vector_type(2))) float f2;
typedef __attribute__((ext_vector_type(4))) u32 u4;
typedef __attribute__((ext_vector_type(2))) u32 u2;
#define LAS __attribute__((address_space(3)))
#define DI __device__ __forceinline__
#define LDS(T, off) (*(LAS T*)(uintptr_t)(u32)(off))

constexpr u32 WS_EPOCH = 0;
constexpr u32 WS_STAT = 4096;
constexpr u32 WS_AR = WS_STAT + 16384;
constexpr u32 WS_XG = 65536;
constexpr u32 WS_YG = WS_XG + 3670016;
constexpr u32 WS_H = WS_YG + 3670016;
constexpr u32 WS_STAMP = 32u << 20;
constexpr u32 L_TAB = 0;
constexpr u32 L_CS = 10240;
constexpr u32 L_ST = 10752;
constexpr u32 L_SEL = 12288;
constexpr u32 L_SCR = 15360;
constexpr u32 L_KC = L_SCR + 2048;
constexpr u32 L_XE = L_SCR;
constexpr u32 L_U = 23552;
constexpr u32 L_RING = 32768;
constexpr u32 L_XT = L_RING + 98304;
constexpr u32 LDS_BYTES = 163840;
constexpr int SPIN_MAX = 1 << 16;
constexpr u32 PF_LINES = 188;

#define STAMP(i)
#define STAMPC(i)
#define STAMPF(i)
#define FSTAMP(s)
#define ACC_BEGIN()
#define ACC_END(i)
#define STAMP_DECL
#define STAMP_FLUSH(n)

DI float bflo(u32 v) { return __uint_as_float(v << 16); }
DI float bfhi(u32 v) { return __uint_as_float(v & 0xffff0000u); }
DI u32 pkbf(float a, float b) { f2 v = {a, b}; return __builtin_bit_cast(u32, __builtin_convertvector(v, bf2)); }
DI float rbf(float a) { return bflo(pkbf(a, a)); }
DI f4 mfma(u4 a, u4 b, f4 c) { return __builtin_amdgcn_mfma_f32_16x16x32_bf16(__builtin_bit_cast(bf8, a), __builtin_bit_cast(bf8, b), c, 0, 0, 0); }
DI f16v mfma32(u4 a, u4 b, f16v c) { return __builtin_amdgcn_mfma_f32_32x32x16_bf16(__builtin_bit_cast(bf8, a), __builtin_bit_cast(bf8, b), c, 0, 0, 0); }
DI u2 trread(u32 addr) { return __builtin_bit_cast(u2, __builtin_amdgcn_ds_read_tr16_b64_v4i16((LAS s4*)(uintptr_t)addr)); }
DI u4 trread2(u32 a, u32 b) { const u2 l = trread(a), h = trread(b); return u4{l.x, l.y, h.x, h.y}; }
template <int C> DI float dpp(float x) { return __builtin_bit_cast(float, __builtin_amdgcn_mov_dpp(__builtin_bit_cast(int, x), C, 0xf, 0xf, true)); }
DI float rsum(float x) { x += dpp<0xB1>(x); x += dpp<0x4E>(x); x += dpp<0x141>(x); x += dpp<0x140>(x); return x; }
DI float rmax(float x) { x = fmaxf(x, dpp<0xB1>(x)); x = fmaxf(x, dpp<0x4E>(x)); x = fmaxf(x, dpp<0x141>(x)); return fmaxf(x, dpp<0x140>(x)); }
DI float sw32(float a, float b) { auto s = __builtin_amdgcn_permlane32_swap(__float_as_uint(a), __float_as_uint(b), false, false); return __uint_as_float(s[0]) + __uint_as_float(s[1]); }
DI float sw16(float a, float b) { auto s = __builtin_amdgcn_permlane16_swap(__float_as_uint(a), __float_as_uint(b), false, false); return __uint_as_float(s[0]) + __uint_as_float(s[1]); }
DI float dot2(u32 a, u32 b, float c) { return __builtin_amdgcn_fdot2_f32_bf16(__builtin_bit_cast(bf2, a), __builtin_bit_cast(bf2, b), c, false); }
DI f4 self4(int c, f4 a, f4 b) { return c ? a : b; }
DI f2 cmul(f2 a, f2 b) { float x, y;
    asm("v_mul_f32 %0, %2, %4\n\tv_mul_f32 %1, %3, %4\n\tv_fma_f32 %0, -%3, %5, %0\n\tv_fmac_f32 %1, %2, %5" : "=&v"(x), "=&v"(y) : "v"(a.x), "v"(a.y), "v"(b.x), "v"(b.y));
    return f2{x, y}; }
DI f2 cmulc(f2 a, f2 b) { float x, y;
    asm("v_mul_f32 %0, %2, %4\n\tv_mul_f32 %1, %3, %4\n\tv_fmac_f32 %0, %3, %5\n\tv_fma_f32 %1, -%2, %5, %1" : "=&v"(x), "=&v"(y) : "v"(a.x), "v"(a.y), "v"(b.x), "v"(b.y));
    return f2{x, y}; }
DI float fmul1(float a, float b) { float r; asm("v_mul_f32 %0, %1, %2" : "=v"(r) : "v"(a), "v"(b)); return r; }
typedef __attribute__((ext_vector_type(2))) _Float16 h2;
typedef __attribute__((ext_vector_type(8))) _Float16 h8;
DI h2 cvth(f2 a) { return __builtin_convertvector(a, h2); }
DI h2 hmul(h2 a, h2 b) { const h2 bx = {b.x, b.x}, by = {-b.y, b.y}; return __builtin_elementwise_fma(__builtin_shufflevector(a, a, 1, 0), by, a * bx); }
DI h2 hmulc(h2 a, h2 b) { const h2 bx = {b.x, b.x}, by = {b.y, -b.y}; return __builtin_elementwise_fma(__builtin_shufflevector(a, a, 1, 0), by, a * bx); }
DI f4 mfmah(u4 a, u4 b, f4 c) { return __builtin_amdgcn_mfma_f32_16x16x32_f16(__builtin_bit_cast(h8, a), __builtin_bit_cast(h8, b), c, 0, 0, 0); }
DI u32 relu2(u32 v) { typedef __attribute__((ext_vector_type(2))) short s2; const s2 z = {0, 0}; return __builtin_bit_cast(u32, __builtin_elementwise_max(__builtin_bit_cast(s2, v), z)); }
DI f2 zpow(float a2, float rev, float p) {
    float e = __builtin_amdgcn_exp2f(a2 * p), r = __builtin_amdgcn_fractf(rev * p);
    float x, y;
    asm("s_nop 0\n\tv_mul_f32 %0, %2, %3\n\tv_mul_f32 %1, %2, %4" : "=&v"(x), "=&v"(y) : "v"(e), "v"(__builtin_amdgcn_cosf(r)), "v"(__builtin_amdgcn_sinf(r)));
    return f2{x, y};
}
DI f4 quad(const f16v& v, int q) { return f4{v[4 * q], v[4 * q + 1], v[4 * q + 2], v[4 * q + 3]}; }

#define SB() __builtin_amdgcn_sched_barrier(0)
#define BARRIER() asm volatile("s_waitcnt lgkmcnt(0)\n\ts_barrier" ::: "memory")
#define WAITV(n) asm volatile("s_waitcnt vmcnt(" #n ")" ::: "memory")
DI void dma16(u32 voff, const void* base, u32 lds) {
    asm volatile("s_mov_b32 m0, %2\n\ts_nop 0\n\tglobal_load_lds_dwordx4 %0, %1" :: "v"(voff), "s"(base), "s"(lds) : "memory");
}
#define SETM0(lds) asm volatile("s_mov_b32 m0, %0\n\ts_nop 0" :: "s"(lds) : "memory")
#define DMAO(voff, base, off) asm volatile("global_load_lds_dwordx4 %0, %1 offset:" #off :: "v"(voff), "s"(base) : "memory")
#define GLD4(dst, voff, base, mod) asm volatile("global_load_dwordx4 %0, %1, %2 " mod : "+v"(dst) : "v"(voff), "s"(base) : "memory")
#define GLD2(dst, voff, base, mod) asm volatile("global_load_dwordx2 %0, %1, %2 " mod : "+v"(dst) : "v"(voff), "s"(base) : "memory")
#define GLD1(dst, voff, base) asm volatile("global_load_dword %0, %1, %2" : "+v"(dst) : "v"(voff), "s"(base) : "memory")
#define GST4(voff, val, base, mod) asm volatile("global_store_dwordx4 %0, %1, %2 " mod "\n\ts_nop 1" :: "v"(voff), "v"(val), "s"(base) : "memory")
#define GST2(voff, val, base, mod) asm volatile("global_store_dwordx2 %0, %1, %2 " mod :: "v"(voff), "v"(val), "s"(base) : "memory")
#define RLD2(dst, voff, base) asm volatile("global_load_dwordx2 %0, %1, %2 sc1\n\ts_waitcnt vmcnt(0)" : "=&v"(dst) : "v"(voff), "s"(base) : "memory")
#define RLD4(dst, voff, base) asm volatile("global_load_dwordx4 %0, %1, %2 sc1\n\ts_waitcnt vmcnt(0)" : "=&v"(dst) : "v"(voff), "s"(base) : "memory")
#define PREFETCH(voff, base) asm volatile("global_load_dword v255, %0, %1" :: "v"(voff), "s"(base) : "v255", "memory")
#define PIN(x) asm volatile("" : "+v"(x))
#define UNDEF(x) asm volatile("" : "=v"(x))

struct ConstTab { u32 tix[3][128][4]; u32 sel[3][64][4]; u32 red[64][4]; };
constexpr int cidx(int l) {
    if (l < 0) return 16 + l;
    const int s = (l >= 256) + (l >= 512) + (l >= 1024);
    return 16 + 128 * s + (s ? (l + (1 << (s - 1)) - 1 + ((l >> s) & 1)) >> s : l);
}
constexpr ConstTab make_tab() {
    ConstTab c{};
    for (int t = 0; t < 128; ++t) {
        u32 v[12] = {};
        for (int i = 0; i < 5; ++i) {
            const int u = t + 128 * i < 607 ? t + 128 * i : 607, pi = u >= 304, ip = u - 304 * pi, top = (ip < 24 ? 1056 : 592) - pi - 2 * ip;
            v[i] = (u32)cidx(top > 1028 ? -1 : top) * 2 | (u32)cidx(top - 1 > 1028 ? -1 : top - 1) << 17;
        }
        v[5] = (404 + (t >> 1) + 2 * (t & 1)) * 0x20002u + 0x20000u;
        v[6] = v[5] + 64 * 0x20002u;
        for (int i = 0; i < 3; ++i) {
            const int u = t + 128 * i < 303 ? t + 128 * i : 303, q = u & 3, m = 2 * (52 + (u >> 2)) + (q < 2 ? 2 - 4 * q : 9 - 4 * q);
            v[7 + i] = (u32)cidx(m >= 129 ? 8 * m - (m == 256) : -1) * 2 | (u32)cidx(m - 2 >= 129 ? 8 * (m - 2) : -1) << 17;
        }
        for (int i = 0; i < 12; ++i) c.tix[i >> 2][t][i & 3] = v[i];
    }
    for (int l = 0; l < 64; ++l) {
        const int fl = l & 15, g = l >> 4, x2 = fl & 7;
        const int m2 = x2 < 2 ? -2 : x2 < 5 ? -1 : 0;
        for (int q = 0; q < 4; ++q)
            for (int h = 0; h < 2; ++h) {
                const int e2 = x2 - 2 * q - h, e = 8 * g + 2 * q + h - 8 - fl;
                c.sel[0][l][q] |= (u32)(((e2 >= -5) + (e2 >= -2) + (e2 >= 3) + (e2 >= 6) - 2 - m2) * 0x0202 + 0x0100) << (16 * h);
                c.sel[1][l][q] |= (u32)(e >= -3 && e <= 3 ? 0x3f80 : e >= -4 && e <= 4 ? 0x3f81 : 0) << (16 * h);
            }
        const int mul = fl < 8 ? 8 : fl < 12 ? 4 : 2, base = fl < 8 ? fl : fl < 12 ? 248 + fl : 384 + (fl & 1) + 64 * ((fl - 12) >> 1);
        c.red[l][0] = L_KC + (16 + base + mul * 4 * g) * 2; c.red[l][1] = 2 * mul;
        c.red[l][2] = L_KC + (fl < 4 ? (528 + 32 * fl + 4 * g) * 2 : fl == 4 && g == 0 ? 1312 : 1528);
        c.red[l][3] = L_KC + (fl < 4 ? (544 + 32 * fl + 4 * g) * 2 : 1528);
        c.sel[2][l][0] = L_U + 2 * (224 + 16 * fl + 8 * g);
        c.sel[2][l][1] = L_TAB + 4 * ((fl & 1) * 304 + ((16 - fl + 8 * g) >> 1));
        c.sel[2][l][2] = L_TAB + 2432 + 8 * (2 * (2 + (fl >> 3) - g) + m2 - 4);
        c.sel[2][l][3] = L_TAB + 3456 + 16 * (12 + fl - 4 * g);
    }
    return c;
}
static __device__ const ConstTab CTAB __attribute__((aligned(128))) = make_tab();

#define H_LDA(j, p) do { \
        if ((j) == 0) asm volatile("global_load_dwordx4 v[196:199], %0, %1 offset:0 sc1" :: "v"(l16), "s"((p) + 0) : "v196", "v197", "v198", "v199", "memory"); \
        if ((j) == 1) asm volatile("global_load_dwordx4 v[200:203], %0, %1 offset:2048 sc1" :: "v"(l16), "s"((p) + 0) : "v200", "v201", "v202", "v203", "memory"); \
        if ((j) == 2) asm volatile("global_load_dwordx4 v[204:207], %0, %1 offset:0 sc1" :: "v"(l16), "s"((p) + 4096) : "v204", "v205", "v206", "v207", "memory"); \
        if ((j) == 3) asm volatile("global_load_dwordx4 v[208:211], %0, %1 offset:2048 sc1" :: "v"(l16), "s"((p) + 4096) : "v208", "v209", "v210", "v211", "memory"); \
        if ((j) == 4) asm volatile("global_load_dwordx4 v[212:215], %0, %1 offset:0 sc1" :: "v"(l16), "s"((p) + 8192) : "v212", "v213", "v214", "v215", "memory"); \
        if ((j) == 5) asm volatile("global_load_dwordx4 v[216:219], %0, %1 offset:2048 sc1" :: "v"(l16), "s"((p) + 8192) : "v216", "v217", "v218", "v219", "memory"); \
        if ((j) == 6) asm volatile("global_load_dwordx4 v[220:223], %0, %1 offset:0 sc1" :: "v"(l16), "s"((p) + 12288) : "v220", "v221", "v222", "v223", "memory"); \
        } while (0)
#define H_LDB(j, p) do { \
        if ((j) == 0) asm volatile("global_load_dwordx4 v[224:227], %0, %1 offset:0 sc1" :: "v"(l16), "s"((p) + 0) : "v224", "v225", "v226", "v227", "memory"); \
        if ((j) == 1) asm volatile("global_load_dwordx4 v[228:231], %0, %1 offset:2048 sc1" :: "v"(l16), "s"((p) + 0) : "v228", "v229", "v230", "v231", "memory"); \
        if ((j) == 2) asm volatile("global_load_dwordx4 v[232:235], %0, %1 offset:0 sc1" :: "v"(l16), "s"((p) + 4096) : "v232", "v233", "v234", "v235", "memory"); \
        if ((j) == 3) asm volatile("global_load_dwordx4 v[236:239], %0, %1 offset:2048 sc1" :: "v"(l16), "s"((p) + 4096) : "v236", "v237", "v238", "v239", "memory"); \
        if ((j) == 4) asm volatile("global_load_dwordx4 v[240:243], %0, %1 offset:0 sc1" :: "v"(l16), "s"((p) + 8192) : "v240", "v241", "v242", "v243", "memory"); \
        if ((j) == 5) asm volatile("global_load_dwordx4 v[244:247], %0, %1 offset:2048 sc1" :: "v"(l16), "s"((p) + 8192) : "v244", "v245", "v246", "v247", "memory"); \
        if ((j) == 6) asm volatile("global_load_dwordx4 v[248:251], %0, %1 offset:0 sc1" :: "v"(l16), "s"((p) + 12288) : "v248", "v249", "v250", "v251", "memory"); \
        } while (0)
#define H_LDC(j, p) do { \
        if ((j) == 0) asm volatile("global_load_dwordx4 v[168:171], %0, %1 offset:0 sc1" :: "v"(l16), "s"((p) + 0) : "v168", "v169", "v170", "v171", "memory"); \
        if ((j) == 1) asm volatile("global_load_dwordx4 v[172:175], %0, %1 offset:2048 sc1" :: "v"(l16), "s"((p) + 0) : "v172", "v173", "v174", "v175", "memory"); \
        if ((j) == 2) asm volatile("global_load_dwordx4 v[176:179], %0, %1 offset:0 sc1" :: "v"(l16), "s"((p) + 4096) : "v176", "v177", "v178", "v179", "memory"); \
        if ((j) == 3) asm volatile("global_load_dwordx4 v[180:183], %0, %1 offset:2048 sc1" :: "v"(l16), "s"((p) + 4096) : "v180", "v181", "v182", "v183", "memory"); \
        if ((j) == 4) asm volatile("global_load_dwordx4 v[184:187], %0, %1 offset:0 sc1" :: "v"(l16), "s"((p) + 8192) : "v184", "v185", "v186", "v187", "memory"); \
        if ((j) == 5) asm volatile("global_load_dwordx4 v[188:191], %0, %1 offset:2048 sc1" :: "v"(l16), "s"((p) + 8192) : "v188", "v189", "v190", "v191", "memory"); \
        if ((j) == 6) asm volatile("global_load_dwordx4 v[192:195], %0, %1 offset:0 sc1" :: "v"(l16), "s"((p) + 12288) : "v192", "v193", "v194", "v195", "memory"); \
        } while (0)
struct ConvOps { u4 B[8], A0, A1; u2 v2, v3; };
template <int TS, int KK> DI void conv_read(ConvOps& o, int i, u32 u, u32 l, u32 q) {
    constexpr int ORD[2][8] = {{0, 3, 4, 7, 2, 6, 1, 5}, {1, 2, 5, 6, 0, 4, 3, 7}};
    auto two = [&](u32 a) { return u2{LDS(u32, a), LDS(u32, a + 4)}; };
    if (i == 0) { const u2 t = two(l + 704 - 64 * KK); o.A0.x = t.x; o.A0.y = t.y; }
    if (i == 1) { const u2 t = two(l + 712 - 64 * KK); o.A0.z = t.x; o.A0.w = t.y; }
    if (i >= 2 && i < 6) o.B[ORD[TS][i - 2]] = LDS(u4, u + 192 - 64 * KK + 512 * ORD[TS][i - 2]);
    if (i == 6) { const u2 t = two(l + 192 - 64 * KK); o.A1.x = t.x; o.A1.y = t.y; }
    if (i == 7) { const u2 t = two(l + 200 - 64 * KK); o.A1.z = t.x; o.A1.w = t.y; }
    if (i >= 8 && i < 12 - TS) o.B[ORD[TS][i - 4]] = LDS(u4, u + 192 - 64 * KK + 512 * ORD[TS][i - 4]);
    if (i == 12) { o.v2 = LDS(u2, q + 64 * KK); o.v3 = LDS(u2, q + 64 * KK + 512); }
}
template <int TS, int KK>
DI void conv_step(f4 (&acc)[4], const ConvOps& c, ConvOps& n, const u4& a2s, bool use2s, u32 u, u32 l, u32 q, const u4& sel2,
                  u32 vq0, u32 vq1, u32 vq2, u32 vq3, const char* src, u32 l16, const char* hrd) {
    constexpr int T[2][4] = {{0, 3, 4, 7}, {1, 2, 5, 6}};
    auto seg2 = [&](u2 v) { u4 r;
        r.x = __builtin_amdgcn_perm(v.y, v.x, sel2.x); r.y = __builtin_amdgcn_perm(v.y, v.x, sel2.y);
        r.z = __builtin_amdgcn_perm(v.y, v.x, sel2.z); r.w = __builtin_amdgcn_perm(v.y, v.x, sel2.w); return r; };
    u4 A2, A3;
    int m = 0;
#pragma unroll
    for (int d = 0; d < 4; ++d)
#pragma unroll
        for (int i = 0; i < 4; ++i)
            if (T[TS][i] >= d) {
                acc[i] = mfma(d == 0 ? c.A0 : d == 1 ? c.A1 : d == 2 ? A2 : A3, c.B[T[TS][i] - d], acc[i]);
                if (KK < 3) conv_read<TS, KK + 1>(n, m, u, l, q);
                if (m == 1) { A2 = seg2(c.v2); if (KK == 0 && use2s) A2 = a2s; PIN(A2); }
                if (m == 3) { A3 = seg2(c.v3); PIN(A3); }
                if (KK < 2) { if (m == 2) DMAO(vq0, src, 0); if (m == 5) DMAO(vq1, src, 1024); if (m == 8) DMAO(vq2, src, 2048); if (m == 11) DMAO(vq3, src, 3072); }
                if (KK == 3 && !(m & 1)) H_LDA(m >> 1, hrd);
                SB();
                ++m;
            }
}
template <int TS>
DI void conv_steps(f4 (&acc)[4], bool first, u32 u, u32 l, u32 q, u32 la, const u4& sel2, u32 vq0, u32 vq1, u32 vq2, u32 vq3, const char* src, u32 dst, u32 l16, const char* hrd) {
    ConvOps a, b;
#pragma unroll
    for (int i = 0; i < 13; ++i) conv_read<TS, 0>(a, i, u, l, q);
    const u4 a2s = {LDS(u32, la + 96), LDS(u32, la + 100), LDS(u32, la + 104), LDS(u32, la + 108)};
    SETM0(dst); __builtin_amdgcn_s_setprio(3); SB();
    conv_step<TS, 0>(acc, a, b, a2s, first, u, l, q, sel2, vq0, vq1, vq2, vq3, src, l16, hrd);
    SETM0(dst + 16384); __builtin_amdgcn_s_setprio(2); SB();
    conv_step<TS, 1>(acc, b, a, a2s, first, u, l, q, sel2, vq0, vq1, vq2, vq3, src + 4 * 7168, l16, hrd);
    __builtin_amdgcn_s_setprio(1);
    conv_step<TS, 2>(acc, a, b, a2s, first, u, l, q, sel2, vq0, vq1, vq2, vq3, src, l16, hrd);
    __builtin_amdgcn_s_setprio(0);
    conv_step<TS, 3>(acc, b, a, a2s, first, u, l, q, sel2, vq0, vq1, vq2, vq3, src, l16, hrd);
}

template <int TS, int S0>
DI void conv_far(f4 (&acc)[4], u32 ub, u32 la, u32 te, const u4& box) {
    constexpr int T2 = TS ? 5 : 4, T3 = TS ? 6 : 7, NS = S0 + 2 < T3 - 3 ? S0 + 2 : T3 - 3;
    const u4 b9 = box & 0xfffefffeu, b7 = box & ~((box & 0x00010001u) * 0xffffu);
    u4 Ue[NS], Uo[NS], Tt[4];
#pragma unroll
    for (int s = S0; s < NS; ++s) { Ue[s] = LDS(u4, ub - 16 + 512 * s); Uo[s] = LDS(u4, ub + 512 * s); }
#pragma unroll
    for (int s = 0; s < T3 - 3 - S0; ++s) Tt[s] = LDS(u4, te + 256 * s);
    { const u4 a4 = {LDS(u32, la), LDS(u32, la + 4), LDS(u32, la + 8), LDS(u32, la + 12)};
      if (S0) acc[3] = mfma(a4, Uo[T3 - 4], acc[3]); else acc[2] = mfma(a4, Uo[T2 - 4], acc[2]); }
#pragma unroll
    for (int s = S0; s < NS; ++s) {
        const f4 e = mfma(Ue[s], b9, f4{0, 0, 0, 0}), o = mfma(Uo[s], b7, f4{0, 0, 0, 0});
        const u4 z = {pkbf(e.x, e.y), pkbf(e.z, e.w), pkbf(o.x, o.y), pkbf(o.z, o.w)};
        acc[3] = mfma(z, Tt[T3 - 4 - s], acc[3]);
        if (s <= T2 - 4) acc[2] = mfma(z, Tt[T2 - 4 - s], acc[2]);
    }
}

__global__ __launch_bounds__(512, 2) void s4ffn_kernel(const u16* __restrict__ x, const u16* __restrict__ Are, const u16* __restrict__ Aim,
        const u16* __restrict__ Bre, const u16* __restrict__ Bim, const u16* __restrict__ Cre, const u16* __restrict__ Cim,
        const u16* __restrict__ logdt, const u16* __restrict__ Dsk, const u16* __restrict__ w1, const u16* __restrict__ w2,
        u16* __restrict__ out, char* __restrict__ ws) {
    const int tid = threadIdx.x, lane = tid & 63, wave = __builtin_amdgcn_readfirstlane(tid >> 6);
    const u32 b = blockIdx.x, xcd = b & 7, idx = b >> 3;
    const int fl = lane & 15, g = lane >> 4, l31 = lane & 31, hh = lane >> 5;
    STAMP_DECL;
    {
        u64 pc; asm volatile("s_getpc_b64 %0" : "=s"(pc));
        pc &= ~127ull;
        if (tid < 8 && idx + tid * 32 < PF_LINES) PREFETCH((idx + tid * 32) * 128, pc);
    }
    STAMP(0); STAMPC(22);
    asm volatile("" :: "s"(x), "s"(Are), "s"(Aim), "s"(Bre), "s"(Bim), "s"(Cre), "s"(Cim), "s"(logdt), "s"(Dsk), "s"(w1), "s"(w2), "s"(out), "s"(ws));
    u32 tag;
    const u32 w4 = wave & 3;
    u4 xr0, xr1; u32 pq0, pq1, pq2, pq3, pq4, pq5;
    if (wave < 4)
        asm volatile("v_mov_b64 v[232:233], 0\n\tv_mov_b64 v[234:235], 0\n\tv_mov_b64 v[236:237], 0\n\tv_mov_b64 v[238:239], 0\n\ts_mov_b32 exec_hi, 0xffffff\n\t"
                     "global_load_dwordx4 v[232:235], %0, %1\n\tglobal_load_dwordx4 v[236:239], %0, %1 offset:896\n\ts_mov_b32 exec_hi, -1"
                     :: "v"(lane * 16), "s"((const char*)x + (b * 8 + 2 * w4) * 896) : "v232", "v233", "v234", "v235", "v236", "v237", "v238", "v239", "memory");
    else
        asm volatile("global_load_dword v240, %0, %1\n\tglobal_load_dword v241, %0, %2\n\tglobal_load_dword v242, %0, %3\n\t"
                     "global_load_dword v243, %0, %4\n\tglobal_load_dword v244, %0, %5\n\tglobal_load_dword v245, %0, %6"
                     :: "v"(min(28 * xcd + idx, 223u) * 1024 + w4 * 256 + lane * 4), "s"(Are), "s"(Aim), "s"(Bre), "s"(Bim), "s"(Cre), "s"(Cim)
                     : "v240", "v241", "v242", "v243", "v244", "v245", "memory");
    asm volatile("s_load_dword s84, %0, 0x0\n\ts_load_dword s85, %1, %3\n\ts_load_dword s86, %2, %3" :: "s"(ws), "s"(logdt), "s"(Dsk), "s"(min(28 * xcd + idx, 223u) * 4) : "s84", "s85", "s86");
    u32 ldt2, dsk2;
#define SCAL_WAIT() asm volatile("s_waitcnt lgkmcnt(0)" : "={s84}"(tag), "={s85}"(ldt2), "={s86}"(dsk2) :: "memory")
#define ROWS_WAIT(n) asm volatile("s_waitcnt vmcnt(" #n ")" : "={v[232:235]}"(xr0), "={v[236:239]}"(xr1) :: "memory")
#define PARS_WAIT(n) asm volatile("s_waitcnt vmcnt(" #n ")" : "={v240}"(pq0), "={v241}"(pq1), "={v242}"(pq2), "={v243}"(pq3), "={v244}"(pq4), "={v245}"(pq5) :: "memory")
#define LN_ROWS() do { \
        float s0 = 0, s1 = 0, q0 = 0, q1 = 0; \
        _Pragma("unroll") for (int i = 0; i < 4; ++i) { s0 = dot2(xr0[i], 0x3f803f80u, s0); s1 = dot2(xr1[i], 0x3f803f80u, s1); q0 = dot2(xr0[i], xr0[i], q0); q1 = dot2(xr1[i], xr1[i], q1); } \
        const u32 v_ = __float_as_uint(rsum(sw16(sw32(s0, s1), sw32(q0, q1))) * (1.f / 448.f)); \
        auto e_ = __builtin_amdgcn_permlane16_swap(v_, v_, false, false);     \
        const float mean = __uint_as_float(e_[0]), var = fmaxf(__uint_as_float(e_[1]) - mean * mean, 0.f); \
        const float mb = rbf(mean), sd_ = rbf(__builtin_amdgcn_sqrtf(rbf(rbf(var) + 1e-5f))), a_ = __builtin_amdgcn_rcpf(sd_), c_ = -mb * a_; \
        const u32 st_ = (__float_as_uint(mb) >> 16) | __float_as_uint(sd_); \
        auto a2_ = __builtin_amdgcn_permlane32_swap(__float_as_uint(a_), __float_as_uint(a_), false, false); \
        auto c2_ = __builtin_amdgcn_permlane32_swap(__float_as_uint(c_), __float_as_uint(c_), false, false); \
        u4 o0, o1; \
        _Pragma("unroll") for (int i = 0; i < 4; ++i) { \
            o0[i] = pkbf(fmaf(bflo(xr0[i]), __uint_as_float(a2_[0]), __uint_as_float(c2_[0])), fmaf(bfhi(xr0[i]), __uint_as_float(a2_[0]), __uint_as_float(c2_[0]))); \
            o1[i] = pkbf(fmaf(bflo(xr1[i]), __uint_as_float(a2_[1]), __uint_as_float(c2_[1])), fmaf(bfhi(xr1[i]), __uint_as_float(a2_[1]), __uint_as_float(c2_[1]))); } \
        if (lane < 56) { LDS(u4, L_XT + w4 * 1920 + lane * 16) = o0; LDS(u4, L_XT + w4 * 1920 + 960 + lane * 16) = o1; } \
        if (l31 == 0) LDS(u32, L_XT + w4 * 1920 + hh * 960 + 896) = st_; } while (0)
#define LN_PUBLISH() do { \
        const u32 a_ = L_XT + (fl >> 2) * 960 + w4 * 128 + g * 32 + (lane & 3) * 8; \
        const u4 v0 = trread2(a_, a_ + 3840), v1 = trread2(a_ + 512, a_ + 3840 + 512); \
        GST4(lane * 16, v0, ws + WS_XG + b * 7168 + w4 * 1024, "sc1"); \
        if (w4 < 3) GST4(lane * 16, v1, ws + WS_XG + b * 7168 + w4 * 1024 + 4096, "sc1"); \
        else if (lane < 2) { const u32 s_ = L_XT + lane * 3840 + 896; const u4 st = {LDS(u32, s_), LDS(u32, s_ + 960), LDS(u32, s_ + 1920), LDS(u32, s_ + 2880)}; \
            GST4(lane * 16, st, ws + WS_STAT + b * 32, "sc1"); } } while (0)
    if (idx >= 28) {
        auto prefetch = [&](u32 k, u32 j) {
            const u32 n = ((k * 4 + idx - 28) * 8 + j) * 64 + lane, m = n - 7168;
            const u32 o = n < 7168 ? ((n / 224 & 3) + (n < 3584 ? 4 : 0)) * 200704 + (n / 896 & 3) * 28672 + n % 224 * 128 : m / 672 * 200704 + 114688 + m % 672 * 128;
            PREFETCH(min(o, 1605504u), w2);
        };
        if (wave < 4) {
            ROWS_WAIT(0);
            STAMP(1);
            LN_ROWS();
            BARRIER();
            __builtin_amdgcn_s_sleep(8);
            prefetch(1, wave); prefetch(1, wave + 4);
        } else {
            prefetch(0, wave - 4); prefetch(0, wave);
            BARRIER();
            LN_PUBLISH();
            WAITV(0);
        }
        SCAL_WAIT();
        BARRIER();
        tag += 1;
        STAMP(3);
        if (tid == 256) { const u2 ar = {tag, tag}; GST2(0, ar, ws + WS_AR + b * 8, "sc1"); }
#pragma unroll 1
        for (u32 k = 2; k < 7; ++k) prefetch(k, wave);
        if (b == 224 && wave == 0) {
            u4 t0, t1;
#pragma unroll 1
            for (int sp = 0; sp < SPIN_MAX; ++sp) {
                RLD4(t0, lane * 16, ws + WS_AR); RLD4(t1, lane * 16, ws + WS_AR + 1024);
                if (!__any(((t0.x ^ tag) | (t0.z ^ tag) | (t1.x ^ tag) | (t1.z ^ tag)) != 0)) break;
            }
            if (lane == 0) { const u2 e = {tag, tag}; GST2(0, e, ws + WS_EPOCH, "sc1"); }
        }
        WAITV(0);
        STAMP(5); STAMP_FLUSH(24);
        return;
    }
    const bool isF = wave < 4;
    const u32 q7 = idx / 7, r7 = idx - 7 * q7;
    const u32 rp = 4 * (xcd >> 1) + q7, cc = 7 * (xcd & 1) + r7, P = 4 * xcd + q7, ct = r7, dp = 28 * xcd + idx;
    const u32 rg = w4 >> 1, ch = wave & 1, hi4 = wave >> 2;
    u32 kt = (r7 + 2 * q7) % 7;
    const int r8 = lane >> 3, p8 = lane & 7, rw = (lane >> 2) & 7;
    u32 vo0 = (32 * w4 + r8) * 896 + 16 * (p8 ^ (r8 >> 1)), vo1 = ((vo0 + 8 * 896) ^ 64) + 2048, vo2 = vo0 + 16 * 896 + 1024, vo3 = vo1 + 16 * 896 - 2048;
    u32 vo4 = (16 * w4 + rw) * 3584 + 64 * hh + 16 * ((lane & 3) ^ (rw >> 2)), vo5 = vo4 + 128 + 2048, vo6 = ((vo4 + 8 * 3584) ^ 32) + 1024, vo7 = vo6 + 128 - 1024;
    vo0 += 3072; vo4 += 3072;
    const char *xb = (const char*)x + rp * 114688 - 3072, *wb = (const char*)w1 + cc * 256 - 3072;
    const char *pa, *pw;
#define RING_SET() do { asm volatile("" : "+s"(kt)); pa = xb + kt * 128; pw = wb + kt * 229376; kt = kt == 6 ? 0 : kt + 1; } while (0)
#define DMA_X(i) do { if ((i) == 0) DMAO(vo0, pa, 0); if ((i) == 1) DMAO(vo1, pa, 1024); if ((i) == 2) DMAO(vo2, pa, 2048); if ((i) == 3) DMAO(vo3, pa, 3072); } while (0)
#define DMA_W(i) do { if ((i) == 0) DMAO(vo4, pw, 0); if ((i) == 1) DMAO(vo5, pw, 1024); if ((i) == 2) DMAO(vo6, pw, 2048); if ((i) == 3) DMAO(vo7, pw, 3072); } while (0)
#define DMA_X4(l_) do { SETM0(l_); DMA_X(0); DMA_X(1); DMA_X(2); DMA_X(3); } while (0)
#define DMA_W4(l_) do { SETM0((l_) + 16384); DMA_W(0); DMA_W(1); DMA_W(2); DMA_W(3); } while (0)
    const u32 lw = L_RING + w4 * 4096;
    u4 sel2, sel3; u32 cv_ub, cv_la, cv_q2, cv_p3, hm0, hm2;
#define SEL_READ() do { sel2 = LDS(u4, L_SEL + lane * 16); sel3 = LDS(u4, L_SEL + 1024 + lane * 16); const u4 c_ = LDS(u4, L_SEL + 2048 + lane * 16); \
        cv_ub = c_.x + rg * 4608; cv_la = c_.y + rg * 5120; cv_q2 = c_.z + rg * 5120; cv_p3 = c_.w + rg * 5120; } while (0)
#define SCALARS_DONE() do { tag += 1; const u32 pat = 1 + tag % 15; hm0 = (pat & 1) << 15 | (pat & 2) << 30; hm2 = (pat & 4) << 13 | (pat & 8) << 28; } while (0)
    const char* hb = ws + WS_H + ((2 * rp + rg) * 112 + 8 * cc + 4 * ch) * 2048;
    const u32 vw2 = r8 * 896 + 16 * (p8 ^ (4 * ((r8 >> 1) & 1)));
    const char* w2b = (const char*)w2 + wave * (224 * 896) + ct * 128;
    const u32 pb0 = lw + (isF ? 32768 : 98304), pb1 = lw + (isF ? 65536 : 0);
    u4 Bw[14][2];
    const u32 aw2a = pb0 + (4 * hh + (fl >> 2)) * 128 + 16 * ((2 * (g & 1) + ((lane & 3) >> 1)) ^ (4 * ((fl >> 3) & 1))) + 8 * (lane & 1);
    const u32 aw2b = aw2a ^ 64, aw2c = aw2a - pb0 + pb1, aw2d = aw2c ^ 64;
#define W2_RD(j, p, n) do { const u32 o_ = ((n) >> 1) * 16384 + ((n) & 1) * 2048; \
        Bw[j][0] = trread2(((p) ? aw2c : aw2a) + o_, ((p) ? aw2c : aw2a) + o_ + 1024); Bw[j][1] = trread2(((p) ? aw2d : aw2b) + o_, ((p) ? aw2d : aw2b) + o_ + 1024); } while (0)
    const u32 vq0 = vw2 + 3072, vq1 = vw2 + 7168 + 2048, vq2 = vw2 + 14336 + 1024, vq3 = vw2 + 21504;
#define W2_4(src, dst) do { SETM0(dst); DMAO(vq0, src, 0); DMAO(vq1, src, 1024); DMAO(vq2, src, 2048); DMAO(vq3, src, 3072); } while (0)
#define W2_PIECE(n, dst) do { W2_4(w2b - 3072 + (n) * 57344, dst); W2_4(w2b - 3072 + (n) * 57344 + 28672, (dst) + 16384); } while (0)
#define W2_CH_IN(n) do { W2_4(w2b - 3072 + (n) * 28672, pb0 + ((n) & 1) * 16384); SETM0(L_SCR + w4 * 4352); } while (0)
#define W2_CH_OUT(n) do { W2_RD(2 * (n), 0, 2 * ((n) & 1)); W2_RD(2 * (n) + 1, 0, 2 * ((n) & 1) + 1); \
        PIN(Bw[2 * (n)][0]); PIN(Bw[2 * (n)][1]); PIN(Bw[2 * (n) + 1][0]); PIN(Bw[2 * (n) + 1][1]); } while (0)
#define W2_DRAIN(n, p) do { W2_RD(4 * (n), p, 0); W2_RD(4 * (n) + 1, p, 1); W2_RD(4 * (n) + 2, p, 2); W2_RD(4 * (n) + 3, p, 3); \
        _Pragma("unroll") for (int j = 4 * (n); j < 4 * (n) + 4; ++j) { PIN(Bw[j][0]); PIN(Bw[j][1]); } } while (0)
    f4 cacc[4];
#pragma unroll
    for (int i = 0; i < 4; ++i) cacc[i] = f4{0, 0, 0, 0};
    if (isF) {
        __builtin_amdgcn_s_setprio(2); RING_SET(); DMA_X4(lw); DMA_W4(lw); __builtin_amdgcn_s_setprio(1); RING_SET();
        DMA_X4(lw + 32768);
        __builtin_amdgcn_s_setprio(0);
        f16v acc[2][2]; f4 cs = {0, 0, 0, 0};
#pragma unroll
        for (int i = 0; i < 16; ++i) { acc[0][0][i] = 0; acc[0][1][i] = 0; acc[1][0][i] = 0; acc[1][1][i] = 0; }
        const u32 o1 = ((g ^ lane) & 1) ? 0 : 0x3f803f80u; u4 ones = {o1, o1, o1, o1};
        u32 ax0 = L_RING + (64 * rg + l31) * 128 + 16 * (hh ^ ((l31 >> 1) & 7)), ax1 = ax0 ^ 32, ax2 = ax0 ^ 64, ax3 = ax0 ^ 96;
        u32 aw0 = L_RING + 16384 + 2048 * hh + 1024 * ch + 512 * rg + 64 * (fl >> 2) + 16 * ((2 * (g & 1) + ((lane & 3) >> 1)) ^ (2 * hh)) + 8 * (lane & 1);
        u32 aw1 = aw0 ^ 272, aw2 = aw0 ^ 512, aw3 = aw0 ^ 784;
        u4 X0[4], W0[4], X1[4], W1[4];
#define G1_RD(i, X, W, a0, a1, o) do { if ((i) == 0) { X[0] = LDS(u4, a0); W[0] = trread2(aw0 + (o), aw1 + (o)); } \
        if ((i) == 1) { X[1] = LDS(u4, a0 + 4096); W[1] = trread2(aw2 + (o), aw3 + (o)); } \
        if ((i) == 2) { X[2] = LDS(u4, a1); W[2] = trread2(aw0 + (o) + 4096, aw1 + (o) + 4096); } \
        if ((i) == 3) { X[3] = LDS(u4, a1 + 4096); W[3] = trread2(aw2 + (o) + 4096, aw3 + (o) + 4096); } } while (0)
#define G1_PHASE(X, W, RD, DM, EX) do { \
        acc[0][0] = mfma32(W[0], X[0], acc[0][0]); RD(0); SB(); \
        acc[0][1] = mfma32(W[0], X[1], acc[0][1]); RD(1); SB(); \
        cs = mfma(W[0], ones, cs); SB(); \
        acc[1][0] = mfma32(W[1], X[0], acc[1][0]); DM(0); EX(0); SB(); \
        acc[1][1] = mfma32(W[1], X[1], acc[1][1]); RD(2); SB(); \
        acc[0][0] = mfma32(W[2], X[2], acc[0][0]); DM(1); RD(3); SB(); \
        cs = mfma(W[2], ones, cs); SB(); \
        acc[0][1] = mfma32(W[2], X[3], acc[0][1]); DM(2); EX(1); SB(); \
        acc[1][0] = mfma32(W[3], X[2], acc[1][0]); EX(2); SB(); \
        acc[1][1] = mfma32(W[3], X[3], acc[1][1]); DM(3); EX(3); SB(); } while (0)
#define RDA(i) G1_RD(i, X1, W1, ax2, ax3, 8192)
#define RDB(i) G1_RD(i, X0, W0, ax0, ax1, 0)
#define NONE(i)
#define EXA(i) do { if ((i) == 0) { ax0 ^= m_; ax1 ^= m_; asm volatile("" : "+s"(kt)); pa = s < 4 ? xb + kt * 128 : w2b - 3072 + (s - 4) * 57344; } \
        if ((i) == 1) { aw0 ^= m_; aw1 ^= m_; ax2 ^= m_; } if ((i) == 2) { aw2 ^= m_; aw3 ^= m_; ax3 ^= m_; \
            if (s == 4) { vo0 = vw2 + 3072; vo1 = vw2 + 7168 + 2048; vo2 = vw2 + 14336 + 1024; vo3 = vw2 + 21504; } } \
        if ((i) == 3) { ld = lw + sl * 32768; SETM0(ld); } } while (0)
#define EXB(i) do { if ((i) == 0) { pw = s < 4 ? wb + kt * 229376 : pa; kt = kt == 6 ? 0 : kt + 1; } \
        if ((i) == 1) { sl = sl == 2 ? 0 : sl + 1; m_ = 0x8000u * (sl == 0 ? 3 : sl); } \
        if ((i) == 2) { if (s == 4) { vo4 = vw2 + 28672 + 3072; vo5 = vw2 + 35840 + 2048; vo6 = vw2 + 43008 + 1024; vo7 = vw2 + 50176; } } \
        if ((i) == 3) SETM0(ld + 16384); } while (0)
        PIN(acc[0][0]); PIN(acc[0][1]); PIN(acc[1][0]); PIN(acc[1][1]);
        PIN(ax0); PIN(ax1); PIN(ax2); PIN(ax3); PIN(aw0); PIN(aw1); PIN(aw2); PIN(aw3); PIN(ones);
        __builtin_amdgcn_s_setprio(2);
        ROWS_WAIT(12);
        STAMP(1);
        DMA_W4(lw + 32768);
        RING_SET();
        LN_ROWS();
        SCAL_WAIT();
        SCALARS_DONE();
        __builtin_amdgcn_s_setprio(0);
        STAMP(2);
        WAITV(8);
        BARRIER();
        RDB(0); RDB(1); RDB(2); RDB(3);
        DMA_X4(lw + 65536);
        SETM0(lw + 65536 + 16384);
        STAMP(3);
        u32 ld, sl = 0, m_ = 0x18000;
#pragma unroll 1
        for (u32 s = 0; s < 6; ++s) {
            G1_PHASE(X0, W0, RDA, DMA_W, EXA);
            FSTAMP(s);
            WAITV(8); BARRIER();
            SB();
            G1_PHASE(X1, W1, RDB, DMA_X, EXB);
        }
        G1_PHASE(X0, W0, RDA, DMA_W, NONE);
        G1_PHASE(X1, W1, NONE, NONE, NONE);
        if (fl < 2) LDS(f4, L_CS + (64 * ch + 32 * rg + 16 * fl + 4 * g) * 4) = cs;
        STAMP(4);
        BARRIER();
#pragma unroll
        for (int tt = 0; tt < 2; ++tt) {
            const u32 sv = LDS(u32, L_ST + (64 * rg + 32 * tt + l31) * 4);
            const float m = -bflo(sv), rs = __builtin_amdgcn_rcpf(bfhi(sv));
            const f2 m2 = {m, m}, rs2 = {rs, rs};
#pragma unroll
            for (int c = 0; c < 2; ++c)
#pragma unroll
                for (int k = 0; k < 2; ++k) {
                    u4 o;
#pragma unroll
                    for (int q = 0; q < 2; ++q) {
                        const f4 cv = LDS(f4, L_CS + ((64 * ch + 32 * rg + 4 * hh) * 4 ^ (128 * c)) + (2 * k + q) * 32), av = quad(acc[c][tt], 2 * k + q);
                        const f2 v0 = __builtin_elementwise_fma(m2, f2{cv.x, cv.y}, f2{av.x, av.y}) * rs2, v1 = __builtin_elementwise_fma(m2, f2{cv.z, cv.w}, f2{av.z, av.w}) * rs2;
                        o[2 * q] = relu2(pkbf(v0.x, v0.y)) | (q ? hm2 : hm0); o[2 * q + 1] = relu2(pkbf(v1.x, v1.y));
                    }
                    const char* hp = hb + (2 * (c ^ rg) + k) * 2048;
                    if (tt) GST4(lane * 16, o, hp, "offset:1024 sc1"); else GST4(lane * 16, o, hp, "sc1");
                }
        }
        STAMP(12);
        SEL_READ();
        STAMP(5);
        BARRIER();
        WAITV(8);
        W2_DRAIN(0, 0); W2_DRAIN(1, 1);
        W2_4(w2b - 3072 + 6 * 28672, pb1);
        STAMP(6);
    } else {
        __builtin_amdgcn_s_setprio(1);
        SCAL_WAIT();
        SCALARS_DONE();
        STAMP(1);
        const float dt = rbf(expf(rg ? bfhi(ldt2) : bflo(ldt2)));
        u32 sr = L_SCR + w4 * 4352 + fl * 272 + g * 16; PIN(sr);
        asm volatile("s_mov_b32 m0, %0" :: "s"(L_SCR + w4 * 4352));
        PARS_WAIT(0);
        STAMP(2);
        f2 c0 = cmulc(f2{bflo(pq4), -bflo(pq5)}, f2{dt * bflo(pq2), dt * bflo(pq3)}), c1 = cmulc(f2{bfhi(pq4), -bfhi(pq5)}, f2{dt * bfhi(pq2), dt * bfhi(pq3)});
        float isc;
        {
            float m = rmax(fmaxf(fmaxf(fabsf(c0.x), fabsf(c0.y)), fmaxf(fabsf(c1.x), fabsf(c1.y))));
            auto s1 = __builtin_amdgcn_permlane16_swap(__float_as_uint(m), __float_as_uint(m), false, false); u32 e = max(s1[0], s1[1]);
            auto s2 = __builtin_amdgcn_permlane32_swap(e, e, false, false); e = max(s2[0], s2[1]) & 0x7f800000u;
            isc = __uint_as_float(e);
            const float sc = __uint_as_float(0x7f000000u - e);
            c0.x *= sc; c0.y *= sc; c1.x *= sc; c1.y *= sc;
        }
        f4 dk[4];
#pragma unroll
        for (int t = 0; t < 4; ++t) dk[t] = f4{0, 0, 0, 0};
        u2 arf; u32 stv; u4 xg0, xg1;
        const u32 xo = ((tid & 255) >> 1) * 7168 + (tid & 1) * 16;
#define SBAR() do { SB(); ACC_BEGIN(); asm volatile("s_barrier" ::: "memory"); ACC_END(21); SB(); } while (0)
#define PUT(e, v) asm volatile("ds_write_addtid_b32 %0 offset:%1" :: "v"(v), "i"((e) * 272) : "memory")
#define RD4(A) _Pragma("unroll") for (int s = 0; s < 4; ++s) A[s] = LDS(u4, sr + s * 64)
#define MM2(h, s) do { dk[2 * (h)] = mfmah(A[s], B0[s], dk[2 * (h)]); dk[2 * (h) + 1] = mfmah(A[s], B1[s], dk[2 * (h) + 1]); } while (0)
        u4 A[4], B0[4], B1[4];
        f2 z1, z8, z32, z256;
        h2 w8, w16, w24, a32, a64, a96, a128;
#define KG_POW(hi) do { const float a2 = fmul1(dt, (hi) ? bfhi(pq0) : bflo(pq0)) * 1.44269504089f, rev = fmul1(dt, (hi) ? bfhi(pq1) : bflo(pq1)) * 0.15915494309f; \
        z1 = zpow(a2, rev, 1.f); z8 = zpow(a2, rev, 8.f); z32 = zpow(a2, rev, 32.f); z256 = zpow(a2, rev, 256.f); } while (0)
        f2 z512, e4, e5; h2 f1, f2_, f4_, g0, g1, g2, g3;
#define KG_COLS_A(c, PEND) do { \
        const f2 z2 = cmul(z1, z1), e1 = cmulc(c, z256), z16 = cmul(z8, z8), z64 = cmul(z32, z32); z512 = cmul(z256, z256); \
        if (PEND) MM2(1, 0); \
        const f2 z4 = cmul(z2, z2), e2 = cmulc(c, z512), e3 = cmulc(e1, z512), z128 = cmul(z64, z64); \
        if (PEND) MM2(1, 1); \
        f1 = cvth(z1); f2_ = cvth(z2); f4_ = cvth(z4); g0 = cvth(c); g1 = cvth(e1); g2 = cvth(e2); g3 = cvth(e3); \
        if (PEND) MM2(1, 2); \
        e4 = cmulc(e2, z512); e5 = cmulc(e3, z512); \
        if (PEND) MM2(1, 3); \
        w8 = cvth(z8); w16 = cvth(z16); w24 = hmul(w8, w16); a32 = cvth(z32); a64 = cvth(z64); a96 = hmul(a32, a64); a128 = cvth(z128); } while (0)
#define KG_COLS_B() do { \
        const h2 f3 = hmul(f1, f2_), f6 = hmul(f2_, f4_), g04 = hmulc(g0, f4_); \
        PUT(0, g0); PUT(1, hmulc(g0, f1)); PUT(2, hmulc(g0, f2_)); PUT(3, hmulc(g0, f3)); \
        PUT(4, g04); PUT(5, hmulc(g04, f1)); PUT(6, hmulc(g0, f6)); PUT(7, hmulc(g04, f3)); \
        const f2 e6 = cmulc(e4, z512), e7 = cmulc(e5, z512); \
        PUT(8, g1); PUT(9, hmulc(g1, f2_)); PUT(10, hmulc(g1, f4_)); PUT(11, hmulc(g1, f6)); \
        const f2 e8 = cmulc(e6, z512); \
        PUT(12, g2); PUT(13, hmulc(g2, f4_)); PUT(14, g3); PUT(15, hmulc(g3, f4_)); \
        RD4(B0); \
        PUT(0, cvth(e4)); PUT(1, cvth(e5)); PUT(2, cvth(e6)); PUT(3, cvth(e7)); PUT(4, cvth(e8)); \
        RD4(B1); } while (0)
#define KG_QUAD(e, a) do { PUT(e, a); PUT((e) + 1, hmul(a, w8)); PUT((e) + 2, hmul(a, w16)); PUT((e) + 3, hmul(a, w24)); } while (0)
#define KG_ROWS_A() do { const h2 one = {1, 0}; PUT(0, one); PUT(1, w8); PUT(2, w16); PUT(3, w24); KG_QUAD(4, a32); KG_QUAD(8, a64); KG_QUAD(12, a96); RD4(A); } while (0)
#define KG_ROWS_B() do { const h2 a160 = hmul(a128, a32), a192 = hmul(a128, a64), a224 = hmul(a128, a96); \
        KG_QUAD(0, a128); MM2(0, 0); KG_QUAD(4, a160); MM2(0, 1); KG_QUAD(8, a192); MM2(0, 2); KG_QUAD(12, a224); MM2(0, 3); RD4(A); } while (0)
        KG_POW(0); KG_COLS_A(c0, 0); KG_COLS_B(); KG_ROWS_A();
        STAMP(3);
        SBAR();
        LN_PUBLISH();
        KG_ROWS_B();
        STAMPF(15); SBAR();
        { const u32 i0 = min(w4, 2u); SETM0(L_TAB + i0 * 2048); DMAO(lane * 16, (const char*)&CTAB + i0 * 2048, 0); DMAO(lane * 16, (const char*)&CTAB + i0 * 2048, 1024);
          dma16(lane * 16, (const char*)&CTAB + 6144 + i0 * 1024, L_SEL + i0 * 1024); }
        u4 red; UNDEF(red); GLD4(red, lane * 16, (const char*)&CTAB + 9216, "");
        W2_CH_IN(0);
        KG_POW(1);
        STAMPF(16); SBAR();
        W2_CH_IN(1);
        KG_COLS_A(c1, 1);
        asm volatile("s_waitcnt vmcnt(0)" : "+v"(red) :: "memory");
        STAMPF(17); SBAR();
        if (tid == 256) { const u2 ar = {tag, tag}; GST2(0, ar, ws + WS_AR + b * 8, "sc1"); }
        W2_CH_OUT(0); W2_CH_IN(2);
        KG_COLS_B();
        STAMPF(18); SBAR();
        W2_CH_OUT(1); W2_CH_IN(3);
        KG_ROWS_A();
        STAMPF(19); SBAR();
        WAITV(4); W2_CH_OUT(2);
        UNDEF(arf); GLD2(arf, (tid & 255) * 8, ws + WS_AR, "sc1");
        KG_ROWS_B();
        MM2(1, 0); MM2(1, 1); MM2(1, 2); MM2(1, 3);
        { const u32 sw = L_SCR + w4 * 4352 + lane * 16;
          LDS(f4, sw) = self4(ch, dk[0], dk[2]) * isc; LDS(f4, sw + 1024) = self4(ch, dk[1], dk[3]) * isc; }
        asm volatile("s_waitcnt vmcnt(0)" : "+v"(arf) :: "memory");
#pragma unroll 1
        for (int sp = 0; __any(arf.x != tag) && sp < SPIN_MAX; ++sp) RLD2(arf, (tid & 255) * 8, ws + WS_AR);
        STAMP(12);
        SB(); BARRIER();
        UNDEF(stv); UNDEF(xg0); UNDEF(xg1);
        asm volatile("global_load_dword %0, %1, %2 sc1" : "+v"(stv) : "v"((tid & 127) * 4), "s"(ws + WS_STAT + rp * 512) : "memory");
        GLD4(xg0, xo, ws + WS_XG + dp * 32, "sc1"); GLD4(xg1, xo, ws + WS_XG + dp * 32 + 128 * 7168, "sc1");
        {
            const u32 a = L_SCR + (w4 ^ 1) * 4352 + lane * 16;
            const f4 s0 = self4(ch, dk[2], dk[0]) * isc + LDS(f4, a), s1 = self4(ch, dk[3], dk[1]) * isc + LDS(f4, a + 1024);
            const u32 o = red.x + ch * 16 * red.y + rg * 4352, p01 = pkbf(s0.x, s0.y), p23 = pkbf(s0.z, s0.w);
            LDS(u16, o) = (u16)p01; LDS(u16, o + red.y) = (u16)(p01 >> 16); LDS(u16, o + 2 * red.y) = (u16)p23; LDS(u16, o + 3 * red.y) = (u16)(p23 >> 16);
            LDS(u2, (ch ? red.w : red.z) + rg * 4352) = u2{pkbf(s1.x, s1.y), pkbf(s1.z, s1.w)};
            if (lane == 0) LDS(u4, L_KC + rg * 4352 + ch * 16) = u4{0, 0, 0, 0};
        }
        const u4 tixa = LDS(u4, L_TAB + (tid & 127) * 16), tixb = LDS(u4, L_TAB + 2048 + (tid & 127) * 16), tixc = LDS(u4, L_TAB + 4096 + (tid & 127) * 16);
        SEL_READ();
        asm volatile("s_waitcnt vmcnt(2)" : "+v"(stv) :: "memory");
        if (w4 < 2) LDS(u32, L_ST + (tid & 127) * 4) = stv;
        __builtin_amdgcn_s_setprio(0);
        W2_CH_OUT(3);
        STAMP(4);
        BARRIER();
        W2_4(w2b - 3072 + 6 * 28672, pb1);
        {
            u32 kc = L_KC + rg * 4352, td = L_TAB + rg * 5120 + (tid & 127) * 4; PIN(td);
            u32 v[10];
#pragma unroll
            for (int i = 0; i < 10; ++i) { const u32 tx = i < 4 ? tixa[i & 3] : i < 8 ? tixb[i & 3] : tixc[i & 3]; v[i] = (u32)LDS(u16, kc + (tx & 0xffff)) | ((u32)LDS(u16, kc + (tx >> 16)) << 16); }
#pragma unroll
            for (int i = 0; i < 4; ++i) LDS(u32, td + 512 * i) = v[i];
            if ((tid & 127) < 96) LDS(u32, td + 2048) = v[4];
            LDS(u32, td + 2432) = v[5]; LDS(u32, td + 2944) = v[6]; LDS(u32, td + 3456) = v[7]; LDS(u32, td + 3968) = v[8]; LDS(u32, td + 4480) = v[9];
        }
        {
            asm volatile("s_waitcnt vmcnt(4)" : "+v"(xg0), "+v"(xg1) :: "memory");
            u32 a = L_U + 480 + (tid & 1) * 4608 + ((tid & 255) >> 1) * 16; PIN(a);
            LDS(u4, a) = xg0; LDS(u4, a + 2048) = xg1;
            if ((tid & 255) < 60) LDS(u4, a - 480) = u4{0, 0, 0, 0};
        }
        STAMP(5);
        BARRIER();
        STAMP(6);
    }
    u4 hA0, hA1, hA2, hA3, hA4, hA5, hA6, hB0, hB1, hB2, hB3, hB4, hB5, hB6, hC0, hC1, hC2, hC3, hC4, hC5, hC6;
    const char* hrd = ws + WS_H + (P * 112 + wave * 14) * 2048;
    u32 l16 = lane * 16; PIN(l16);
#define H_TAKE_hA(n, NW) do { const char* p_ = hrd + ((n) & 1) * 14336 + ((n) >> 1) * 1024; u32 t0_, t1_, t2_, sp_; asm volatile( \
        "s_waitcnt vmcnt(" #NW ")\n\ts_mov_b32 %10, 0x10000\n" "1:\n\t" \
        "v_bitop3_b32 %8, v196, v200, %16 bitop3:0x7e\n\tv_bitop3_b32 %9, v204, v208, %16 bitop3:0x7e\n\t" \
        "v_or3_b32 %7, v221, %8, %9\n\t" \
        "v_bitop3_b32 %8, v212, v216, %16 bitop3:0x7e\n\tv_xor_b32 %9, %16, v220\n\tv_or3_b32 %7, %7, %8, %9\n\t" \
        "v_or3_b32 %8, v197, v201, v205\n\tv_or3_b32 %9, v209, v213, v217\n\tv_or3_b32 %7, %7, %8, %9\n\t" \
        "v_bitop3_b32 %8, v198, v202, %17 bitop3:0x7e\n\tv_bitop3_b32 %9, v206, v210, %17 bitop3:0x7e\n\t" \
        "v_or3_b32 %7, %7, %8, %9\n\tv_or_b32 %7, %7, v223\n\t" \
        "v_bitop3_b32 %8, v214, v218, %17 bitop3:0x7e\n\tv_xor_b32 %9, %17, v222\n\tv_or3_b32 %7, %7, %8, %9\n\t" \
        "v_or3_b32 %8, v199, v203, v207\n\tv_or3_b32 %9, v211, v215, v219\n\tv_or3_b32 %7, %7, %8, %9\n\t" \
        "v_and_b32 %7, 0x80008000, %7\n\tv_cmp_ne_u32 vcc, 0, %7\n\ts_cbranch_vccz 2f\n\t" \
        "global_load_dwordx4 v[196:199], %11, %12 sc1\n\t" \
        "global_load_dwordx4 v[200:203], %11, %12 offset:2048 sc1\n\t" \
        "global_load_dwordx4 v[204:207], %11, %13 sc1\n\t" \
        "global_load_dwordx4 v[208:211], %11, %13 offset:2048 sc1\n\t" \
        "global_load_dwordx4 v[212:215], %11, %14 sc1\n\t" \
        "global_load_dwordx4 v[216:219], %11, %14 offset:2048 sc1\n\t" \
        "global_load_dwordx4 v[220:223], %11, %15 sc1\n\t" \
        "s_sub_u32 %10, %10, 1\n\ts_waitcnt vmcnt(0)\n\ts_cbranch_scc0 1b\n" "2:" \
        : "={v[196:199]}"(hA0), "={v[200:203]}"(hA1), "={v[204:207]}"(hA2), "={v[208:211]}"(hA3), "={v[212:215]}"(hA4), "={v[216:219]}"(hA5), "={v[220:223]}"(hA6), "=&v"(t0_), "=&v"(t1_), "=&v"(t2_), "=&s"(sp_) \
        : "v"(l16), "s"(p_), "s"(p_ + 4096), "s"(p_ + 8192), "s"(p_ + 12288), "s"(hm0), "s"(hm2) : "vcc", "scc", "memory"); } while (0)
#define H_TAKE_hB(n, NW) do { const char* p_ = hrd + ((n) & 1) * 14336 + ((n) >> 1) * 1024; u32 t0_, t1_, t2_, sp_; asm volatile( \
        "s_waitcnt vmcnt(" #NW ")\n\ts_mov_b32 %10, 0x10000\n" "1:\n\t" \
        "v_bitop3_b32 %8, v224, v228, %16 bitop3:0x7e\n\tv_bitop3_b32 %9, v232, v236, %16 bitop3:0x7e\n\t" \
        "v_or3_b32 %7, v249, %8, %9\n\t" \
        "v_bitop3_b32 %8, v240, v244, %16 bitop3:0x7e\n\tv_xor_b32 %9, %16, v248\n\tv_or3_b32 %7, %7, %8, %9\n\t" \
        "v_or3_b32 %8, v225, v229, v233\n\tv_or3_b32 %9, v237, v241, v245\n\tv_or3_b32 %7, %7, %8, %9\n\t" \
        "v_bitop3_b32 %8, v226, v230, %17 bitop3:0x7e\n\tv_bitop3_b32 %9, v234, v238, %17 bitop3:0x7e\n\t" \
        "v_or3_b32 %7, %7, %8, %9\n\tv_or_b32 %7, %7, v251\n\t" \
        "v_bitop3_b32 %8, v242, v246, %17 bitop3:0x7e\n\tv_xor_b32 %9, %17, v250\n\tv_or3_b32 %7, %7, %8, %9\n\t" \
        "v_or3_b32 %8, v227, v231, v235\n\tv_or3_b32 %9, v239, v243, v247\n\tv_or3_b32 %7, %7, %8, %9\n\t" \
        "v_and_b32 %7, 0x80008000, %7\n\tv_cmp_ne_u32 vcc, 0, %7\n\ts_cbranch_vccz 2f\n\t" \
        "global_load_dwordx4 v[224:227], %11, %12 sc1\n\t" \
        "global_load_dwordx4 v[228:231], %11, %12 offset:2048 sc1\n\t" \
        "global_load_dwordx4 v[232:235], %11, %13 sc1\n\t" \
        "global_load_dwordx4 v[236:239], %11, %13 offset:2048 sc1\n\t" \
        "global_load_dwordx4 v[240:243], %11, %14 sc1\n\t" \
        "global_load_dwordx4 v[244:247], %11, %14 offset:2048 sc1\n\t" \
        "global_load_dwordx4 v[248:251], %11, %15 sc1\n\t" \
        "s_sub_u32 %10, %10, 1\n\ts_waitcnt vmcnt(0)\n\ts_cbranch_scc0 1b\n" "2:" \
        : "={v[224:227]}"(hB0), "={v[228:231]}"(hB1), "={v[232:235]}"(hB2), "={v[236:239]}"(hB3), "={v[240:243]}"(hB4), "={v[244:247]}"(hB5), "={v[248:251]}"(hB6), "=&v"(t0_), "=&v"(t1_), "=&v"(t2_), "=&s"(sp_) \
        : "v"(l16), "s"(p_), "s"(p_ + 4096), "s"(p_ + 8192), "s"(p_ + 12288), "s"(hm0), "s"(hm2) : "vcc", "scc", "memory"); } while (0)
#define H_TAKE_hC(n, NW) do { const char* p_ = hrd + ((n) & 1) * 14336 + ((n) >> 1) * 1024; u32 t0_, t1_, t2_, sp_; asm volatile( \
        "s_waitcnt vmcnt(" #NW ")\n\ts_mov_b32 %10, 0x10000\n" "1:\n\t" \
        "v_bitop3_b32 %8, v168, v172, %16 bitop3:0x7e\n\tv_bitop3_b32 %9, v176, v180, %16 bitop3:0x7e\n\t" \
        "v_or3_b32 %7, v193, %8, %9\n\t" \
        "v_bitop3_b32 %8, v184, v188, %16 bitop3:0x7e\n\tv_xor_b32 %9, %16, v192\n\tv_or3_b32 %7, %7, %8, %9\n\t" \
        "v_or3_b32 %8, v169, v173, v177\n\tv_or3_b32 %9, v181, v185, v189\n\tv_or3_b32 %7, %7, %8, %9\n\t" \
        "v_bitop3_b32 %8, v170, v174, %17 bitop3:0x7e\n\tv_bitop3_b32 %9, v178, v182, %17 bitop3:0x7e\n\t" \
        "v_or3_b32 %7, %7, %8, %9\n\tv_or_b32 %7, %7, v195\n\t" \
        "v_bitop3_b32 %8, v186, v190, %17 bitop3:0x7e\n\tv_xor_b32 %9, %17, v194\n\tv_or3_b32 %7, %7, %8, %9\n\t" \
        "v_or3_b32 %8, v171, v175, v179\n\tv_or3_b32 %9, v183, v187, v191\n\tv_or3_b32 %7, %7, %8, %9\n\t" \
        "v_and_b32 %7, 0x80008000, %7\n\tv_cmp_ne_u32 vcc, 0, %7\n\ts_cbranch_vccz 2f\n\t" \
        "global_load_dwordx4 v[168:171], %11, %12 sc1\n\t" \
        "global_load_dwordx4 v[172:175], %11, %12 offset:2048 sc1\n\t" \
        "global_load_dwordx4 v[176:179], %11, %13 sc1\n\t" \
        "global_load_dwordx4 v[180:183], %11, %13 offset:2048 sc1\n\t" \
        "global_load_dwordx4 v[184:187], %11, %14 sc1\n\t" \
        "global_load_dwordx4 v[188:191], %11, %14 offset:2048 sc1\n\t" \
        "global_load_dwordx4 v[192:195], %11, %15 sc1\n\t" \
        "s_sub_u32 %10, %10, 1\n\ts_waitcnt vmcnt(0)\n\ts_cbranch_scc0 1b\n" "2:" \
        : "={v[168:171]}"(hC0), "={v[172:175]}"(hC1), "={v[176:179]}"(hC2), "={v[180:183]}"(hC3), "={v[184:187]}"(hC4), "={v[188:191]}"(hC5), "={v[192:195]}"(hC6), "=&v"(t0_), "=&v"(t1_), "=&v"(t2_), "=&s"(sp_) \
        : "v"(l16), "s"(p_), "s"(p_ + 4096), "s"(p_ + 8192), "s"(p_ + 12288), "s"(hm0), "s"(hm2) : "vcc", "scc", "memory"); } while (0)
    {
        const int kh = hi4, ts = ch;
        dma16(r8 * 896 + 16 * (p8 ^ (wave & 6)), (const char*)x + (P * 64 + wave * 8) * 896 + ct * 128, L_XE + wave * 1024);
        STAMP(13);
        if (isF) { if (ch) conv_far<1, 2>(cacc, cv_ub, cv_la, cv_p3, sel3); else conv_far<0, 2>(cacc, cv_ub, cv_la, cv_p3, sel3); }
        else { if (ch) conv_far<1, 0>(cacc, cv_ub, cv_la, cv_p3, sel3); else conv_far<0, 0>(cacc, cv_ub, cv_la, cv_p3, sel3); }
        STAMP(14);
        {
            u32 u = cv_ub - 192 - 256 * hi4, l = cv_la + 416 - 256 * hi4, q = cv_q2 + 256 * hi4; PIN(u); PIN(l); PIN(q);
            if (ch) conv_steps<1>(cacc, isF, u, l, q, cv_la, sel2, vq0, vq1, vq2, vq3, w2b + 16 * 7168 - 3072, pb0, l16, hrd);
            else conv_steps<0>(cacc, isF, u, l, q, cv_la, sel2, vq0, vq1, vq2, vq3, w2b + 16 * 7168 - 3072, pb0, l16, hrd);
        }
        STAMP(7);
        WAITV(15);
        BARRIER();
        u32 ra = lw + (isF ? 0 : 65536) + 16384 + lane * 16; PIN(ra);
        LDS(f4, ra) = self4(kh, cacc[0], cacc[2]);
        LDS(f4, ra + 1024) = self4(kh, cacc[1], cacc[3]);
        H_LDB(0, hrd + 14336); H_LDB(1, hrd + 14336); H_LDB(2, hrd + 14336); H_LDB(3, hrd + 14336); H_LDB(4, hrd + 14336); H_LDB(5, hrd + 14336); H_LDB(6, hrd + 14336);
        ra = pb1 + 16384 + lane * 16; PIN(ra);
        BARRIER();
        const float dk = rg ? bfhi(dsk2) : bflo(dsk2);
#pragma unroll
        for (int i = 0; i < 2; ++i) {
            const int tau = kh ? (ts ? 5 + i : 4 + 3 * i) : (ts ? 1 + i : 3 * i);
            const f4 y = self4(kh, cacc[2 + i], cacc[i]) + LDS(f4, ra + i * 1024);
            const u2 uu = LDS(u2, L_U + rg * 4608 + 480 + (256 * tau + 16 * fl + 4 * g) * 2);
            const u32 y01 = pkbf(y.x, y.y), y23 = pkbf(y.z, y.w);
            const u32 s01 = pkbf(bflo(uu.x) * dk, bfhi(uu.x) * dk), s23 = pkbf(bflo(uu.y) * dk, bfhi(uu.y) * dk);
            const u4 o = {pkbf(bflo(y01) + bflo(s01), bfhi(y01) + bfhi(s01)), tag, pkbf(bflo(y23) + bflo(s23), bfhi(y23) + bfhi(s23)), tag};
            GST4((fl >> 2) * 114688 + ((fl & 3) + 4 * g) * 16, o, ws + WS_YG + (4 * tau * 448 + 2 * dp + rg) * 256, "sc1");
        }
    }
    STAMP(8);
    u32 pw0 = pb0 + lane * 16, pw1 = (pb1 + lane * 16) ^ 128; PIN(pw0); PIN(pw1);
#define H_MM1(h, j, o) do { a0 = mfma32(h##j, Bw[(o) + j][0], a0); a1 = mfma32(h##j, Bw[(o) + j][1], a1); } while (0)
#define H_MMA(h, o) do { H_MM1(h, 0, o); H_MM1(h, 1, o); H_MM1(h, 2, o); H_MM1(h, 3, o); H_MM1(h, 4, o); H_MM1(h, 5, o); H_MM1(h, 6, o); } while (0)
#define W2_SGN(j) do { Bw[j][0] ^= u4{hm0, 0, hm2, 0}; Bw[j][1] ^= u4{hm0, 0, hm2, 0}; PIN(Bw[j][0]); PIN(Bw[j][1]); } while (0)
#define H_MMS(h, o) do { H_MM1(h, 0, o); W2_SGN((o) + 1); SB(); H_MM1(h, 1, o); W2_SGN((o) + 2); SB(); H_MM1(h, 2, o); W2_SGN((o) + 3); SB(); H_MM1(h, 3, o); W2_SGN((o) + 4); SB(); \
        H_MM1(h, 4, o); W2_SGN((o) + 5); SB(); H_MM1(h, 5, o); W2_SGN((o) + 6); SB(); H_MM1(h, 6, o); } while (0)
#define H_ZERO() _Pragma("unroll") for (int i = 0; i < 16; ++i) { a0[i] = 0; a1[i] = 0; }
#define H_PUT(pw) _Pragma("unroll") for (int q = 0; q < 4; ++q) { const u32 a_ = ((pw) ^ (q >> 1) * 64) + q * 1024; LDS(f4, a_) = quad(a0, q); LDS(f4, a_ + 16384) = quad(a1, q); }
    f16v a0, a1;
    H_ZERO();
    __builtin_amdgcn_s_setprio(3);
    W2_SGN(0); W2_SGN(7);
    H_TAKE_hA(0, 9);
    {
        const char* p3 = hrd + 1024;
        H_MM1(hA, 0, 0); W2_SGN(1); W2_RD(8, 0, 0); SB(); H_LDC(0, p3); H_MM1(hA, 1, 0); W2_SGN(2); W2_RD(9, 0, 1); SB(); H_LDC(1, p3);
        H_MM1(hA, 2, 0); W2_SGN(3); W2_RD(10, 0, 2); SB(); H_LDC(2, p3); H_MM1(hA, 3, 0); W2_SGN(4); W2_RD(11, 0, 3); SB(); H_LDC(3, p3);
        H_MM1(hA, 4, 0); W2_SGN(5); W2_RD(12, 1, 0); SB(); H_LDC(4, p3); H_MM1(hA, 5, 0); W2_SGN(6); W2_RD(13, 1, 1); SB(); H_LDC(5, p3);
        H_MM1(hA, 6, 0); SB(); H_LDC(6, p3);
    }
    STAMP(9);
    __builtin_amdgcn_s_setprio(2);
    H_TAKE_hB(1, 9);
    {
        const char* p3 = hrd + 14336 + 1024;
        H_MM1(hB, 0, 7); W2_SGN(8); SB(); H_LDA(0, p3); H_MM1(hB, 1, 7); W2_SGN(9); SB(); H_LDA(1, p3); H_MM1(hB, 2, 7); W2_SGN(10); SB(); H_LDA(2, p3);
        H_MM1(hB, 3, 7); W2_SGN(11); SB(); H_LDA(3, p3); H_MM1(hB, 4, 7); W2_SGN(12); SB(); H_LDA(4, p3); H_MM1(hB, 5, 7); W2_SGN(13); SB(); H_LDA(5, p3);
        H_MM1(hB, 6, 7); SB(); H_LDA(6, p3);
    }
    H_PUT(pw0);
    H_ZERO();
    __builtin_amdgcn_s_setprio(1);
    H_TAKE_hC(2, 7); H_MMA(hC, 0);
    const u32 al = lane & 3, dc = 16 * rg + (lane >> 2);
    const u32 yo = (32 * hi4 + dc) * 256 + 16 * al + 128 * ch;
    const char* yb = ws + WS_YG + (P * 448 + 64 * ct) * 256;
    u4 yv0, yv1;
    asm volatile("global_load_dwordx4 v[168:171], %0, %1 sc1\n\tglobal_load_dwordx4 v[172:175], %0, %1 offset:64 sc1" :: "v"(yo), "s"(yb)
                 : "v168", "v169", "v170", "v171", "v172", "v173", "v174", "v175", "memory");
    u32 ax = L_XE + (16 * al + 8 * ch) * 128 + ((64 * hi4 + 2 * dc) ^ (32 * al)); PIN(ax);
    const u32 o_ = L_RING + hi4 * 16384 + (2 * (al & 1) + ch) * 1024 + (dc ^ (4 * al)) * 16;
    u32 aF = o_ + (al & 2 ? 65536 : 32768), aS = o_ + (al & 2 ? 0 : 98304); PIN(aF); PIN(aS);
    u32 xe[8];
#pragma unroll
    for (int i = 0; i < 8; ++i) xe[i] = LDS(u16, ax + i * 128);
    __builtin_amdgcn_s_setprio(0);
    H_TAKE_hA(3, 2);
    a0 = mfma32(hA0, Bw[7][0], a0); a0 = mfma32(hA1, Bw[8][0], a0); a0 = mfma32(hA2, Bw[9][0], a0); a0 = mfma32(hA3, Bw[10][0], a0);
    a0 = mfma32(hA4, Bw[11][0], a0); a0 = mfma32(hA5, Bw[12][0], a0); a0 = mfma32(hA6, Bw[13][0], a0);
    SB();
    a1 = mfma32(hA0, Bw[7][1], a1); SB();
    {
        u32 t0_, sp_;
        asm volatile("s_waitcnt vmcnt(0)\n\ts_mov_b32 %3, 0x10000\n" "1:\n\t"
                     "v_bitop3_b32 %2, v169, v171, %6 bitop3:0x7e\n\tv_bitop3_b32 %2, %2, v173, %6 bitop3:0xf6\n\tv_bitop3_b32 %2, %2, v175, %6 bitop3:0xf6\n\t"
                     "v_cmp_ne_u32 vcc, 0, %2\n\ts_cbranch_vccz 2f\n\t"
                     "global_load_dwordx4 v[168:171], %4, %5 sc1\n\tglobal_load_dwordx4 v[172:175], %4, %5 offset:64 sc1\n\t"
                     "s_sub_u32 %3, %3, 1\n\ts_waitcnt vmcnt(0)\n\ts_cbranch_scc0 1b\n" "2:"
                     : "={v[168:171]}"(yv0), "={v[172:175]}"(yv1), "=&v"(t0_), "=&s"(sp_) : "v"(yo), "s"(yb), "s"(tag) : "vcc", "scc", "memory");
    }
    u32 pre[4];
    a1 = mfma32(hA1, Bw[8][1], a1); pre[0] = pkbf(bflo(xe[0]) + bflo(yv0.x), bflo(xe[1]) + bfhi(yv0.x)); PIN(pre[0]); LDS(f4, pw1) = quad(a0, 0); SB();
    a1 = mfma32(hA2, Bw[9][1], a1); pre[1] = pkbf(bflo(xe[2]) + bflo(yv0.z), bflo(xe[3]) + bfhi(yv0.z)); PIN(pre[1]); LDS(f4, pw1 + 1024) = quad(a0, 1); SB();
    a1 = mfma32(hA3, Bw[10][1], a1); pre[2] = pkbf(bflo(xe[4]) + bflo(yv1.x), bflo(xe[5]) + bfhi(yv1.x)); PIN(pre[2]); LDS(f4, (pw1 ^ 64) + 2048) = quad(a0, 2); SB();
    a1 = mfma32(hA4, Bw[11][1], a1); pre[3] = pkbf(bflo(xe[6]) + bflo(yv1.z), bflo(xe[7]) + bfhi(yv1.z)); PIN(pre[3]); LDS(f4, (pw1 ^ 64) + 3072) = quad(a0, 3); SB();
    a1 = mfma32(hA5, Bw[12][1], a1); a1 = mfma32(hA6, Bw[13][1], a1);
#pragma unroll
    for (int q = 0; q < 4; ++q) LDS(f4, (pw1 ^ (q >> 1) * 64) + q * 1024 + 16384) = quad(a1, q);
    BARRIER();
    STAMP(10);
#pragma unroll
    for (int i = 0; i < 2; ++i) {
        const f4 r = ((LDS(f4, aF + i * 512) + LDS(f4, aF + i * 512 + 4096)) + (LDS(f4, aF + i * 512 + 8192) + LDS(f4, aF + i * 512 + 12288))) +
                     ((LDS(f4, aS + i * 512) + LDS(f4, aS + i * 512 + 4096)) + (LDS(f4, aS + i * 512 + 8192) + LDS(f4, aS + i * 512 + 12288)));
        const u32 a01 = pre[2 * i], a23 = pre[2 * i + 1], f01 = pkbf(r.x, r.y), f23 = pkbf(r.z, r.w);
        const u32 o01 = pkbf(bflo(a01) + bflo(f01), bfhi(a01) + bfhi(f01)), o23 = pkbf(bflo(a23) + bflo(f23), bfhi(a23) + bfhi(f23));
        LDS(u16, ax + i * 512) = (u16)o01; LDS(u16, ax + i * 512 + 128) = (u16)(o01 >> 16); LDS(u16, ax + i * 512 + 256) = (u16)o23; LDS(u16, ax + i * 512 + 384) = (u16)(o23 >> 16);
    }
    BARRIER();
    { const u4 ov = LDS(u4, L_XE + tid * 16); GST4(r8 * 896 + 16 * (p8 ^ (wave & 6)), ov, (char*)out + (P * 64 + wave * 8) * 896 + ct * 128, "sc1"); }
    STAMP(11); STAMPC(23);
    STAMP_FLUSH(24);
}

extern "C" void kernel_launch(void* const* d_in, const int* in_sizes, int n_in, void* d_out, int out_size, void* d_ws, size_t ws_size,
                              hipStream_t stream) {
    (void)hipFuncSetAttribute((const void*)s4ffn_kernel, hipFuncAttributeMaxDynamicSharedMemorySize, LDS_BYTES);
    s4ffn_kernel<<<256, 512, LDS_BYTES, stream>>>((const u16*)d_in[0], (const u16*)d_in[1], (const u16*)d_in[2], (const u16*)d_in[3],
        (const u16*)d_in[4], (const u16*)d_in[5], (const u16*)d_in[6], (const u16*)d_in[7], (const u16*)d_in[8],
        (const u16*)d_in[10], (const u16*)d_in[11], (u16*)d_out, (char*)d_ws);
}
```
